```python
import torch

def _softmax(x, axis=-1):
    return torch.softmax(x, dim=axis)
def _gelu(x):
    return torch.nn.functional.gelu(x, approximate="tanh")
def _silu(x):
    return torch.nn.functional.silu(x)
def _sigmoid(x):
    return torch.sigmoid(x)
def _layernorm(x, axis=-1, eps=1e-5):
    m = x.mean(dim=axis, keepdim=True)
    v = x.var(dim=axis, keepdim=True, unbiased=False)
    return (x - m) / torch.sqrt(v + eps)
def _scatter_add(tgt, idx, src, axis=0):
    return tgt.clone().index_add_(axis, idx, src)
def _segment_sum(src, seg, n):
    out = torch.zeros((n,) + src.shape[1:], dtype=src.dtype)
    return out.index_add_(0, seg, src)
def _pad(x, pw):
    flat = tuple(v for pair in reversed(pw) for v in pair)
    return torch.nn.functional.pad(x, flat)

kernel_name = 'parallel_sum__ssm_conv_s4__geglu_ffn__prompt'

def setup_inputs():

    _w = (lambda a: (lambda t: t.to(torch.bfloat16) if t.is_floating_point() else t)(torch.as_tensor(a)))
    def _A_setup():
        B, L, D, N = 1, 2048, 512, 128
        A_re = -(lambda _x: torch.exp(torch.as_tensor(_x)))((lambda *s: torch.randn(*s, dtype=torch.bfloat16) * 3.2)(D, N))
        A_im = torch.pi * (lambda *s: torch.randn(*s, dtype=torch.bfloat16) * 0.2)(D, N)
        B_re = (lambda *s: torch.randn(*s, dtype=torch.bfloat16) * 0.4)(D, N)
        B_im = (lambda *s: torch.randn(*s, dtype=torch.bfloat16) * 0.4)(D, N)
        C_re = (lambda *s: torch.randn(*s, dtype=torch.bfloat16) * 0.4)(D, N)
        C_im = (lambda *s: torch.randn(*s, dtype=torch.bfloat16) * 0.4)(D, N)
        log_dt = (lambda *s: torch.randn(*s, dtype=torch.bfloat16) * 0.2)(D) * 0.5 - 4.0
        Dskip = (lambda *s: torch.randn(*s, dtype=torch.bfloat16) * 0.4)(D)
        return {
            "u": _w((lambda *s: torch.randn(*s, dtype=torch.bfloat16) * 0.05)(B, L, D)),
            "A_re": _w(A_re),
            "A_im": _w(A_im),
            "B_re": _w(B_re),
            "B_im": _w(B_im),
            "C_re": _w(C_re),
            "C_im": _w(C_im),
            "log_dt": _w(log_dt),
            "Dskip": _w(Dskip),
        }
    _A_in = _A_setup()

    def _B_setup():
        B, S, D, F = 1, 2048, 512, 1792
        return {
            "x": _w((lambda *s: torch.randn(*s, dtype=torch.bfloat16) * 0.05)(B, S, D)),
            "w_gate": _w((lambda *s: torch.randn(*s, dtype=torch.bfloat16) * 0.05)(D, F)),
            "w_up": _w((lambda *s: torch.randn(*s, dtype=torch.bfloat16) * 0.05)(D, F)),
            "w_down": _w((lambda *s: torch.randn(*s, dtype=torch.bfloat16) * 0.05)(F, D)),
        }
    _B_in = _B_setup()

    return {
        "A_u": _A_in["u"],
        "A_A_re": _A_in["A_re"],
        "A_A_im": _A_in["A_im"],
        "A_B_re": _A_in["B_re"],
        "A_B_im": _A_in["B_im"],
        "A_C_re": _A_in["C_re"],
        "A_C_im": _A_in["C_im"],
        "A_log_dt": _A_in["log_dt"],
        "A_Dskip": _A_in["Dskip"],
        "B_x": _B_in["x"],
        "B_w_gate": _B_in["w_gate"],
        "B_w_up": _B_in["w_up"],
        "B_w_down": _B_in["w_down"],
    }

def reference(A_u, A_A_re, A_A_im, A_B_re, A_B_im, A_C_re, A_C_im, A_log_dt, A_Dskip, B_x, B_w_gate, B_w_up, B_w_down):
    def _A_ref(u, A_re, A_im, B_re, B_im, C_re, C_im, log_dt, Dskip):
        Bsz, L, D = u.shape
        dt = torch.exp(log_dt)[:, None]
        A = A_re + 1j * A_im
        Bc = B_re + 1j * B_im
        Cc = C_re + 1j * C_im
        logdA = dt * A
        dB = dt * Bc
        l = (lambda x, dt: x.to(dt))(torch.arange(L), A_re.dtype)
        P = torch.exp(logdA[:, :, None] * l[None, None, :])
        K = torch.einsum("dn,dnl,dn->dl", Cc, P, dB).real
        L2 = 2 * L
        Kp = _pad(K, ((0, 0), (0, L)))
        up = (lambda x, *d: x.permute(*d))(u, 0, 2, 1)
        up = _pad(up, ((0, 0), (0, 0), (0, L)))
        Kf = (lambda x, axes=(-2,-1): torch.fft.rfft2(x.float(), dim=axes))(Kp[:, None, :], axes=(-2, -1))
        Uf = (lambda x, axes=(-2,-1): torch.fft.rfft2(x.float(), dim=axes))(up[:, :, None, :], axes=(-2, -1))
        Yf = Uf * Kf[None, :, :, :]
        y = (lambda x, s, axes=(-2,-1): torch.fft.irfft2(x, s=s, dim=axes).to(torch.bfloat16))(Yf, (1, L2), axes=(-2, -1))[:, :, 0, :L]
        y = (lambda x, *d: x.permute(*d))(y, 0, 2, 1) + u * Dskip[None, None, :]
        return y

    def _B_ref(x, w_gate, w_up, w_down):
        return (_gelu(x @ w_gate) * (x @ w_up)) @ w_down

    x = A_u
    xn = _layernorm(x, axis=-1)
    return x + _A_ref(xn, A_A_re, A_A_im, A_B_re, A_B_im, A_C_re, A_C_im, A_log_dt, A_Dskip) + _B_ref(xn, B_w_gate, B_w_up, B_w_down)
```

```cpp
#include <hip/hip_runtime.h>
#include <hip/hip_bf16.h>
#include <stdint.h>
typedef unsigned short u16;
__device__ __forceinline__ float bf2f(u16 v){ return __uint_as_float(((unsigned)v)<<16); }
__device__ __forceinline__ u16 f2bf(float f){ unsigned u=__float_as_uint(f); u += 0x7fffu + ((u>>16)&1u); return (u16)(u>>16); }
__device__ __forceinline__ float rbf(float f){ return bf2f(f2bf(f)); }
constexpr int L=2048, D=512, N=128, F=1792;
#define LROUND 1

__global__ void ln_k(const u16* x, u16* xn){
  int t=blockIdx.x; int tid=threadIdx.x;
  float v[8]; float s=0;
  for(int j=0;j<8;++j){ v[j]=bf2f(x[t*D+tid*8+j]); s+=v[j]; }
  for(int o=32;o>0;o>>=1) s+=__shfl_xor(s,o);
  float mean=s/D; float q=0;
  for(int j=0;j<8;++j){ float d=v[j]-mean; q+=d*d; }
  for(int o=32;o>0;o>>=1) q+=__shfl_xor(q,o);
  float m=rbf(mean); float var=rbf(q/D); float sd=rbf(sqrtf(rbf(var+1e-5f)));
  for(int j=0;j<8;++j){ xn[t*D+tid*8+j]=f2bf(rbf(v[j]-m)/sd); }
}
__global__ void kgen_k(const u16* Are,const u16* Aim,const u16* Bre,const u16* Bim,const u16* Cre,const u16* Cim,const u16* logdt,float* K){
  int d=blockIdx.x; __shared__ float ar[N],ai[N],cr[N],ci[N];
  float dt=rbf(expf(bf2f(logdt[d])));
  if(threadIdx.x<N){ int n=threadIdx.x; int i=d*N+n;
    ar[n]=dt*bf2f(Are[i]); ai[n]=dt*bf2f(Aim[i]);
    float br=dt*bf2f(Bre[i]), bi=dt*bf2f(Bim[i]); float c0=bf2f(Cre[i]), c1=bf2f(Cim[i]);
    cr[n]=c0*br-c1*bi; ci[n]=c0*bi+c1*br; }
  __syncthreads();
  for(int l=threadIdx.x;l<L;l+=blockDim.x){
    float lf = LROUND ? rbf((float)l) : (float)l; float s=0;
    for(int n=0;n<N;++n){ float e=expf(ar[n]*lf); float sn,cs; sincosf(ai[n]*lf,&sn,&cs); s+=e*(cr[n]*cs-ci[n]*sn); }
    K[d*L+l]=s; }
}
__global__ void conv_k(const u16* xn,const float* K,float* y){
  int d=blockIdx.x; __shared__ float ks[L], us[L];
  for(int l=threadIdx.x;l<L;l+=blockDim.x){ ks[l]=K[d*L+l]; us[l]=bf2f(xn[l*D+d]); }
  __syncthreads();
  for(int t=threadIdx.x;t<L;t+=blockDim.x){ float s=0; for(int j=0;j<=t;++j) s+=ks[t-j]*us[j]; y[t*D+d]=s; }
}
__global__ void g1_k(const u16* xn,const u16* wg,const u16* wu,u16* h){
  int t=blockIdx.x; __shared__ float xs[D];
  for(int k=threadIdx.x;k<D;k+=blockDim.x) xs[k]=bf2f(xn[t*D+k]);
  __syncthreads();
  for(int f=threadIdx.x;f<F;f+=blockDim.x){ float g=0,u=0;
    for(int k=0;k<D;++k){ g+=xs[k]*bf2f(wg[k*F+f]); u+=xs[k]*bf2f(wu[k*F+f]); }
    g=rbf(g); u=rbf(u);
    float z=0.7978845608028654f*(g+0.044715f*g*g*g);
    float ge=rbf(0.5f*g*(1.f+tanhf(z)));
    h[t*F+f]=f2bf(ge*u); }
}
__global__ void g2_k(const u16* h,const u16* wd,const u16* x,const u16* xn,const float* y,const u16* dsk,u16* out){
  int t=blockIdx.x; __shared__ float hs[F];
  for(int k=threadIdx.x;k<F;k+=blockDim.x) hs[k]=bf2f(h[t*F+k]);
  __syncthreads();
  for(int d=threadIdx.x;d<D;d+=blockDim.x){ float a=0;
    for(int k=0;k<F;++k) a+=hs[k]*bf2f(wd[k*D+d]);
    a=rbf(a);
    float ya=rbf(rbf(y[t*D+d])+rbf(bf2f(xn[t*D+d])*bf2f(dsk[d])));
    out[t*D+d]=f2bf(rbf(bf2f(x[t*D+d])+ya)+a); }
}
extern "C" void kernel_launch(void* const* d_in, const int* in_sizes, int n_in, void* d_out, int out_size, void* d_ws, size_t ws_size, hipStream_t stream){
  const u16* x=(const u16*)d_in[0];
  char* ws=(char*)d_ws;
  u16* xn=(u16*)ws; float* K=(float*)(ws+(4<<20)); float* y=(float*)(ws+(12<<20)); u16* h=(u16*)(ws+(20<<20));
  ln_k<<<L,64,0,stream>>>(x,xn);
  kgen_k<<<D,256,0,stream>>>((const u16*)d_in[1],(const u16*)d_in[2],(const u16*)d_in[3],(const u16*)d_in[4],(const u16*)d_in[5],(const u16*)d_in[6],(const u16*)d_in[7],K);
  conv_k<<<D,256,0,stream>>>(xn,K,y);
  g1_k<<<L,256,0,stream>>>(xn,(const u16*)d_in[10],(const u16*)d_in[11],h);
  g2_k<<<L,256,0,stream>>>(h,(const u16*)d_in[12],x,xn,y,(const u16*)d_in[8],(u16*)d_out);
}
```

```cpp
#include <hip/hip_runtime.h>
#include <stdint.h>

typedef unsigned short u16;
typedef unsigned u32;
typedef unsigned long long u64;
typedef __bf16 bf16x2 __attribute__((ext_vector_type(2)));
typedef __bf16 bf16x4 __attribute__((ext_vector_type(4)));
typedef __bf16 bf16x8 __attribute__((ext_vector_type(8)));
typedef float f32x2 __attribute__((ext_vector_type(2)));
typedef float f32x4 __attribute__((ext_vector_type(4)));
typedef float f32x16 __attribute__((ext_vector_type(16)));
typedef u32 u32x2 __attribute__((ext_vector_type(2)));
typedef u32 u32x4 __attribute__((ext_vector_type(4)));
#define LAS __attribute__((address_space(3)))
#define GAS __attribute__((address_space(1)))
#define DI __device__ __forceinline__
typedef __amdgpu_buffer_rsrc_t rsrc_t;

constexpr int L = 2048, D = 512, NM = 128, F = 1792;
constexpr u32 WS_H = 0, WS_XNT = 8u << 20, WS_ZT = 10u << 20, WS_ST = 12u << 20, WS_FL = 13u << 20, WS_END = 14u << 20;
constexpr u32 FL_E = WS_FL, FL_1 = WS_FL + 4096, FL_2 = WS_FL + 8192, FL_3 = WS_FL + 32768;
constexpr u32 SLOT = 49152, TAIL = 3 * SLOT;
constexpr u32 KCP = 4160;
constexpr u32 K0S = 4352, KSS = 3 * KCP + 64;
constexpr u32 L_K0 = TAIL;
constexpr u32 L_U0 = TAIL + 2 * K0S;
constexpr u32 L_CS = L_U0 + 4608;
constexpr u32 L_CNT = L_CS + 2048;
constexpr u32 L_KS = SLOT + 16384 + 64;
constexpr u32 L_U1 = L_KS + 2 * KSS;
constexpr u32 L_SCR = 2 * SLOT;
constexpr u32 SPIN_CAP = 1u << 18;

struct Params {
  const u16 *x, *are, *aim, *bre, *bim, *cre, *cim, *logdt, *dskip, *wg, *wu, *wd;
  u16* out;
  char* ws;
};

#define STAMP_AT(k)
#define STAMP_RT(k)

DI float bflo(u32 v) { return __builtin_bit_cast(float, v << 16); }
DI float bfhi(u32 v) { return __builtin_bit_cast(float, v & 0xffff0000u); }
DI u32 pk(float a, float b) { f32x2 f = {a, b}; return __builtin_bit_cast(u32, __builtin_convertvector(f, bf16x2)); }
DI float rbf(float a) { return bflo(pk(a, a)); }
DI void dma16(rsrc_t r, u32 lds, u32 voff, u32 soff) {
  asm volatile("s_mov_b32 m0, %0\n\ts_nop 0\n\tbuffer_load_dwordx4 %1, %2, %3 offen lds" :: "s"(lds), "v"(voff), "s"(r), "s"(soff));
}
#define WAIT_V(n) asm volatile("s_waitcnt vmcnt(" #n ")" ::: "memory")
#define BARRIER() asm volatile("s_waitcnt lgkmcnt(0)\n\ts_barrier" ::: "memory")
#define SCHED() __builtin_amdgcn_sched_barrier(0)
DI bf16x8 ldfrag(LAS char* a) { return *(LAS bf16x8*)a; }
DI bf16x8 trfrag(LAS char* a0, LAS char* a1) {
  bf16x4 lo = __builtin_amdgcn_ds_read_tr16_b64_v4bf16((LAS bf16x4*)a0);
  bf16x4 hi = __builtin_amdgcn_ds_read_tr16_b64_v4bf16((LAS bf16x4*)a1);
  return __builtin_shufflevector(lo, hi, 0, 1, 2, 3, 4, 5, 6, 7);
}
#define MFMA32(a, b, c) __builtin_amdgcn_mfma_f32_32x32x16_bf16(a, b, c, 0, 0, 0)
#define MFMA16(a, b, c) __builtin_amdgcn_mfma_f32_16x16x32_bf16(a, b, c, 0, 0, 0)
DI u32x2 ld8_sc1(rsrc_t r, u32 voff, u32 soff) { return __builtin_amdgcn_raw_buffer_load_b64(r, voff, soff, 16); }
DI u32x4 ld16_sc1(rsrc_t r, u32 voff, u32 soff) { return __builtin_amdgcn_raw_buffer_load_b128(r, voff, soff, 16); }
DI void st8_sc1(rsrc_t r, u32x2 v, u32 voff, u32 soff) { __builtin_amdgcn_raw_buffer_store_b64(v, r, voff, soff, 16); }
DI void st16_sc1(rsrc_t r, u32x4 v, u32 voff, u32 soff) { __builtin_amdgcn_raw_buffer_store_b128(v, r, voff + soff, 0, 16); }
DI void wait_flags(rsrc_t r, u32 voff, u32 tlo, u32 thi) {
#pragma unroll 1
  for (u32 s = 0; s < SPIN_CAP; ++s) {
    asm volatile("" ::: "memory");
    u32x2 f = ld8_sc1(r, voff, 0);
    if (__all(f.x == tlo && f.y == thi)) break;
    __builtin_amdgcn_s_sleep(1);
  }
  asm volatile("s_waitcnt vmcnt(0)" ::: "memory");
}
DI void lds_meet(LAS u32* c, u32 target, int lane) {
  asm volatile("s_waitcnt lgkmcnt(0)" ::: "memory");
  if (lane == 0) __hip_atomic_fetch_add(c, 1u, __ATOMIC_RELAXED, __HIP_MEMORY_SCOPE_WORKGROUP);
#pragma unroll 1
  for (u32 s = 0; s < SPIN_CAP * 16; ++s) {
    u32 v = __hip_atomic_load(c, __ATOMIC_RELAXED, __HIP_MEMORY_SCOPE_WORKGROUP);
    if (__builtin_amdgcn_readfirstlane(v) >= target) break;
  }
  asm volatile("s_waitcnt lgkmcnt(0)" ::: "memory");
}
template <int C> DI float dppadd(float x) {
  return x + __builtin_bit_cast(float, __builtin_amdgcn_update_dpp(0, __builtin_bit_cast(int, x), C, 0xf, 0xf, true));
}
DI float wave_sum(float x) {
  x = dppadd<0xB1>(x); x = dppadd<0x4E>(x); x = dppadd<0x141>(x); x = dppadd<0x140>(x);
  u32 u = __builtin_bit_cast(u32, x);
  auto a = __builtin_amdgcn_permlane16_swap(u, u, false, false);
  x = __builtin_bit_cast(float, (u32)a[0]) + __builtin_bit_cast(float, (u32)a[1]);
  u = __builtin_bit_cast(u32, x);
  auto b = __builtin_amdgcn_permlane32_swap(u, u, false, false);
  return __builtin_bit_cast(float, (u32)b[0]) + __builtin_bit_cast(float, (u32)b[1]);
}
struct cpx { float r, i; };
DI cpx cmul(cpx a, cpx b) { return {a.r * b.r - a.i * b.i, a.r * b.i + a.i * b.r}; }
DI cpx cexpl(float ar2, float air, float l) {
  float m = __builtin_amdgcn_exp2f(ar2 * l), f = __builtin_amdgcn_fractf(air * l);
  return {m * __builtin_amdgcn_cosf(f), m * __builtin_amdgcn_sinf(f)};
}

__global__ __launch_bounds__(512, 2) void s4ffn_kernel(Params p) {
  __shared__ __attribute__((aligned(16))) char smem[163840];
  LAS char* sm = (LAS char*)smem;
  const u32 smb = (u32)(size_t)sm;
  const int tid = threadIdx.x, lane = tid & 63;
  const int w = __builtin_amdgcn_readfirstlane(tid >> 6);
  const int b = blockIdx.x, xcd = b & 7, o = b >> 3;
  const bool aux = o >= 28;
  const int ma = xcd >> 1, fh = xcd & 1;
  const int il = o / 7, jl = o - il * 7;
  const int mi = 4 * ma + il, fj = 7 * fh + jl;
  const int at0 = 512 * ma + 64 * (4 * fh + (o - 28));
  const int ti2 = 4 * xcd + (o >> 3), n2 = o & 7;
  const int ch0 = 2 * b;
  const int hh = lane >> 5, blk = (lane >> 4) & 1, q4 = (lane >> 2) & 3, p4 = lane & 3;
  STAMP_RT(30); STAMP_AT(0);

  const rsrc_t rws = __builtin_amdgcn_make_buffer_rsrc((void*)p.ws, 0, WS_END, 0x00020000);
  const rsrc_t rx = __builtin_amdgcn_make_buffer_rsrc((void*)p.x, 0, L * D * 2, 0x00020000);
  const rsrc_t rwg = __builtin_amdgcn_make_buffer_rsrc((void*)p.wg, 0, D * F * 2, 0x00020000);
  const rsrc_t rwu = __builtin_amdgcn_make_buffer_rsrc((void*)p.wu, 0, D * F * 2, 0x00020000);
  const rsrc_t rwd = __builtin_amdgcn_make_buffer_rsrc((void*)p.wd, 0, D * F * 2, 0x00020000);

  u32x2 ev;
  asm volatile("buffer_load_dwordx2 %0, off, %1, %2 sc1" : "=&v"(ev) : "s"(rws), "s"(FL_E));

  const u32 vx0 = (lane >> 3) * 1024 + (((lane & 7) ^ (lane >> 4)) << 4);
  const u32 vx1 = vx0 ^ 64;
  const u32 vwg = (lane >> 4) * (F * 2) + (((((lane & 15) >> 1) ^ (lane >> 4)) << 5) | ((lane & 1) << 4));
  const u32 vwu = (lane >> 4) * (F * 2) + (((((lane & 15) >> 1) ^ (lane >> 4) ^ 4) << 5) | ((lane & 1) << 4));
  const u32 vwd = (lane >> 3) * (D * 2) + (((lane & 7) ^ ((lane >> 2) & 4)) << 4);
#define ROT (2 * ma)
  const int c0 = ROT;
  auto dma_chunk = [&](int step, int v, u32 slot) {
    const u32 c = (c0 + step) & 7;
    const u32 sx = (mi * 128 + 16 * v) * 1024 + c * 128;
    dma16(rx, smb + slot + 2048 * v, vx0, sx);
    dma16(rx, smb + slot + 2048 * v + 1024, vx1, sx + 8192);
    const u32 sw = (c * 64 + 8 * v) * (F * 2) + fj * 256;
    dma16(rwg, smb + slot + 16384 + 2048 * v, vwg, sw);
    dma16(rwg, smb + slot + 16384 + 2048 * v + 1024, vwg, sw + 4 * F * 2);
    dma16(rwu, smb + slot + 32768 + 2048 * v, vwu, sw);
    dma16(rwu, smb + slot + 32768 + 2048 * v + 1024, vwu, sw + 4 * F * 2);
  };
#define ROT2 0
  const int rot2 = ROT2;
  auto ks_of = [&](int gw, int idx) { int k = idx + rot2; k = k >= 28 ? k - 28 : k; return 28 * gw + k; };
  auto dma_wd = [&](int v, int e, int part) {
    const u32 dst = e < 3 ? 2 * SLOT + 6144 * v + 2048 * e : e < 6 ? 6144 * v + 2048 * (e - 3) : SLOT + 2048 * v;
    const u32 so = ks_of(v & 3, 14 * part + 7 * (v >> 2) + e) * (16 * D * 2) + n2 * 128;
    dma16(rwd, smb + dst, vwd, so);
    dma16(rwd, smb + dst + 1024, vwd, so + 8 * D * 2);
  };

  if (tid < 8) *(LAS u32*)(sm + L_CNT + 4 * tid) = 0;
  f32x16 kacc;
  if (w >= 4) {
    if (!aux) {
      dma_chunk(0, 2 * (w - 4), 0); dma_chunk(0, 2 * (w - 4) + 1, 0);
      dma_chunk(1, 2 * (w - 4), SLOT); dma_chunk(1, 2 * (w - 4) + 1, SLOT);
    } else {
#pragma unroll 4
      for (int r = 0; r < 16; ++r) dma16(rx, smb + (16 * (w - 4) + r) * 1024, lane * 16, (at0 + 16 * (w - 4) + r) * 1024);
    }
  } else {
    const int ch = ch0 + (w >> 1), gi = ch * NM + 64 * (w & 1) + lane;
    const u32 r0 = p.are[gi], r1 = p.aim[gi], r2 = p.bre[gi], r3 = p.bim[gi], r4 = p.cre[gi], r5 = p.cim[gi], r6 = p.logdt[ch];
    const float dt = rbf(__builtin_amdgcn_exp2f(bflo(r6) * 1.44269504088896f));
    const float ar2 = dt * bflo(r0) * 1.44269504088896f, air = dt * bflo(r1) * 0.159154943091895f;
    const cpx c = cmul({bflo(r4), bflo(r5)}, {dt * bflo(r2), dt * bflo(r3)});
    LAS char* scr = sm + L_SCR + 8192 * w;
    LAS char* wr = scr + lane * 128;
    const int sw = lane & 7;
    const int md = 4 * hh + (q4 >> 1), cki = 4 * (q4 & 1) + 2 * blk + (p4 >> 1);
    LAS char* rd0 = scr + md * 128 + ((cki ^ md) << 4) + 8 * (p4 & 1);
    LAS char* rd1 = scr + (md + 2) * 128 + ((cki ^ (md + 2)) << 4) + 8 * (p4 & 1);
    auto put8 = [&](int chunk, const cpx (&v)[8], float sgn) {
      u32x4 a = {pk(v[0].r, v[1].r), pk(v[2].r, v[3].r), pk(v[4].r, v[5].r), pk(v[6].r, v[7].r)};
      u32x4 d = {pk(sgn * v[0].i, sgn * v[1].i), pk(sgn * v[2].i, sgn * v[3].i), pk(sgn * v[4].i, sgn * v[5].i), pk(sgn * v[6].i, sgn * v[7].i)};
      *(LAS u32x4*)(wr + ((chunk ^ sw) << 4)) = a;
      *(LAS u32x4*)(wr + (((chunk + 4) ^ sw) << 4)) = d;
    };
    const cpx e8 = cexpl(ar2, air, 8.f);
    bf16x8 yf[8];
    {
      cpx lo[8], t[8];
      lo[0] = {1.f, 0.f}; lo[1] = e8; lo[2] = cmul(e8, e8); lo[3] = cmul(lo[2], e8); lo[4] = cmul(lo[2], lo[2]);
      lo[5] = cmul(lo[4], e8); lo[6] = cmul(lo[4], lo[2]); lo[7] = cmul(lo[4], lo[3]);
      put8(0, lo, 1.f);
      const cpx e64 = cexpl(ar2, air, 64.f);
      cpx hi = e64;
#pragma unroll
      for (int g = 1; g < 4; ++g) {
        t[0] = hi;
#pragma unroll
        for (int a = 1; a < 8; ++a) t[a] = cmul(hi, lo[a]);
        put8(g, t, 1.f);
        hi = cmul(hi, e64);
      }
#pragma unroll
      for (int s = 0; s < 8; ++s) yf[s] = trfrag(rd0 + 1024 * s, rd1 + 1024 * s);
    }
    {
      const cpx e1 = cexpl(ar2, air, 1.f), e256 = cexpl(ar2, air, 256.f);
      const cpx P2 = cmul(e1, e1), P3 = cmul(P2, e1), P4 = cmul(P2, P2), P5 = cmul(P4, e1), P6 = cmul(P4, P2), P7 = cmul(P4, P3);
      const cpx e512 = cmul(e256, e256), e1024 = cmul(e512, e512);
      const cpx B1 = cmul(c, e256), B2 = cmul(c, e512), B3 = cmul(B1, e512), B4 = cmul(c, e1024), B5 = cmul(B1, e1024), B6 = cmul(B2, e1024), B7 = cmul(B3, e1024);
      const cpx b14 = cmul(B1, P4), z = {0.f, 0.f};
      const cpx g0[8] = {c, cmul(c, e1), cmul(c, P2), cmul(c, P3), B1, B1, cmul(B1, P2), b14};
      const cpx g1[8] = {cmul(c, P4), cmul(c, P5), cmul(c, P6), cmul(c, P7), b14, b14, cmul(B1, P6), cmul(B1, e8)};
      const cpx g2[8] = {B2, cmul(B2, P4), cmul(B2, e8), B4, B3, cmul(B3, P4), cmul(B3, e8), B6};
      const cpx g3[8] = {cmul(B4, e8), B5, cmul(B5, e8), z, cmul(B6, e8), B7, cmul(B7, e8), z};
      asm volatile("s_waitcnt lgkmcnt(0)" ::: "memory");
      put8(0, g0, -1.f); put8(1, g1, -1.f); put8(2, g2, -1.f); put8(3, g3, -1.f);
      kacc = (f32x16)(0.f);
#pragma unroll
      for (int s = 0; s < 8; ++s) kacc = MFMA32(trfrag(rd0 + 1024 * s, rd1 + 1024 * s), yf[s], kacc);
    }
#pragma unroll
    for (int g = 0; g < 4; ++g) {
      f32x4 v = {kacc[4 * g], kacc[4 * g + 1], kacc[4 * g + 2], kacc[4 * g + 3]};
      *(LAS f32x4*)(sm + L_SCR + 32768 + 4096 * w + 1024 * g + 16 * lane) = v;
    }
  }
  STAMP_AT(1);
  BARRIER();
  if (w < 4) {
    float v[16];
#pragma unroll
    for (int g = 0; g < 4; ++g) {
      f32x4 t = *(LAS f32x4*)(sm + L_SCR + 32768 + 4096 * (w ^ 1) + 1024 * g + 16 * lane);
#pragma unroll
      for (int e = 0; e < 4; ++e) v[4 * g + e] = kacc[4 * g + e] + t[e];
    }
    LAS char* k0 = sm + L_K0 + K0S * (w >> 1) + 32 + 16 * (lane & 31);
    const bool odd = lane & 1;
    if (w & 1) {
      u32 aa = pk(v[11], v[11]), cc = pk(v[13], v[13]);
      u32x4 c2 = {aa, aa, pk(odd ? v[12] : v[11], v[12]), pk(v[12], v[12])};
      u32x4 c3 = {cc, cc, pk(odd ? v[14] : v[13], v[14]), pk(v[14], v[14])};
      *(LAS u32x4*)(k0 + 2048 + 1024 * hh) = c2;
      *(LAS u32x4*)(k0 + 2560 + 1024 * hh) = c3;
    } else {
      u32x4 c0v = {pk(v[0], v[1]), pk(v[2], v[3]), pk(v[4], v[5]), pk(v[6], v[7])};
      u32x4 c1v = {pk(v[8], v[8]), pk(v[8], v[9]), pk(v[9], v[9]), pk(v[10], v[10])};
      *(LAS u32x4*)(k0 + 512 * hh) = c0v;
      *(LAS u32x4*)(k0 + 1024 + 512 * hh) = c1v;
      if (lane < 2) *(LAS u32x4*)(k0 - 32) = (u32x4)(0u);
    }
  }
  if (aux) asm volatile("s_waitcnt vmcnt(0)" : "+v"(ev) :: "memory");
  else asm volatile("s_waitcnt vmcnt(12)" : "+v"(ev) :: "memory");
  u32 tlo, thi;
  {
    const u64 e = ((u64)__builtin_amdgcn_readfirstlane(ev.y) << 32 | (u32)__builtin_amdgcn_readfirstlane(ev.x)) + 0x9E3779B97F4A7C15ull;
    tlo = (u32)e; thi = (u32)(e >> 32);
  }
  const u32x2 tag = {tlo, thi};
  const u32 tg32 = tlo ^ thi;
  STAMP_AT(2);
  BARRIER();
  STAMP_AT(3);

  const u32 vf1 = FL_1 + ((ch0 >> 6) * 32 + (lane & 31)) * 8;
  u32x4 uval;
  if (!aux) {
    const int tg = w >> 2, fp = w & 3;
    u32 xa[4], wa[2];
    {
      const u32 row = 64 * tg + (lane & 31), f = (row >> 1) & 7;
#pragma unroll
      for (int s = 0; s < 4; ++s) xa[s] = row * 128 + (((2 * s + hh) ^ f) << 4);
#pragma unroll
      for (int t = 0; t < 2; ++t) wa[t] = 16384 + blk * 16384 + (8 * hh + q4) * 256 + (((2 * fp + (t ^ tg)) ^ (q4 + 4 * blk)) << 5) + p4 * 8;
    }
    f32x16 acc[2][2];
#pragma unroll
    for (int a = 0; a < 4; ++a) acc[a >> 1][a & 1] = (f32x16)(0.f);
    bf16x8 X0[2], W0[2], X1[2], W1[2];
    X1[0] = X1[1] = W1[0] = W1[1] = __builtin_bit_cast(bf16x8, (u32x4)(0u));
    float cs0 = 0.f, cs1 = 0.f;
    auto ld = [&](bf16x8 (&X)[2], bf16x8 (&W)[2], u32 sb, int s) {
      W[0] = trfrag(sm + sb + wa[0] + 4096 * s, sm + sb + wa[0] + 4096 * s + 1024);
      X[0] = ldfrag(sm + sb + xa[s]);
      X[1] = ldfrag(sm + sb + xa[s] + 4096);
      W[1] = trfrag(sm + sb + wa[1] + 4096 * s, sm + sb + wa[1] + 4096 * s + 1024);
    };
    auto mm = [&](const bf16x8 (&X)[2], const bf16x8 (&W)[2]) {
      acc[0][0] = MFMA32(W[0], X[0], acc[0][0]);
      acc[1][0] = MFMA32(W[0], X[1], acc[1][0]);
      acc[0][1] = MFMA32(W[1], X[0], acc[0][1]);
      acc[1][1] = MFMA32(W[1], X[1], acc[1][1]);
    };
    auto csum = [&](const bf16x8& W) {
      const u32x4 u = __builtin_bit_cast(u32x4, W);
#pragma unroll
      for (int e = 0; e < 4; ++e) {
        float a = bflo(u[e]), c = bfhi(u[e]);
        asm("v_add_f32 %0, %0, %1" : "+v"(cs0) : "v"(a));
        asm("v_add_f32 %0, %0, %1" : "+v"(cs1) : "v"(c));
      }
    };
    auto body = [&](u32 sb, auto issue) {
      ld(X0, W0, sb, 0); SCHED();
      issue(0); mm(X1, W1); SCHED();
      ld(X1, W1, sb, 1); SCHED();
      issue(1); mm(X0, W0); csum(W0[0]); SCHED();
      ld(X0, W0, sb, 2); SCHED();
      issue(2); mm(X1, W1); csum(W1[0]); SCHED();
      ld(X1, W1, sb, 3); SCHED();
      mm(X0, W0); csum(W0[0]); csum(W1[0]); SCHED();
    };
    u32 sb = 0, nb = 2 * SLOT;
#pragma unroll 1
    for (int it = 0; it < 6; ++it) {
      const u32 c = (c0 + it + 2) & 7;
      const u32 sx = (mi * 128 + 16 * w) * 1024 + c * 128, sw = (c * 64 + 8 * w) * (F * 2) + fj * 256, dst = smb + nb + 2048 * w;
      body(sb, [&](int e) {
        if (e == 0) { dma16(rx, dst, vx0, sx); dma16(rx, dst + 1024, vx1, sx + 8192); }
        if (e == 1) { dma16(rwg, dst + 16384, vwg, sw); dma16(rwg, dst + 17408, vwg, sw + 4 * F * 2); }
        if (e == 2) { dma16(rwu, dst + 32768, vwu, sw); dma16(rwu, dst + 33792, vwu, sw + 4 * F * 2); }
      });
      WAIT_V(6);
      BARRIER();
      nb = sb; sb = sb == 2 * SLOT ? 0 : sb + SLOT;
    }
    STAMP_AT(4);
    body(0, [&](int e) { dma_wd(w, e, 0); });
    WAIT_V(6);
    BARRIER();
    u32x2 g0, g1, f1;
    const u32 vst = WS_ST + (mi * 128 + 64 * tg + (lane & 31)) * 8;
    asm volatile("buffer_load_dwordx2 %0, %3, %5, 0 offen sc1\n\tbuffer_load_dwordx2 %1, %3, %5, 0 offen offset:256 sc1\n\tbuffer_load_dwordx2 %2, %4, %5, 0 offen sc1"
                 : "=&v"(g0), "=&v"(g1), "=&v"(f1) : "v"(vst), "v"(vf1), "s"(rws));
    body(SLOT, [&](int e) { dma_wd(w, e + 3, 0); });
    const float cs = cs0 + cs1, csh = rbf(cs);
    const u32 csp = pk(csh, cs - csh);
    *(LAS u32*)(sm + L_CS + 4 * tid) = csp;
    asm volatile("s_waitcnt vmcnt(6)" : "+v"(g0), "+v"(g1), "+v"(f1) :: "memory");
    BARRIER();
    STAMP_AT(5);
    const u32 csq = *(LAS u32*)(sm + L_CS + 4 * (tid ^ 256));
    mm(X1, W1);
    dma_wd(w, 6, 0);
#pragma unroll 1
    for (u32 s = 0; s < SPIN_CAP && !__all(g0.y == tg32 && g1.y == tg32); ++s) {
      asm volatile("" ::: "memory");
      g0 = ld8_sc1(rws, vst, 0); g1 = ld8_sc1(rws, vst, 256);
    }
    if (!__all(f1.x == tlo && f1.y == thi)) wait_flags(rws, vf1, tlo, thi);
    asm volatile("buffer_load_dwordx4 %0, %1, %2, %3 offen sc1" : "=&v"(uval) : "v"(tid * 16), "s"(rws), "s"(WS_XNT + ch0 * 4096) : "memory");
    {
      const u32x4 a0 = {csp, 0, 0, 0}, a1 = {csq, 0, 0, 0};
      const u32 m0 = (g0.x & 0xffffu) ^ 0x8000u, m1 = (g1.x & 0xffffu) ^ 0x8000u;
      const u32x4 b0 = {m0 | m0 << 16, 0, 0, 0}, b1 = {m1 | m1 << 16, 0, 0, 0};
      bf16x8 XM[2] = {__builtin_bit_cast(bf16x8, b0), __builtin_bit_cast(bf16x8, b1)};
      bf16x8 WM[2] = {__builtin_bit_cast(bf16x8, a0), __builtin_bit_cast(bf16x8, a1)};
      mm(XM, WM);
      const u32 sh = ((2 * mi + tg) * 112 + 8 * fj + 2 * fp) * 2048;
#pragma unroll
      for (int tt = 0; tt < 2; ++tt) {
        const float sd = bfhi(tt ? g1.x : g0.x), r = __builtin_amdgcn_rcpf(sd);
        const float kb = -2.f * 0.7978845608028654f * 1.44269504088896f * r, ka = kb * 0.044715f * r * r, kr = sd * sd;
#pragma unroll
        for (int ft = 0; ft < 2; ++ft) {
          u32x4 hv;
#pragma unroll
          for (int e = 0; e < 4; ++e) {
            const f32x2 g = {acc[tt][ft][2 * e], acc[tt][ft][2 * e + 1]}, u = {acc[tt][ft][8 + 2 * e], acc[tt][ft][9 + 2 * e]};
            const f32x2 z = (g * g * ka + kb) * g;
            const f32x2 ex = {__builtin_amdgcn_exp2f(z.x), __builtin_amdgcn_exp2f(z.y)};
            const f32x2 dn = ex * kr + kr;
            const f32x2 rc = {__builtin_amdgcn_rcpf(dn.x), __builtin_amdgcn_rcpf(dn.y)};
            const f32x2 hq = g * u * rc;
            hv[e] = pk(hq.x, hq.y);
          }
          st16_sc1(rws, hv, lane * 16 + 1024 * tt, sh + (ft ^ tg) * 2048);
        }
      }
    }
    STAMP_AT(6);
    asm volatile("s_waitcnt vmcnt(4)" : "+v"(uval) :: "memory");
  } else {
    u32 xp[8][4];
    u32 gran = 0;
#pragma unroll
    for (int r2 = 0; r2 < 4; ++r2) {
      float n[2][8];
#pragma unroll
      for (int rr = 0; rr < 2; ++rr) {
        const u32x4 u = *(LAS u32x4*)(sm + (8 * w + 2 * r2 + rr) * 1024 + lane * 16);
        float v[8], s = 0.f, qq = 0.f;
#pragma unroll
        for (int e = 0; e < 4; ++e) { v[2 * e] = bflo(u[e]); v[2 * e + 1] = bfhi(u[e]); }
#pragma unroll
        for (int e = 0; e < 8; ++e) s += v[e];
        const float mean = wave_sum(s) * (1.f / D);
#pragma unroll
        for (int e = 0; e < 8; ++e) qq += (v[e] - mean) * (v[e] - mean);
        const float m = rbf(mean), var = rbf(wave_sum(qq) * (1.f / D));
        const float sd = rbf(__builtin_sqrtf(rbf(var + 1e-5f))), r = 1.f / sd;
#pragma unroll
        for (int e = 0; e < 8; ++e) n[rr][e] = rbf(v[e] - m) * r;
        if (lane == 2 * r2 + rr) gran = pk(m, sd);
      }
#pragma unroll
      for (int e = 0; e < 8; ++e) xp[e][r2] = pk(n[0][e], n[1][e]);
    }
    if (lane < 8) { u32x2 g = {gran, tg32}; st8_sc1(rws, g, WS_ST + (at0 + 8 * w + lane) * 8, 0); }
#pragma unroll
    for (int e = 0; e < 8; ++e) {
      u32x4 v = {xp[e][0], xp[e][1], xp[e][2], xp[e][3]};
      *(LAS u32x4*)(sm + 65536 + (8 * lane + e) * 128 + ((w ^ (lane & 7)) << 4)) = v;
    }
    BARRIER();
#pragma unroll
    for (int n = 0; n < 8; ++n) {
      const u32 row = 64 * w + 8 * n + (lane >> 3);
      const u32x4 v = *(LAS u32x4*)(sm + 65536 + row * 128 + (((lane & 7) ^ n) << 4));
      st16_sc1(rws, v, row * 4096 + (lane & 7) * 16, WS_XNT + at0 * 2);
    }
    WAIT_V(0);
    if (lane == 0) st8_sc1(rws, tag, FL_1 + (w * 32 + (at0 >> 6)) * 8, 0);
    STAMP_AT(4);
    BARRIER();
#pragma unroll
    for (int e = 0; e < 7; ++e) dma_wd(w, e, 0);
    wait_flags(rws, vf1, tlo, thi);
    STAMP_AT(5);
    uval = ld16_sc1(rws, tid * 16, WS_XNT + ch0 * 4096);
  }
  {
    const int c = tid >> 8, m = tid & 255;
    *(LAS u32x4*)(sm + (c ? L_U1 : L_U0) + 512 + 16 * m) = uval;
    LAS char* src = sm + L_K0 + K0S * c + 32 + 16 * m;
    const u32x2 pr = *(LAS u32x2*)(src - 8);
    const u32x4 cu = *(LAS u32x4*)src;
    const u32 s0 = __builtin_amdgcn_alignbit(pr.y, pr.x, 16), s1 = __builtin_amdgcn_alignbit(cu.x, pr.y, 16), s2 = __builtin_amdgcn_alignbit(cu.y, cu.x, 16),
              s3 = __builtin_amdgcn_alignbit(cu.z, cu.y, 16), s4 = __builtin_amdgcn_alignbit(cu.w, cu.z, 16);
    LAS char* dst = sm + L_KS + KSS * c + 32 + 16 * m;
    const u32x4 k1 = {s1, s2, s3, s4}, k2 = {pr.y, cu.x, cu.y, cu.z}, k3 = {s0, s1, s2, s3};
    *(LAS u32x4*)dst = k1;
    *(LAS u32x4*)(dst + KCP) = k2;
    *(LAS u32x4*)(dst + 2 * KCP) = k3;
    if (m < 32) *(LAS u32x4*)(sm + (c ? L_U1 : L_U0) + 16 * m) = (u32x4)(0u);
    if (m < 6) *(LAS u32x4*)(sm + L_KS + KSS * c + KCP * (m >> 1) + 16 * (m & 1)) = (u32x4)(0u);
  }
  WAIT_V(0);
  if (!aux && lane == 0) st8_sc1(rws, tag, FL_2 + ((2 * mi + (w >> 2)) * 64 + 4 * fj + (w & 3)) * 8, 0);
  STAMP_AT(7);
  BARRIER();
  STAMP_AT(8);

  const u32 vo = (64 * ti2 + (tid >> 3)) * (D * 2) + n2 * 128 + (tid & 7) * 16;
  const u32x4 xres = __builtin_amdgcn_raw_buffer_load_b128(rx, vo, 0, 0);
  if (w < 4) {
    const int c = w >> 1, ah = w & 1, g = lane >> 4;
    const float dsk = bflo(p.dskip[ch0 + c]);
    LAS char* ub = sm + (c ? L_U1 : L_U0);
    LAS char* ka = sm + (q4 ? L_KS + KSS * c + KCP * (q4 - 1) : L_K0 + K0S * c) + 2 * (28 + 128 * ah + 4 * p4 - 8 * g);
    LAS char* ua = ub + 2 * (240 - 96 - 128 * ah + 16 * (lane & 15) + 8 * g);
    f32x4 y[8];
#pragma unroll
    for (int i = 0; i < 8; ++i) y[i] = (f32x4)(0.f);
#pragma unroll
    for (int a = 0; a < 4; ++a) {
      bf16x8 A[8], B[8];
#pragma unroll
      for (int i = 0; i < 8; ++i) {
        A[i] = trfrag(ka + 64 * a + 512 * i + 8, ka + 64 * a + 512 * i);
        B[i] = ldfrag(ua + 64 * (3 - a) + 512 * i);
      }
#pragma unroll
      for (int s = 0; s < 8; ++s)
#pragma unroll
        for (int i = 0; i <= s; ++i) {
          if (i + s < 8) y[i + s] = MFMA16(A[i], B[s], y[i + s]);
          if (i < s && i + s < 8) y[i + s] = MFMA16(A[s], B[i], y[i + s]);
        }
    }
    STAMP_AT(9);
    LAS u32* cnt = (LAS u32*)(sm + L_CNT) + 2 * c;
    LAS char* ex = sm + L_KS + KSS * c;
    lds_meet(cnt, 2, lane);
#pragma unroll
    for (int i = 0; i < 4; ++i) {
      const f32x4 lo = y[i], hi = y[4 + i];
      *(LAS f32x4*)(ex + 4096 * ah + 1024 * i + 16 * lane) = ah ? lo : hi;
      y[i] = ah ? hi : lo;
    }
    lds_meet(cnt + 1, 2, lane);
    const u32 tb = 2 * (1024 * ah + 16 * (lane & 15) + 4 * g);
#pragma unroll
    for (int i = 0; i < 4; ++i) {
      const f32x4 v = *(LAS f32x4*)(ex + 4096 * (ah ^ 1) + 1024 * i + 16 * lane) + y[i];
      const u32x2 u = *(LAS u32x2*)(ub + 512 + tb + 512 * i);
      const u32 y0 = pk(v[0], v[1]), y1 = pk(v[2], v[3]);
      const u32 d0 = pk(bflo(u.x) * dsk, bfhi(u.x) * dsk), d1 = pk(bflo(u.y) * dsk, bfhi(u.y) * dsk);
      const u32x2 z = {pk(bflo(y0) + bflo(d0), bfhi(y0) + bfhi(d0)), pk(bflo(y1) + bflo(d1), bfhi(y1) + bfhi(d1))};
      st8_sc1(rws, z, tb + 512 * i, WS_ZT + (ch0 + c) * 4096);
    }
    WAIT_V(0);
    if (lane == 0) st8_sc1(rws, tag, FL_3 + (ah * 512 + ch0 + c) * 8, 0);
    STAMP_AT(10);
    wait_flags(rws, FL_3 + ((ti2 >> 4) * 512 + 64 * n2 + 16 * w + (lane & 15)) * 8, tlo, thi);
    STAMP_AT(11);
    const u32 vz = (64 * n2 + 16 * w + (lane >> 3)) * 4096 + 128 * ti2 + (lane & 7) * 16;
    const u32x4 z0 = ld16_sc1(rws, vz, WS_ZT), z1 = ld16_sc1(rws, vz, WS_ZT + 8 * 4096);
    LAS char* stg = sm + L_K0 + K0S * c + 2048 * ah;
    *(LAS u32x4*)(stg + 16 * lane) = z0;
    *(LAS u32x4*)(stg + 1024 + 16 * lane) = z1;
    LAS char* tr = stg + q4 * 128 + 32 * g + 8 * p4;
    const bf16x8 r0 = trfrag(tr, tr + 512), r1 = trfrag(tr + 1024, tr + 1536);
    LAS char* rb = ex + 8192 + 2048 * ah + 32 * lane;
    *(LAS bf16x8*)rb = r0;
    *(LAS bf16x8*)(rb + 16) = r1;
    if (b == 255 && w == 0) {
#pragma unroll 1
      for (int i = 0; i < 4; ++i) wait_flags(rws, FL_3 + (128 * i + 2 * lane) * 8, tlo, thi);
      if (lane == 0) st8_sc1(rws, tag, FL_E, 0);
    }
    STAMP_AT(12);
  } else {
    const int gw = w - 4;
    const u32 lo = (4 * hh + q4) * 128 + p4 * 8;
    const u32 o0 = lo + ((blk ^ (q4 & 2)) << 5), o1 = o0 ^ 64;
    auto wfrag = [&](int i, int ct) {
      const int v = i / 7, e = i % 7;
      const u32 base = e < 3 ? 2 * SLOT + 6144 * (gw + 4 * v) + 2048 * e : e < 6 ? 6144 * (gw + 4 * v) + 2048 * (e - 3) : SLOT + 2048 * (gw + 4 * v);
      LAS char* a = sm + base + (ct ? o1 : o0);
      return trfrag(a, a + 1024);
    };
    bf16x8 wr[14][2];
#pragma unroll
    for (int i = 0; i < 14; ++i) { wr[i][0] = wfrag(i, 0); wr[i][1] = wfrag(i, 1); }
    asm volatile("s_waitcnt lgkmcnt(0)" ::: "memory");
    SCHED();
#pragma unroll
    for (int e = 0; e < 7; ++e) { dma_wd(gw, e, 1); dma_wd(gw + 4, e, 1); }
    STAMP_AT(9);
    wait_flags(rws, FL_2 + (ti2 * 64 + 14 * gw + (lane < 14 ? lane : 0)) * 8, tlo, thi);
    STAMP_AT(10);
    f32x16 acc[2][2];
#pragma unroll
    for (int a = 0; a < 4; ++a) acc[a >> 1][a & 1] = (f32x16)(0.f);
    constexpr int PD = 4;
    u32x4 hf[28][2];
    auto hload = [&](int i) {
      const u32 so = (ti2 * 112 + ks_of(gw, i)) * 2048;
      hf[i][0] = ld16_sc1(rws, lane * 16, so);
      hf[i][1] = ld16_sc1(rws, lane * 16 + 1024, so);
    };
#pragma unroll
    for (int i = 0; i < PD; ++i) hload(i);
    bf16x8 wl[2][2];
    SCHED();
#pragma unroll
    for (int i = 0; i < 28; ++i) {
      if (i + 1 >= 14 && i + 1 < 28) { wl[(i + 1) & 1][0] = wfrag(i + 1 - 14, 0); wl[(i + 1) & 1][1] = wfrag(i + 1 - 14, 1); }
      const bf16x8 a0 = i < 14 ? wr[i < 14 ? i : 0][0] : wl[i & 1][0], a1 = i < 14 ? wr[i < 14 ? i : 0][1] : wl[i & 1][1];
      const bf16x8 b0 = __builtin_bit_cast(bf16x8, hf[i][0]), b1 = __builtin_bit_cast(bf16x8, hf[i][1]);
      acc[0][0] = MFMA32(a0, b0, acc[0][0]);
      acc[1][0] = MFMA32(a1, b0, acc[1][0]);
      acc[0][1] = MFMA32(a0, b1, acc[0][1]);
      acc[1][1] = MFMA32(a1, b1, acc[1][1]);
      SCHED();
      if (i < 4) { hload(PD + 2 * i); hload(PD + 2 * i + 1); }
      else if (i + PD + 4 < 28) hload(i + PD + 4);
      SCHED();
    }
    STAMP_AT(11);
#pragma unroll
    for (int a = 0; a < 4; ++a) {
      LAS char* t = sm + (a & 1 ? 2 * SLOT : 0) + 6144 * (gw + 4 * (a >> 1)) + (lane & 31) * 144 + 16 * hh;
#pragma unroll
      for (int g = 0; g < 4; ++g) {
        const f32x16& s = acc[a >> 1][a & 1];
        f32x4 v = {s[4 * g], s[4 * g + 1], s[4 * g + 2], s[4 * g + 3]};
        *(LAS f32x4*)(t + 32 * g) = v;
      }
    }
    STAMP_AT(12);
  }
  BARRIER();
  STAMP_AT(13);
  {
    const int tok = tid >> 3, dc = tid & 7;
    LAS char* t = sm + (tok >> 5 ? 2 * SLOT : 0) + 6144 * 4 * (dc >> 2) + (tok & 31) * 144 + 32 * (dc & 3);
    f32x4 s0 = (f32x4)(0.f), s1 = (f32x4)(0.f);
#pragma unroll
    for (int g = 0; g < 4; ++g) { s0 += *(LAS f32x4*)(t + 6144 * g); s1 += *(LAS f32x4*)(t + 6144 * g + 16); }
    const u32x4 z = *(LAS u32x4*)(sm + L_KS + KSS * (dc >> 2) + 8192 + 2048 * ((dc >> 1) & 1) + 32 * tok + 16 * (dc & 1));
    const u32x4 ff = {pk(s0[0], s0[1]), pk(s0[2], s0[3]), pk(s1[0], s1[1]), pk(s1[2], s1[3])};
    u32x4 r;
#pragma unroll
    for (int e = 0; e < 4; ++e) {
      const u32 a = pk(bflo(xres[e]) + bflo(z[e]), bfhi(xres[e]) + bfhi(z[e]));
      r[e] = pk(bflo(a) + bflo(ff[e]), bfhi(a) + bfhi(ff[e]));
    }
    *(u32x4*)((char*)p.out + vo) = r;
  }
  STAMP_AT(14); STAMP_RT(31);
}

extern "C" void kernel_launch(void* const* d_in, const int* in_sizes, int n_in, void* d_out, int out_size, void* d_ws,
                              size_t ws_size, hipStream_t stream) {
  Params p;
  p.x = (const u16*)d_in[0]; p.are = (const u16*)d_in[1]; p.aim = (const u16*)d_in[2];
  p.bre = (const u16*)d_in[3]; p.bim = (const u16*)d_in[4]; p.cre = (const u16*)d_in[5];
  p.cim = (const u16*)d_in[6]; p.logdt = (const u16*)d_in[7]; p.dskip = (const u16*)d_in[8];
  p.wg = (const u16*)d_in[10]; p.wu = (const u16*)d_in[11]; p.wd = (const u16*)d_in[12];
  p.out = (u16*)d_out; p.ws = (char*)d_ws;
  s4ffn_kernel<<<256, 512, 0, stream>>>(p);
}
```

```cpp
#include <hip/hip_runtime.h>
#include <stdint.h>

typedef unsigned short u16;
typedef unsigned u32;
typedef unsigned long long u64;
typedef __bf16 bf16x2 __attribute__((ext_vector_type(2)));
typedef __bf16 bf16x4 __attribute__((ext_vector_type(4)));
typedef __bf16 bf16x8 __attribute__((ext_vector_type(8)));
typedef float f32x2 __attribute__((ext_vector_type(2)));
typedef float f32x4 __attribute__((ext_vector_type(4)));
typedef float f32x16 __attribute__((ext_vector_type(16)));
typedef u32 u32x2 __attribute__((ext_vector_type(2)));
typedef u32 u32x4 __attribute__((ext_vector_type(4)));
#define LAS __attribute__((address_space(3)))
#define GAS __attribute__((address_space(1)))
#define DI __device__ __forceinline__
typedef __amdgpu_buffer_rsrc_t rsrc_t;

constexpr int L = 2048, D = 512, NM = 128, F = 1792;
constexpr u32 WS_H = 0, WS_U = 8u << 20, WS_ZT = 10u << 20, WS_ST = 12u << 20, WS_CS = WS_ST + 65536, WS_FL = 13u << 20, WS_END = 14u << 20;
constexpr u32 FL_E = WS_FL, FL_1 = WS_FL + 4096, FL_2 = WS_FL + 131072, FL_3 = WS_FL + 262144, FS = 128;
constexpr u32 UNIT = 16384, FREE = 8 * UNIT;
constexpr u32 KCP = 4160, K0S = 4352, KSS = 3 * KCP + 64;
constexpr u32 L_KS = 7 * UNIT + 64;
constexpr u32 L_SCR = FREE;
constexpr u32 L_U = FREE + 8832, US = 4608;
constexpr u32 L_K0 = FREE + 18176;
constexpr u32 L_ST = L_K0 + 2 * K0S;
constexpr u32 L_CS = L_ST + 1024;
constexpr u32 L_SY = L_CS + 2048;
constexpr u32 S_FRE = L_SY + 16, S_HP = L_SY + 32, S_GP = L_SY + 48;
constexpr u32 SPIN_CAP = 1u << 16;

struct Params {
  const u16 *x, *are, *aim, *bre, *bim, *cre, *cim, *logdt, *dskip, *wg, *wu, *wd;
  u16* out;
  char* ws;
};

#define STAMP_DECL
#define STAMP_AT(k)
#define STAMP_RT(v)
#define STAMP_OUT()

DI float bflo(u32 v) { return __builtin_bit_cast(float, v << 16); }
DI float bfhi(u32 v) { return __builtin_bit_cast(float, v & 0xffff0000u); }
DI u32 pk(float a, float b) { f32x2 f = {a, b}; return __builtin_bit_cast(u32, __builtin_convertvector(f, bf16x2)); }
DI float rbf(float a) { return bflo(pk(a, a)); }
DI void dma4(rsrc_t ra, rsrc_t rb, u32 lds, u32 v0, u32 v1, u32 v2, u32 v3, u32 soff) {
  asm volatile("s_mov_b32 m0, %0\n\ts_nop 0\n\tbuffer_load_dwordx4 %1, %5, %7 offen lds\n\tbuffer_load_dwordx4 %2, %5, %7 offen offset:1024 lds\n\t"
               "buffer_load_dwordx4 %3, %6, %7 offen offset:2048 lds\n\tbuffer_load_dwordx4 %4, %6, %7 offen offset:3072 lds"
               :: "s"(lds), "v"(v0), "v"(v1), "v"(v2), "v"(v3), "s"(ra), "s"(rb), "s"(soff));
}
#define WAIT_V(n) asm volatile("s_waitcnt vmcnt(" #n ")" ::: "memory")
#define WAIT_L() asm volatile("s_waitcnt lgkmcnt(0)" ::: "memory")
#define BARRIER() asm volatile("s_waitcnt lgkmcnt(0)\n\ts_barrier" ::: "memory")
#define FENCE() asm volatile("" ::: "memory")
#define SCHED() __builtin_amdgcn_sched_barrier(0)
DI bf16x8 ldfrag(LAS char* a) { return *(LAS bf16x8*)a; }
DI bf16x8 trfrag(LAS char* a0, LAS char* a1) {
  bf16x4 lo = __builtin_amdgcn_ds_read_tr16_b64_v4bf16((LAS bf16x4*)a0);
  bf16x4 hi = __builtin_amdgcn_ds_read_tr16_b64_v4bf16((LAS bf16x4*)a1);
  return __builtin_shufflevector(lo, hi, 0, 1, 2, 3, 4, 5, 6, 7);
}
#define MFMA32(a, b, c) __builtin_amdgcn_mfma_f32_32x32x16_bf16(a, b, c, 0, 0, 0)
#define MFMA16(a, b, c) __builtin_amdgcn_mfma_f32_16x16x32_bf16(a, b, c, 0, 0, 0)
DI u32x2 ld8_sc1(rsrc_t r, u32 voff, u32 soff) { return __builtin_amdgcn_raw_buffer_load_b64(r, voff, soff, 16); }
DI u32x4 ld16_sc1(rsrc_t r, u32 voff, u32 soff) { return __builtin_amdgcn_raw_buffer_load_b128(r, voff, soff, 16); }
DI void st8_sc1(rsrc_t r, u32x2 v, u32 voff, u32 soff) { __builtin_amdgcn_raw_buffer_store_b64(v, r, voff, soff, 16); }
DI void st16_sc1(rsrc_t r, u32x4 v, u32 voff) { __builtin_amdgcn_raw_buffer_store_b128(v, r, voff, 0, 16); }
template <int SLEEP> DI void wait_flags(rsrc_t r, u32 voff, u32 tlo, u32 thi) {
#pragma unroll 1
  for (u32 s = 0; s < SPIN_CAP; ++s) {
    FENCE();
    u32x2 f = ld8_sc1(r, voff, 0);
    if (__all(f.x == tlo && f.y == thi)) break;
    __builtin_amdgcn_s_sleep(SLEEP);
  }
  WAIT_V(0);
}
DI void post(LAS char* word, u32 v) {
  asm volatile("s_mov_b64 exec, 1\n\tds_write_b32 %0, %1\n\ts_mov_b64 exec, -1" :: "v"(word), "v"(v) : "memory");
}
DI u32 peek(LAS char* word) { return *(volatile LAS u32*)word; }
DI void await(LAS char* word, u32 seen, u32 need) {
  if (__builtin_expect(__any(seen < need), 0)) {
#pragma unroll 1
    for (u32 s = 0; s < SPIN_CAP * 16; ++s) if (!__any(peek(word) < need)) break;
  }
  FENCE();
}
#define DPPADD(name, ctl) DI float name(float x) { float r; asm("s_nop 1\n\tv_add_f32_dpp %0, %1, %1 " ctl " row_mask:0xf bank_mask:0xf" : "=v"(r) : "v"(x)); return r; }
DPPADD(add_x1, "quad_perm:[1,0,3,2]") DPPADD(add_x2, "quad_perm:[2,3,0,1]") DPPADD(add_m8, "row_half_mirror") DPPADD(add_x8, "row_ror:8")
DI float fold32(float a, float b) {
  asm("s_nop 1\n\tv_permlane32_swap_b32 %0, %1\n\tv_add_f32 %0, %0, %1" : "+v"(a), "+v"(b)); return a;
}
DI float fold16(float a, float b) {
  asm("s_nop 1\n\tv_permlane16_swap_b32 %0, %1\n\tv_add_f32 %0, %0, %1" : "+v"(a), "+v"(b)); return a;
}
struct cpx { float r, i; };
struct cp2 { f32x2 r, i; };
DI cpx cmul(cpx a, cpx b) { return {a.r * b.r - a.i * b.i, a.r * b.i + a.i * b.r}; }
DI cpx csq(cpx a) { return {a.r * a.r - a.i * a.i, (a.r + a.r) * a.i}; }
DI cp2 cmul2(cp2 a, cpx b) { return {a.r * b.r - a.i * b.i, a.i * b.r + a.r * b.i}; }
DI cpx cexpl(float ar2, float air, float l) {
  float m = __builtin_amdgcn_exp2f(ar2 * l), f = __builtin_amdgcn_fractf(air * l);
  return {m * __builtin_amdgcn_cosf(f), m * __builtin_amdgcn_sinf(f)};
}

__global__ __launch_bounds__(512, 2) void s4ffn_kernel(Params p) {
  __shared__ __attribute__((aligned(16))) char smem[163840];
  LAS char* sm = (LAS char*)smem;
  const u32 smb = (u32)(size_t)sm;
  const int tid = threadIdx.x, lane = tid & 63;
  const int w = __builtin_amdgcn_readfirstlane(tid >> 6), v = w & 3;
  const int b = blockIdx.x, xcd = b & 7, o = b >> 3;
  const bool aux = o >= 28;
  const int ma = xcd >> 1, fh = xcd & 1;
  const int il = o / 7, jl = o - il * 7;
  const int mi = 4 * ma + il, fj = 7 * fh + jl;
  const int ti2 = 4 * xcd + (o >> 3), n2 = o & 7;
  const int tb = 32 * xcd + o, ch0 = 16 * o + xcd;
  const int hh = lane >> 5, blk = (lane >> 4) & 1, q4 = (lane >> 2) & 3, p4 = lane & 3, g4 = lane >> 4;
  STAMP_DECL; STAMP_RT(rt0_); STAMP_AT(0);
  const rsrc_t rx = __builtin_amdgcn_make_buffer_rsrc((void*)p.x, 0, L * D * 2, 0x00020000);
  const rsrc_t rwg = __builtin_amdgcn_make_buffer_rsrc((void*)p.wg, 0, D * F * 2, 0x00020000);
#define ROT (2 * ma)
#define ROT2 0
  const rsrc_t rws = __builtin_amdgcn_make_buffer_rsrc((void*)p.ws, 0, WS_END, 0x00020000);
  u64 ev;
#define LOAD_EPOCH() asm volatile("s_load_dwordx2 %0, %1, 0\n\ts_waitcnt lgkmcnt(0)" : "=&s"(ev) : "s"(p.ws + FL_E) : "memory")
#define MAKE_TAG() const u32 tlo = (u32)(ev + 0x9E3779B97F4A7C15ull), thi = (u32)((ev + 0x9E3779B97F4A7C15ull) >> 32), tg32 = tlo ^ thi; const u32x2 tag = {tlo, thi}
  LAS char* const sy = sm + 4 * p4;
  const u32 vo = (64 * ti2 + (tid >> 3)) * (D * 2) + n2 * 128 + (tid & 7) * 16;
  u32x4 xres;

  if (w < 4) {
  if (w < 2) {
    if (!aux) {
      const rsrc_t rwu2 = __builtin_amdgcn_make_buffer_rsrc((void*)((const char*)p.wu - 2048), 0, D * F * 2 + 2048, 0x00020000);
      const u32 vx0 = (lane >> 3) * 1024 + (((lane & 7) ^ g4) << 4), vx1 = (vx0 ^ 64) + 7168, vx2 = vx0 + 14336, vx3 = vx1 + 14336;
      const u32 vg0 = g4 * (F * 2) + (((((lane & 15) >> 1) ^ g4) << 5) | ((lane & 1) << 4)), vg1 = vg0 + 4 * F * 2 - 1024;
      const u32 vu0 = vg0 ^ 128, vu1 = vu0 + 4 * F * 2 - 1024;
      const u32 sx0 = (mi * 128 + 64 * v) * 1024, sw0 = 16 * v * (F * 2) + fj * 256;
      u32 dst = 8192 * v;
      auto ux = [&](u32 c) {
        const u32 s = sx0 + ((ROT + c) & 7) * 128;
        dma4(rx, rx, smb + dst, vx0, vx1, vx2, vx3, s); dma4(rx, rx, smb + dst + 4096, vx0, vx1, vx2, vx3, s + 32768);
        dst = (dst + UNIT) & (FREE - 1);
      };
      auto uw = [&](u32 c, u32 h) {
        const u32 s = sw0 + (2 * ((ROT + c) & 7) + h) * (32 * F * 2);
        dma4(rwg, rwu2, smb + dst, vg0, vg1, vu0, vu1, s); dma4(rwg, rwu2, smb + dst + 4096, vg0, vg1, vu0, vu1, s + 8 * F * 2);
        dst = (dst + UNIT) & (FREE - 1);
      };
#define LDELAY 2
      __builtin_amdgcn_s_sleep(LDELAY);
      ux(0); uw(0, 0);
      LOAD_EPOCH();
#pragma unroll 1
      for (int c = 0; c < 7; ++c) {
        uw(c, 1); ux(c + 1); uw(c + 1, 0);
        WAIT_V(16);
        if (c == 0) STAMP_AT(1);
        __builtin_amdgcn_s_barrier();
      }
      uw(7, 1);
      asm volatile("s_mov_b32 m0, %0\n\ts_nop 0\n\tbuffer_load_dwordx4 %2, %3, %4 offen sc1 lds\n\ts_mov_b32 m0, %1\n\ts_nop 0\n\tbuffer_load_dwordx4 %2, %3, %5 offen sc1 lds"
                   :: "s"(smb + L_ST), "s"(smb + L_CS + 1024 * v), "v"(lane * 16), "s"(rws), "s"(WS_ST + mi * 1024), "s"(WS_CS + (v * F + fj * 128) * 8));
      STAMP_AT(2);
    } else { LOAD_EPOCH(); __builtin_amdgcn_s_barrier(); }
  }
    const rsrc_t rwd = __builtin_amdgcn_make_buffer_rsrc((void*)p.wd, 0, D * F * 2, 0x00020000);
    const int rot2 = ROT2;
    u32x4 kc[4][4], uu[4], f1;
    const int k = v - 2;
    const u32 vf1 = FL_1 + (128 * k + 2 * lane) * 8;
    if (v >= 2) {
      auto bar = [&] { SCHED(); if (!aux) __builtin_amdgcn_s_barrier(); FENCE(); SCHED(); };
      auto pull = [&](int i, int skip) {
        u32 d;
        asm volatile(".set pf_off, 0\n\t.rept 14\n\ts_load_dword %0, %1, pf_off\n\t.set pf_off, pf_off + 64\n\t.endr\n\t"
                     "s_cmp_ge_i32 %2, 28\n\ts_cbranch_scc1 1f\n\ts_barrier\n1:\n\ts_waitcnt lgkmcnt(0)"
                     : "=&s"(d) : "s"((const char*)p.wd + tb * 7168 + 896 * i), "s"(skip) : "scc", "memory");
      };
      if (lane < 16) *(LAS u32*)(sm + L_SY + 4 * lane) = (0x0300 >> lane & 1) * 255;
      if (k == 0) {
        u32x4 xr[8];
#pragma unroll
        for (int i = 0; i < 8; ++i) xr[i] = __builtin_amdgcn_raw_buffer_load_b128(rx, lane * 16 + 1024 * (i & 3), (8 * tb + (i & 4)) * 1024, 0);
        LOAD_EPOCH();
        float x[8][8], su[8], sq[8];
#pragma unroll
        for (int r = 0; r < 8; ++r) {
#pragma unroll
          for (int e = 0; e < 4; ++e) { x[r][2 * e] = bflo(xr[r][e]); x[r][2 * e + 1] = bfhi(xr[r][e]); }
          asm("v_add_f32 %0, %2, %3\n\tv_mul_f32 %1, %2, %2\n\tv_add_f32 %0, %0, %4\n\tv_fmac_f32 %1, %3, %3\n\tv_add_f32 %0, %0, %5\n\tv_fmac_f32 %1, %4, %4\n\t"
              "v_add_f32 %0, %0, %6\n\tv_fmac_f32 %1, %5, %5\n\tv_add_f32 %0, %0, %7\n\tv_fmac_f32 %1, %6, %6\n\tv_add_f32 %0, %0, %8\n\tv_fmac_f32 %1, %7, %7\n\t"
              "v_add_f32 %0, %0, %9\n\tv_fmac_f32 %1, %8, %8\n\tv_fmac_f32 %1, %9, %9"
              : "=&v"(su[r]), "=&v"(sq[r])
              : "v"(x[r][0]), "v"(x[r][1]), "v"(x[r][2]), "v"(x[r][3]), "v"(x[r][4]), "v"(x[r][5]), "v"(x[r][6]), "v"(x[r][7]));
        }
        STAMP_AT(1);
        bar();
        MAKE_TAG();
        auto total = [&](const float (&a)[8]) {
          const float lo = add_x8(fold16(fold32(a[0], a[2]), fold32(a[1], a[3]))), hi = add_x8(fold16(fold32(a[4], a[6]), fold32(a[5], a[7])));
          return add_m8(add_x2(add_x1(lane & 8 ? hi : lo)));
        };
        const float mean = total(su) * (1.f / D), nsq = -mean * mean;
        const float var = rbf(__builtin_fmaxf(__builtin_fmaf(total(sq), 1.f / D, nsq), 0.f));
        const u32 gran = pk(mean, __builtin_amdgcn_sqrtf(rbf(var + 1e-5f)));
        const float r = __builtin_amdgcn_rcpf(bfhi(gran)), mr = -bflo(gran) * r;
        if ((lane & 7) == 0) st8_sc1(rws, (u32x2){gran, tg32}, 8 * g4 + 4 * (lane & 8), WS_ST + 64 * tb);
        u32 n[8][4];
#pragma unroll
        for (int i = 0; i < 4; ++i) {
          if (i == 2) bar();
          const int l0 = 16 * (2 * i & 3) + 8 * (i >> 1);
          const u32 ra = __builtin_amdgcn_readlane(__builtin_bit_cast(u32, r), l0), rb = __builtin_amdgcn_readlane(__builtin_bit_cast(u32, r), l0 + 16);
          u32 ma_ = __builtin_amdgcn_readlane(__builtin_bit_cast(u32, mr), l0), mb_ = __builtin_amdgcn_readlane(__builtin_bit_cast(u32, mr), l0 + 16);
          asm volatile("s_nop 1" : "+v"(ma_), "+v"(mb_) : "s"(ra), "s"(rb));
#pragma unroll
          for (int e = 0; e < 8; ++e)
            asm("v_fma_f32 %1, %1, %3, %5\n\tv_fma_f32 %2, %2, %4, %6\n\tv_cvt_pk_bf16_f32 %0, %1, %2"
                : "=v"(n[e][i]), "+v"(x[2 * i][e]), "+v"(x[2 * i + 1][e]) : "s"(ra), "s"(rb), "v"(ma_), "v"(mb_));
        }
#pragma unroll
        for (int e = 0; e < 8; ++e) st16_sc1(rws, (u32x4){n[e][0], n[e][1], n[e][2], n[e][3]}, WS_U + tb * 8192 + lane * 16 + 1024 * e);
        SCHED(); __builtin_amdgcn_s_barrier();
        WAIT_V(0);
        if (lane == 0) st8_sc1(rws, tag, FL_1 + tb * 8, 0);
        STAMP_AT(2);
        __builtin_amdgcn_s_barrier();
      } else {
        LOAD_EPOCH();
#pragma unroll 1
        for (int i = 0; i < 4; ++i) pull(i, o - 16 * (i >> 1));
      }
      FENCE(); SCHED();
      {
        float kv[16];
#pragma unroll
        for (int g = 0; g < 4; ++g) {
          LAS char* s = sm + L_SCR + 8192 * k + 1024 * g + 16 * lane;
          const f32x4 a = *(LAS f32x4*)s, d = *(LAS f32x4*)(s + 4096);
#pragma unroll
          for (int e = 0; e < 4; ++e) asm("v_add_f32 %0, %1, %2" : "=v"(kv[4 * g + e]) : "v"(a[e]), "v"(d[e]));
        }
        const bool odd = lane & 1;
        kc[0][0] = (u32x4){pk(kv[0], kv[1]), pk(kv[2], kv[3]), pk(kv[4], kv[5]), pk(kv[6], kv[7])};
        kc[1][0] = (u32x4){pk(kv[8], kv[8]), pk(kv[8], kv[9]), pk(kv[9], kv[9]), pk(kv[10], kv[10])};
        kc[2][0] = (u32x4){pk(kv[11], kv[11]), pk(kv[11], kv[11]), pk(odd ? kv[12] : kv[11], kv[12]), pk(kv[12], kv[12])};
        kc[3][0] = (u32x4){pk(kv[13], kv[13]), pk(kv[13], kv[13]), pk(odd ? kv[14] : kv[13], kv[14]), pk(kv[14], kv[14])};
#pragma unroll
        for (int g = 0; g < 4; ++g) {
          const u32x4 cu = kc[g][0];
          int px = 0, py = 0;
          if (g) {
            px = __builtin_amdgcn_update_dpp(0, (int)kc[g ? g - 1 : 0][0].z, 0x13C, 0xf, 0xf, false);
            py = __builtin_amdgcn_update_dpp(0, (int)kc[g ? g - 1 : 0][0].w, 0x13C, 0xf, 0xf, false);
          }
          px = __builtin_amdgcn_update_dpp(px, (int)cu.z, 0x138, 0xf, 0xf, false);
          py = __builtin_amdgcn_update_dpp(py, (int)cu.w, 0x138, 0xf, 0xf, false);
          const u32 s0 = __builtin_amdgcn_alignbit(py, px, 16), s1 = __builtin_amdgcn_alignbit(cu.x, py, 16), s2 = __builtin_amdgcn_alignbit(cu.y, cu.x, 16),
                    s3 = __builtin_amdgcn_alignbit(cu.z, cu.y, 16), s4 = __builtin_amdgcn_alignbit(cu.w, cu.z, 16);
          kc[g][1] = (u32x4){s1, s2, s3, s4}; kc[g][2] = (u32x4){(u32)py, cu.x, cu.y, cu.z}; kc[g][3] = (u32x4){s0, s1, s2, s3};
          asm volatile("" : "+v"(kc[g][1]), "+v"(kc[g][2]), "+v"(kc[g][3]));
        }
      }
      STAMP_AT(3);
#pragma unroll 1
      for (int i = 0; i < 2; ++i) pull(4 + 2 * k + i, o);
      bar();
      if (aux) { post(sm + S_HP + 4 * v, 4); await(sy + S_HP, peek(sy + S_HP), 4); }
#define AUXSLEEP 1
      if (aux) for (int i = 0; i < AUXSLEEP; ++i) __builtin_amdgcn_s_sleep(16);
      FENCE();
      f1 = ld16_sc1(rws, vf1, 0);
    } else if (aux) __builtin_amdgcn_s_barrier();
    MAKE_TAG();
    const u32 dq = smb + 4096 * v;
    const u32 vd0 = (lane >> 3) * (D * 2) + (((lane & 7) ^ ((lane >> 2) & 4)) << 4), vd1 = vd0 + 7168, vd2 = vd0 + 14336, vd3 = vd0 + 21504;
    auto ks_of = [&](int q) { int k = q + rot2; k = k >= 14 ? k - 14 : k; return 28 * v + 2 * k; };
    auto wdq = [&](int q, int slot) { dma4(rwd, rwd, dq + slot * UNIT, vd0, vd1, vd2, vd3, ks_of(q) * (16 * D * 2) + n2 * 128); };
    const u32 wo0 = 4096 * v + (4 * hh + q4) * 128 + p4 * 8 + ((blk ^ (q4 & 2)) << 5), wo1 = wo0 ^ 64;
    auto wfrag = [&](int slot, int e, int ct) { LAS char* a = sm + slot * UNIT + 2048 * e + (ct ? wo1 : wo0); return trfrag(a, a + 1024); };
    bf16x8 wr_[14][2];
    auto take = [&](int q, int slot) {
#pragma unroll
      for (int e = 0; e < 4; ++e) wr_[2 * q + (e >> 1)][e & 1] = wfrag(slot, e >> 1, e & 1);
    };
    wdq(0, 0); wdq(1, 1);
    WAIT_V(8);
    STAMP_AT(4);
    if (!aux) __builtin_amdgcn_s_barrier();
    if (v >= 2) {
#pragma unroll 1
      for (u32 s = 0; s < SPIN_CAP && !__all(f1.x == tlo && f1.y == thi && f1.z == tlo && f1.w == thi); ++s) {
        __builtin_amdgcn_s_sleep(16);
        FENCE();
        f1 = ld16_sc1(rws, vf1, 0);
      }
      FENCE();
#pragma unroll
      for (int e = 0; e < 4; ++e) uu[e] = ld16_sc1(rws, (lane >> 1) * 8192 + (lane & 1) * 16, WS_U + (k * 128 + 32 * e) * 8192 + tb * 32);
    }
    STAMP_AT(5);
    wdq(2, 2); wdq(3, 3); wdq(4, 4);
    WAIT_V(12); take(0, 0); take(1, 1); WAIT_L(); SCHED();
    wdq(5, 0); wdq(6, 1);
    WAIT_V(8); take(2, 2); take(3, 3); take(4, 4);
    WAIT_V(0);
    take(5, 0); take(6, 1);
    STAMP_AT(6);
    if (!aux) await(sy + S_FRE, peek(sy + S_FRE), 8);
    STAMP_AT(7);
    if (v >= 2) {
      LAS char* kd = sm + L_KS + KSS * k + 16 * lane, *k0 = sm + L_K0 + K0S * k + 16 * lane;
      LAS char* ud = sm + L_U + US * (lane & 1) + 512 + 2048 * k + 16 * (lane >> 1);
#pragma unroll
      for (int g = 0; g < 4; ++g) {
        *(LAS u32x4*)(k0 + 32 + 1024 * g) = kc[g][0];
#pragma unroll
        for (int e = 0; e < 3; ++e) *(LAS u32x4*)(kd + 32 + KCP * e + 1024 * g) = kc[g][e + 1];
        *(LAS u32x4*)(ud + 512 * g) = uu[g];
      }
      if (k == 0) *(LAS u32x4*)(ud - 512) = (u32x4)(0u);
      if (lane < 2) { *(LAS u32x4*)k0 = (u32x4)(0u); *(LAS u32x4*)kd = (u32x4)(0u); *(LAS u32x4*)(kd + KCP) = (u32x4)(0u); *(LAS u32x4*)(kd + 2 * KCP) = (u32x4)(0u); }
      post(sm + S_HP + 4 * v, 5);
    }
    await(sy + S_HP, peek(sy + S_HP), 5);
    WAIT_L(); SCHED();
    STAMP_AT(8);
    if (b == 255 && tid == 0) st8_sc1(rws, tag, FL_E, 0);
    wdq(7, 0); wdq(8, 1); wdq(9, 2); wdq(10, 3); wdq(11, 4); wdq(12, 5); wdq(13, 6);
    xres = __builtin_amdgcn_raw_buffer_load_b128(rx, vo, 0, 0);
    STAMP_AT(9);
    wait_flags<8>(rws, FL_2 + (ti2 * 28 + 7 * v + (lane < 7 ? lane : 0)) * FS, tlo, thi);
    STAMP_AT(10);
    f32x16 acc[2][2];
#pragma unroll
    for (int a = 0; a < 4; ++a) acc[a >> 1][a & 1] = (f32x16)(0.f);
    constexpr int PD = 4;
    u32x4 hf[28][2];
    auto hload = [&](int i) {
      const u32 so = (ti2 * 112 + ks_of(i >> 1)) * 2048;
#define HAUX 16
      hf[i][0] = __builtin_amdgcn_raw_buffer_load_b128(rws, lane * 16 + 2048 * (i & 1), so, HAUX);
      hf[i][1] = __builtin_amdgcn_raw_buffer_load_b128(rws, lane * 16 + 2048 * (i & 1) + 1024, so, HAUX);
    };
#pragma unroll
    for (int i = 0; i < PD; ++i) hload(i);
    bf16x8 wl[2][2];
    SCHED();
#pragma unroll
    for (int i = 0; i < 28; ++i) {
      if (i + 1 >= 14 && i + 1 < 28) { wl[(i + 1) & 1][0] = wfrag((i - 13) >> 1, (i + 1) & 1, 0); wl[(i + 1) & 1][1] = wfrag((i - 13) >> 1, (i + 1) & 1, 1); }
      const bf16x8 a0 = i < 14 ? wr_[i < 14 ? i : 0][0] : wl[i & 1][0], a1 = i < 14 ? wr_[i < 14 ? i : 0][1] : wl[i & 1][1];
      const bf16x8 b0 = __builtin_bit_cast(bf16x8, hf[i][0]), b1 = __builtin_bit_cast(bf16x8, hf[i][1]);
      acc[0][0] = MFMA32(a0, b0, acc[0][0]);
      acc[1][0] = MFMA32(a1, b0, acc[1][0]);
      acc[0][1] = MFMA32(a0, b1, acc[0][1]);
      acc[1][1] = MFMA32(a1, b1, acc[1][1]);
      if (i == 6) STAMP_AT(15);
      if (i == 13) STAMP_AT(16);
      if (i == 20) STAMP_AT(17);
      SCHED();
      if (i < 4) { hload(PD + 2 * i); hload(PD + 2 * i + 1); }
      else if (i + PD + 4 < 28) hload(i + PD + 4);
      SCHED();
    }
    STAMP_AT(11);
#pragma unroll
    for (int g = 0; g < 4; ++g) {
      LAS char* t = sm + 4096 * v + (lane & 31) * 128 + (((2 * g + hh) ^ (lane & 7)) << 4);
#pragma unroll
      for (int a = 0; a < 4; ++a) {
        const f32x16& s = acc[a >> 1][a & 1];
        *(LAS f32x4*)(t + UNIT * a) = (f32x4){s[4 * g], s[4 * g + 1], s[4 * g + 2], s[4 * g + 3]};
      }
    }
    STAMP_AT(12);
  } else {
    __builtin_amdgcn_s_setprio(1);
    const int c = v >> 1, ah = v & 1, ch = ch0 + 8 * c;
    const int gi = ch * NM + 64 * ah + lane;
    const u32 r6 = p.logdt[ch], r0 = p.are[gi], r1 = p.aim[gi], r2 = p.bre[gi], r3 = p.bim[gi], r4 = p.cre[gi], r5 = p.cim[gi];
    SCHED();
    f32x16 acc[2][4];
#pragma unroll
    for (int a = 0; a < 8; ++a) { acc[a >> 2][a & 3] = (f32x16)(0.f); asm volatile("" : "+v"(acc[a >> 2][a & 3])); }
    LOAD_EPOCH();
    MAKE_TAG();
    SCHED();
    if (aux) {
      const int job = 4 * ma + (o - 28), mat = job >= 7, cg = 7 * fh + job - 7 * mat;
      float s[8];
#pragma unroll
      for (int e = 0; e < 8; ++e) s[e] = 0.f;
      if (job < 14) {
        const rsrc_t rw = __builtin_amdgcn_make_buffer_rsrc((void*)(mat ? p.wu : p.wg), 0, D * F * 2, 0x00020000);
        u32 k0 = 0x00003f80u, k1 = 0x3f800000u;
        asm("" : "+v"(k0), "+v"(k1));
        const bf16x2 s0 = __builtin_bit_cast(bf16x2, k0), s1 = __builtin_bit_cast(bf16x2, k1);
        u32x4 t[8];
        const u32 vw = g4 * (F * 2) + (lane & 15) * 16;
        u32 so = 128 * v * (F * 2) + cg * 256;
#pragma unroll
        for (int i = 0; i < 8; ++i) t[i] = __builtin_amdgcn_raw_buffer_load_b128(rw, vw, so + 4 * i * (F * 2), 0);
#pragma unroll 1
        for (int j = 0; j < 4; ++j) {
          so = j < 3 ? so + 32 * (F * 2) : 0x40000000u;
#pragma unroll
          for (int i = 0; i < 8; ++i) {
#pragma unroll
            for (int e = 0; e < 4; ++e) {
              const u32 d = t[i][e];
              s[2 * e] = __builtin_amdgcn_fdot2_f32_bf16(__builtin_bit_cast(bf16x2, d), s0, s[2 * e], false);
              s[2 * e + 1] = __builtin_amdgcn_fdot2_f32_bf16(__builtin_bit_cast(bf16x2, d), s1, s[2 * e + 1], false);
            }
            t[i] = __builtin_amdgcn_raw_buffer_load_b128(rw, vw, so + 4 * i * (F * 2), 0);
            SCHED();
          }
        }
      }
      const float a0 = fold16(fold32(s[0], s[4]), fold32(s[2], s[6])), a1 = fold16(fold32(s[1], s[5]), fold32(s[3], s[7]));
      *(LAS f32x2*)(sm + L_CS + 512 * v + 32 * (lane & 15) + 8 * g4) = (f32x2){a0, a1};
      BARRIER();
      LAS char* cx = sm + L_CS + 128 * v + 4 * (lane & 31);
      const float tot = (*(LAS float*)cx + *(LAS float*)(cx + 512)) + (*(LAS float*)(cx + 1024) + *(LAS float*)(cx + 1536)), th = rbf(tot);
      if (job < 14 && lane < 32) st8_sc1(rws, (u32x2){pk(th, tot - th), tg32}, lane * 8, WS_CS + (mat * F + cg * 128 + 32 * v) * 8);
      STAMP_AT(15);
    }
    {
      const float dt = rbf(__builtin_amdgcn_exp2f(bflo(r6) * 1.44269504088896f));
      const float ar2 = dt * bflo(r0) * 1.44269504088896f, air = dt * bflo(r1) * 0.159154943091895f;
      LAS char* const scr = sm + 5 * UNIT + 4096 * v;
      LAS char* const wr = scr + lane * 64;
      const int sw = (lane >> 1) & 3, cki = 2 * blk + (p4 >> 1);
      LAS char* const rd0 = scr + (8 * hh + q4) * 64 + ((cki ^ (q4 >> 1)) << 4) + 8 * (p4 & 1);
      LAS char* const rd1 = scr + (8 * hh + 4 + q4) * 64 + ((cki ^ (2 + (q4 >> 1))) << 4) + 8 * (p4 & 1);
      auto put = [&](int img, int g, u32x4 re, u32x4 im) {
        LAS char* d = wr + ((g ^ sw) << 4) + 2 * img * UNIT;
        *(LAS u32x4*)d = re; *(LAS u32x4*)(d + UNIT) = im;
        SCHED();
      };
      auto get = [&](int img, int s) { const u32 d = (2 * img + (s >> 2)) * UNIT + 1024 * (s & 3); return trfrag(rd0 + d, rd1 + d); };
#define P2_(a) pk((a).x, (a).y)
#define P4_(a, b, c, d) (u32x4){P2_(a), P2_(b), P2_(c), P2_(d)}
      STAMP_AT(16);
      const cpx e8 = cexpl(ar2, air, 8.f);
      bf16x8 yf[8], xf[8];
      {
        const cpx s1 = {e8.r, -e8.i}, s2 = csq(s1), s4 = csq(s2), s8 = cexpl(ar2, -air, 64.f), s16 = csq(s8);
        const cp2 y0 = {{1.f, s1.r}, {0.f, s1.i}}, y1 = cmul2(y0, s2), y2 = cmul2(y0, s4), y3 = cmul2(y1, s4);
        put(0, 0, P4_(y0.r, y1.r, y2.r, y3.r), P4_(y0.i, y1.i, y2.i, y3.i));
        const cp2 y4 = cmul2(y0, s8), y5 = cmul2(y1, s8), y6 = cmul2(y2, s8), y7 = cmul2(y3, s8);
        put(0, 1, P4_(y4.r, y5.r, y6.r, y7.r), P4_(y4.i, y5.i, y6.i, y7.i));
        const cp2 y8 = cmul2(y0, s16), y9 = cmul2(y1, s16), ya = cmul2(y2, s16), yb = cmul2(y3, s16);
        put(0, 2, P4_(y8.r, y9.r, ya.r, yb.r), P4_(y8.i, y9.i, ya.i, yb.i));
        const cp2 yc = cmul2(y4, s16), yd = cmul2(y5, s16), ye = cmul2(y6, s16), yg = cmul2(y7, s16);
        put(0, 3, P4_(yc.r, yd.r, ye.r, yg.r), P4_(yc.i, yd.i, ye.i, yg.i));
      }
      {
        const cpx cf = cmul({bflo(r4), bflo(r5)}, {dt * bflo(r2), dt * bflo(r3)});
        const cpx e1 = cexpl(ar2, air, 1.f), e256 = cexpl(ar2, air, 256.f);
        const cpx e2 = csq(e1), e4 = csq(e2), e512 = csq(e256), e1024 = csq(e512);
        const cp2 cb = cmul2({{e1.r, e256.r}, {e1.i, e256.i}}, cf);
        const cp2 a0 = {{cf.r, cb.r.x}, {cf.i, cb.i.x}}, a1 = cmul2(a0, e2);
        const cp2 t0 = cmul2({{e2.r, e4.r}, {e2.i, e4.i}}, {cb.r.y, cb.i.y});
#define XA(m) (u32x4){P2_(a0.m), P2_(a1.m), pk(cb.m.y, cb.m.y), P2_(t0.m)}
        put(1, 0, XA(r), XA(i));
#pragma unroll
        for (int s = 0; s < 4; ++s) yf[s] = get(0, s);
        const cp2 a2 = cmul2(a0, e4), a3 = cmul2(a1, e4), t1 = cmul2(t0, e4);
#define XB(m) (u32x4){P2_(a2.m), P2_(a3.m), pk(t0.m.y, t0.m.y), P2_(t1.m)}
        put(1, 1, XB(r), XB(i));
#pragma unroll
        for (int s = 4; s < 8; ++s) yf[s] = get(0, s);
        const cp2 q0 = {{cf.r, cb.r.y}, {cf.i, cb.i.y}}, q1 = cmul2(q0, e512), q2 = cmul2(q0, e1024);
        const cp2 u4 = cmul2(q1, e4), u8 = cmul2(q1, e8);
#define XC(m) (u32x4){pk(q1.m.x, u4.m.x), pk(u8.m.x, q2.m.x), pk(q1.m.y, u4.m.y), pk(u8.m.y, q2.m.y)}
        put(1, 2, XC(r), XC(i));
        const cp2 q3 = cmul2(q1, e1024), v8 = cmul2(q2, e8), w8 = cmul2(q3, e8);
#define XD(m) (u32x4){pk(v8.m.x, q3.m.x), pk(w8.m.x, 0.f), pk(v8.m.y, q3.m.y), pk(w8.m.y, 0.f)}
        put(1, 3, XD(r), XD(i));
#pragma unroll
        for (int s = 0; s < 8; ++s) xf[s] = get(1, s);
      }
      f32x16 kacc = (f32x16)(0.f);
#pragma unroll
      for (int s = 0; s < 8; ++s) kacc = MFMA32(xf[s], yf[s], kacc);
#pragma unroll
      for (int g = 0; g < 4; ++g) *(LAS f32x4*)(scr + 3 * UNIT + 1024 * g + 16 * lane) = (f32x4){kacc[4 * g], kacc[4 * g + 1], kacc[4 * g + 2], kacc[4 * g + 3]};
      STAMP_AT(17);
    }
    if (aux) BARRIER();
    else {
      const int tg = v >> 1, fq = v & 1;
      u32 xa[4], wa[4];
      {
        const u32 row = 64 * tg + (lane & 31), f = (row >> 1) & 7;
#pragma unroll
        for (int s = 0; s < 4; ++s) {
          xa[s] = row * 128 + (((2 * s + hh) ^ f) << 4);
          wa[s] = hh * 4096 + blk * 2048 + q4 * 256 + (((4 * fq + s) ^ (q4 + 4 * blk)) << 5) + p4 * 8;
        }
      }
      bf16x8 X0[2], W0[4], X1[2], W1[4];
      X1[0] = X1[1] = W1[0] = W1[1] = W1[2] = W1[3] = __builtin_bit_cast(bf16x8, (u32x4)(0u));
      auto step = [&](const bf16x8 (&Xc)[2], const bf16x8 (&Wc)[4], bf16x8 (&Xn)[2], bf16x8 (&Wn)[4], u32 xb, u32 wb, int s, auto mid) {
        LAS char* xp = sm + (xb + xa[s]);
        auto wld = [&](int t) { LAS char* a = sm + (wb + wa[t]) + 8192 * (s & 1); Wn[t] = trfrag(a, a + 1024); };
        acc[0][0] = MFMA32(Wc[0], Xc[0], acc[0][0]); SCHED();
        mid(); wld(0); SCHED();
        acc[1][0] = MFMA32(Wc[0], Xc[1], acc[1][0]); SCHED();
        Xn[0] = ldfrag(xp); Xn[1] = ldfrag(xp + 4096); SCHED();
        acc[0][1] = MFMA32(Wc[1], Xc[0], acc[0][1]); SCHED();
        wld(1); SCHED();
        acc[1][1] = MFMA32(Wc[1], Xc[1], acc[1][1]); SCHED();
        wld(2); SCHED();
        acc[0][2] = MFMA32(Wc[2], Xc[0], acc[0][2]); SCHED();
        wld(3); SCHED();
        acc[1][2] = MFMA32(Wc[2], Xc[1], acc[1][2]); SCHED();
        acc[0][3] = MFMA32(Wc[3], Xc[0], acc[0][3]); SCHED();
        acc[1][3] = MFMA32(Wc[3], Xc[1], acc[1][3]); SCHED();
      };
      u32 ub = 0;
#pragma unroll 1
      for (int cc = 0; cc < 8; ++cc) {
        const u32 xb = ub, w0 = (ub + UNIT) & (FREE - 1), w1 = (ub + 2 * UNIT) & (FREE - 1);
        ub = (ub + 3 * UNIT) & (FREE - 1);
        step(X1, W1, X0, W0, xb, w0, 0, [&] { BARRIER(); });
        step(X0, W0, X1, W1, xb, w0, 1, [&] {});
        step(X1, W1, X0, W0, xb, w1, 2, [&] {});
        step(X0, W0, X1, W1, xb, w1, 3, [&] {});
      }
      STAMP_AT(2);
      u32x2 st[2], cs[4];
      const u32 ost = 8 * (64 * tg + (lane & 31)), ocs = 8 * (64 * fq + (lane & 15));
#pragma unroll
      for (int t = 0; t < 2; ++t) st[t] = *(LAS u32x2*)(sm + L_ST + ost + 256 * t);
#pragma unroll
      for (int t = 0; t < 4; ++t) cs[t] = *(LAS u32x2*)(sm + L_CS + 1024 * blk + ocs + 128 * t);
      post(sm + S_FRE + 4 * v, 8);
#pragma unroll
      for (int a = 0; a < 8; ++a) acc[a & 1][a >> 1] = MFMA32(W1[a >> 1], X1[a & 1], acc[a & 1][a >> 1]);
#pragma unroll 1
      for (u32 s = 0; s < SPIN_CAP && __builtin_expect(!__all(st[0].y == tg32 && st[1].y == tg32 && cs[0].y == tg32 && cs[1].y == tg32 && cs[2].y == tg32 && cs[3].y == tg32), 0); ++s) {
        FENCE();
#pragma unroll
        for (int t = 0; t < 2; ++t) st[t] = ld8_sc1(rws, ost + 256 * t, WS_ST + mi * 1024);
#pragma unroll
        for (int t = 0; t < 4; ++t) cs[t] = ld8_sc1(rws, blk * F * 8 + ocs + 128 * t, WS_CS + fj * 1024);
        __builtin_amdgcn_s_sleep(8);
      }
      bf16x8 XM[2];
#pragma unroll
      for (int t = 0; t < 2; ++t) { const u32 m = hh ? 0u : (st[t].x & 0xffffu) ^ 0x8000u; XM[t] = __builtin_bit_cast(bf16x8, (u32x4){m | m << 16, 0, 0, 0}); }
#pragma unroll
      for (int a = 0; a < 8; ++a) acc[a & 1][a >> 1] = MFMA32(__builtin_bit_cast(bf16x8, (u32x4){cs[a >> 1].x, 0, 0, 0}), XM[a & 1], acc[a & 1][a >> 1]);
      const u32 vh = ((2 * mi + tg) * 112 + 8 * fj + 4 * fq) * 2048 + lane * 16;
      float ka[2], kb[2], kr[2];
#pragma unroll
      for (int t = 0; t < 2; ++t) {
        const float sd = bfhi(st[t].x), r = __builtin_amdgcn_rcpf(sd);
        kb[t] = -2.f * 0.7978845608028654f * 1.44269504088896f * r; ka[t] = kb[t] * 0.044715f * r * r; kr[t] = sd * sd;
      }
#pragma unroll
      for (int a = 0; a < 8; ++a) {
        const int tt = a & 1, ft = a >> 1;
        u32x4 hv;
#pragma unroll
        for (int e = 0; e < 4; ++e) {
          const f32x2 g = {acc[tt][ft][2 * e], acc[tt][ft][2 * e + 1]}, u = {acc[tt][ft][8 + 2 * e], acc[tt][ft][9 + 2 * e]};
          const f32x2 z = (g * g * ka[tt] + kb[tt]) * g;
          const f32x2 ex = {__builtin_amdgcn_exp2f(z.x), __builtin_amdgcn_exp2f(z.y)};
          const f32x2 dn = ex * kr[tt] + kr[tt];
          const f32x2 rc = {__builtin_amdgcn_rcpf(dn.x), __builtin_amdgcn_rcpf(dn.y)};
          const f32x2 hq = g * u * rc;
          hv[e] = pk(hq.x, hq.y);
        }
        st16_sc1(rws, hv, vh + 1024 * a);
        SCHED();
      }
      STAMP_AT(3);
    }
    LAS char* ub = sm + L_U + US * c;
    LAS char* ka = sm + (q4 ? L_KS + KSS * c + KCP * (q4 - 1) : L_K0 + K0S * c) + 2 * (28 + 128 * ah + 4 * p4 - 8 * g4);
    LAS char* ua = ub + 2 * (240 - 96 - 128 * ah + 16 * (lane & 15) + 8 * g4);
    f32x4 y[8];
#pragma unroll
    for (int i = 0; i < 8; ++i) y[i] = (f32x4)(0.f);
    await(sy + S_HP, peek(sy + S_HP), 5);
    STAMP_AT(4);
    bf16x8 A[2][8], B[2][8];
    auto cld = [&](int a, int i) {
      A[a & 1][i] = trfrag(ka + 64 * a + 512 * i + 8, ka + 64 * a + 512 * i);
      B[a & 1][i] = ldfrag(ua + 64 * (3 - a) + 512 * i);
    };
#pragma unroll
    for (int i = 0; i < 8; ++i) cld(0, i);
#pragma unroll
    for (int a = 0; a < 4; ++a) {
#pragma unroll
      for (int s = 0; s < 8; ++s) {
#pragma unroll
        for (int i = 0; i <= s; ++i) {
          if (i + s < 8) y[i + s] = MFMA16(A[a & 1][i], B[a & 1][s], y[i + s]);
          if (i < s && i + s < 8) y[i + s] = MFMA16(A[a & 1][s], B[a & 1][i], y[i + s]);
        }
        SCHED();
        if (a < 3) cld(a + 1, s);
        SCHED();
      }
#define FLAG_AT 0
      if (a == FLAG_AT) {
        STAMP_AT(5);
        WAIT_V(0);
        if (!aux && lane == 0) st8_sc1(rws, tag, FL_2 + ((2 * mi + c) * 28 + 2 * fj + ah) * FS, 0);
        xres = __builtin_amdgcn_raw_buffer_load_b128(rx, vo, 0, 0);
        STAMP_AT(6);
      }
    }
    const float dsk = bflo(p.dskip[ch]);
    STAMP_AT(7);
    LAS char* const pw = sm + S_GP + 4 * (v ^ 1);
    LAS char* ex = sm + L_KS + KSS * c;
    post(sm + S_GP + 4 * v, 1);
    await(pw, peek(pw), 1);
#pragma unroll
    for (int i = 0; i < 4; ++i) {
      const f32x4 lo = y[i], hi = y[4 + i];
      *(LAS f32x4*)(ex + 4096 * ah + 1024 * i + 16 * lane) = ah ? lo : hi;
      y[i] = ah ? hi : lo;
    }
    post(sm + S_GP + 4 * v, 2);
    await(pw, peek(pw), 2);
    STAMP_AT(8);
    const u32 tz = 2 * (1024 * ah + 16 * (lane & 15) + 4 * g4);
#pragma unroll
    for (int i = 0; i < 4; ++i) {
      const f32x4 t = *(LAS f32x4*)(ex + 4096 * (ah ^ 1) + 1024 * i + 16 * lane) + y[i];
      const u32x2 u = *(LAS u32x2*)(ub + 512 + tz + 512 * i);
      const u32x2 z = {pk(t[0] + bflo(u.x) * dsk, t[1] + bfhi(u.x) * dsk), pk(t[2] + bflo(u.y) * dsk, t[3] + bfhi(u.y) * dsk)};
      st8_sc1(rws, z, tz + 512 * i, WS_ZT + ch * 4096);
    }
    STAMP_AT(9);
    WAIT_V(0);
    if (lane == 0) st8_sc1(rws, tag, FL_3 + (ah * 512 + ch) * FS, 0);
    STAMP_AT(10);
#define ZSLEEP 8
    __builtin_amdgcn_s_sleep(ZSLEEP);
    wait_flags<8>(rws, FL_3 + ((ti2 >> 4) * 512 + 64 * n2 + 16 * v + (lane & 15)) * FS, tlo, thi);
    STAMP_AT(11);
    const u32 vz = (64 * n2 + 16 * v + (lane >> 3)) * 4096 + 128 * ti2 + (lane & 7) * 16;
    const u32x4 z0 = ld16_sc1(rws, vz, WS_ZT), z1 = ld16_sc1(rws, vz, WS_ZT + 8 * 4096);
    LAS char* stg = sm + L_K0 + K0S * c + 2048 * ah;
    *(LAS u32x4*)(stg + 16 * lane) = z0;
    *(LAS u32x4*)(stg + 1024 + 16 * lane) = z1;
    LAS char* tr = stg + q4 * 128 + 32 * g4 + 8 * p4;
    const bf16x8 ra = trfrag(tr, tr + 512), rc = trfrag(tr + 1024, tr + 1536);
    LAS char* rb = ex + 8192 + 2048 * ah + 32 * lane;
    *(LAS bf16x8*)rb = ra;
    *(LAS bf16x8*)(rb + 16) = rc;
    STAMP_AT(12);
  }
  {
    const int tok = tid >> 3, dc = tid & 7;
    const u32 to = UNIT * (2 * (dc >> 2) + (tok >> 5)) + (tok & 31) * 128 + (((2 * (dc & 3)) ^ (tok & 7)) << 4);
    LAS char *t0 = sm + to, *t1 = sm + (to ^ 16u);
    LAS char* zp = sm + L_KS + KSS * (dc >> 2) + 8192 + 2048 * ((dc >> 1) & 1) + 32 * tok + 16 * (dc & 1);
    BARRIER();
    STAMP_AT(13);
    const u32x4 z = *(LAS u32x4*)zp;
    f32x4 s0 = {bflo(xres[0]), bfhi(xres[0]), bflo(xres[1]), bfhi(xres[1])}, s1 = {bflo(xres[2]), bfhi(xres[2]), bflo(xres[3]), bfhi(xres[3])};
    s0 += (f32x4){bflo(z[0]), bfhi(z[0]), bflo(z[1]), bfhi(z[1])};
    s1 += (f32x4){bflo(z[2]), bfhi(z[2]), bflo(z[3]), bfhi(z[3])};
#pragma unroll
    for (int g = 0; g < 4; ++g) { s0 += *(LAS f32x4*)(t0 + 4096 * g); s1 += *(LAS f32x4*)(t1 + 4096 * g); }
    *(u32x4*)((char*)p.out + vo) = (u32x4){pk(s0[0], s0[1]), pk(s0[2], s0[3]), pk(s1[0], s1[1]), pk(s1[2], s1[3])};
  }
  STAMP_AT(14); STAMP_OUT();
}

extern "C" void kernel_launch(void* const* d_in, const int* in_sizes, int n_in, void* d_out, int out_size, void* d_ws,
                              size_t ws_size, hipStream_t stream) {
  Params p;
  p.x = (const u16*)d_in[0]; p.are = (const u16*)d_in[1]; p.aim = (const u16*)d_in[2];
  p.bre = (const u16*)d_in[3]; p.bim = (const u16*)d_in[4]; p.cre = (const u16*)d_in[5];
  p.cim = (const u16*)d_in[6]; p.logdt = (const u16*)d_in[7]; p.dskip = (const u16*)d_in[8];
  p.wg = (const u16*)d_in[10]; p.wu = (const u16*)d_in[11]; p.wd = (const u16*)d_in[12];
  p.out = (u16*)d_out; p.ws = (char*)d_ws;
  s4ffn_kernel<<<256, 512, 0, stream>>>(p);
}
```

```cpp
#include <hip/hip_runtime.h>
#include <stdint.h>

typedef unsigned short u16;
typedef unsigned u32;
typedef unsigned long long u64;
typedef __bf16 bf16x2 __attribute__((ext_vector_type(2)));
typedef __bf16 bf16x4 __attribute__((ext_vector_type(4)));
typedef __bf16 bf16x8 __attribute__((ext_vector_type(8)));
typedef float f32x2 __attribute__((ext_vector_type(2)));
typedef float f32x4 __attribute__((ext_vector_type(4)));
typedef float f32x16 __attribute__((ext_vector_type(16)));
typedef u32 u32x2 __attribute__((ext_vector_type(2)));
typedef u32 u32x4 __attribute__((ext_vector_type(4)));
#define LAS __attribute__((address_space(3)))
#define GAS __attribute__((address_space(1)))
#define DI __device__ __forceinline__
typedef __amdgpu_buffer_rsrc_t rsrc_t;

constexpr int L = 2048, D = 512, NM = 128, F = 1792;
constexpr u32 WS_H = 0, WS_U = 8u << 20, WS_ZT = 10u << 20, WS_ST = 14u << 20, WS_CS = WS_ST + 65536, WS_FL = 15u << 20, WS_K = 16u << 20, WS_END = 24u << 20;
constexpr u32 FL_E = WS_FL, FL_1 = WS_FL + 4096, FL_2 = WS_FL + 131072, FS = 128, FL_2B = 131072;
constexpr u32 KCP = 4160, UNIT = 16384, RB = 32768, RING = 8 * UNIT;
constexpr u32 L_U = 8 * KCP;
constexpr u32 L_ST = L_U + 9216;
constexpr u32 L_CS = L_ST + 1024;
constexpr u32 S_FRE = 4128, S_HP = 4144, S_GP = KCP + 4128, S_H2 = KCP + 4144;
constexpr u32 SPIN_CAP = 1u << 16;

struct Params {
  const u16 *x, *are, *aim, *bre, *bim, *cre, *cim, *logdt, *dskip, *wg, *wu, *wd;
  u16* out;
  char* ws;
};

#define STAMP_DECL
#define STAMP_AT(k)
#define STAMP_RT(v)
#define STAMP_NW(k)
#define STAMP_NWEND()
#define STAMP_OUT()

DI float bflo(u32 v) { return __builtin_bit_cast(float, v << 16); }
DI float bfhi(u32 v) { return __builtin_bit_cast(float, v & 0xffff0000u); }
DI u32 pk(float a, float b) { f32x2 f = {a, b}; return __builtin_bit_cast(u32, __builtin_convertvector(f, bf16x2)); }
DI float rbf(float a) { return bflo(pk(a, a)); }
DI void dma4(rsrc_t ra, rsrc_t rb, u32 lds, u32 v0, u32 v1, u32 v2, u32 v3, u32 soff) {
  asm volatile("s_mov_b32 m0, %0\n\ts_nop 0\n\tbuffer_load_dwordx4 %1, %5, %7 offen lds\n\tbuffer_load_dwordx4 %2, %5, %7 offen offset:1024 lds\n\t"
               "buffer_load_dwordx4 %3, %6, %7 offen offset:2048 lds\n\tbuffer_load_dwordx4 %4, %6, %7 offen offset:3072 lds"
               :: "s"(lds), "v"(v0), "v"(v1), "v"(v2), "v"(v3), "s"(ra), "s"(rb), "s"(soff));
}
DI void dma4s(rsrc_t r, u32 lds, u32 v0, u32 v1, u32 v2, u32 v3, u32 soff) {
  asm volatile("s_mov_b32 m0, %0\n\ts_nop 0\n\tbuffer_load_dwordx4 %1, %5, %6 offen sc1 lds\n\tbuffer_load_dwordx4 %2, %5, %6 offen offset:1024 sc1 lds\n\t"
               "buffer_load_dwordx4 %3, %5, %6 offen offset:2048 sc1 lds\n\tbuffer_load_dwordx4 %4, %5, %6 offen offset:3072 sc1 lds"
               :: "s"(lds), "v"(v0), "v"(v1), "v"(v2), "v"(v3), "s"(r), "s"(soff));
}
DI void dma1s(rsrc_t r, u32 lds, u32 v0, u32 soff) {
  asm volatile("s_mov_b32 m0, %0\n\ts_nop 0\n\tbuffer_load_dwordx4 %1, %2, %3 offen sc1 lds" :: "s"(lds), "v"(v0), "s"(r), "s"(soff));
}
#define WAIT_V(n) asm volatile("s_waitcnt vmcnt(" #n ")" ::: "memory")
#define WAIT_L() asm volatile("s_waitcnt lgkmcnt(0)" ::: "memory")
#define BARRIER() asm volatile("s_waitcnt lgkmcnt(0)\n\ts_barrier" ::: "memory")
#define FENCE() asm volatile("" ::: "memory")
#define SCHED() __builtin_amdgcn_sched_barrier(0)
DI bf16x8 ldfrag(LAS char* a) { return *(LAS bf16x8*)a; }
DI bf16x8 trfrag(LAS char* a0, LAS char* a1) {
  bf16x4 lo = __builtin_amdgcn_ds_read_tr16_b64_v4bf16((LAS bf16x4*)a0);
  bf16x4 hi = __builtin_amdgcn_ds_read_tr16_b64_v4bf16((LAS bf16x4*)a1);
  return __builtin_shufflevector(lo, hi, 0, 1, 2, 3, 4, 5, 6, 7);
}
#define MFMA32(a, b, c) __builtin_amdgcn_mfma_f32_32x32x16_bf16(a, b, c, 0, 0, 0)
#define MFMA16(a, b, c) __builtin_amdgcn_mfma_f32_16x16x32_bf16(a, b, c, 0, 0, 0)
DI u32x2 ld8_sc1(rsrc_t r, u32 voff, u32 soff) { return __builtin_amdgcn_raw_buffer_load_b64(r, voff, soff, 16); }
DI u32x4 ld16_sc1(rsrc_t r, u32 voff, u32 soff) { return __builtin_amdgcn_raw_buffer_load_b128(r, voff, soff, 16); }
DI void st8_sc1(rsrc_t r, u32x2 v, u32 voff, u32 soff) { __builtin_amdgcn_raw_buffer_store_b64(v, r, voff, soff, 16); }
DI void st16_sc1(rsrc_t r, u32x4 v, u32 voff) { __builtin_amdgcn_raw_buffer_store_b128(v, r, voff, 0, 16); }
template <int SLEEP> DI void wait_flags(rsrc_t r, u32 voff, u32 tlo, u32 thi) {
#pragma unroll 1
  for (u32 s = 0; s < SPIN_CAP; ++s) {
    FENCE();
    u32x2 f = ld8_sc1(r, voff, 0);
    if (__all(f.x == tlo && f.y == thi)) break;
    __builtin_amdgcn_s_sleep(SLEEP);
  }
  WAIT_V(0);
}
DI void post(LAS char* word, u32 v) {
  asm volatile("s_mov_b64 exec, 1\n\tds_write_b32 %0, %1\n\ts_mov_b64 exec, -1" :: "v"(word), "v"(v) : "memory");
}
DI u32 peek(LAS char* word) { return *(volatile LAS u32*)word; }
DI void await(LAS char* word, u32 seen, u32 need) {
  if (__builtin_expect(__any(seen < need), 0)) {
#pragma unroll 1
    for (u32 s = 0; s < SPIN_CAP * 16; ++s) { __builtin_amdgcn_s_sleep(1); if (!__any(peek(word) < need)) break; }
  }
  FENCE();
}
#define DPPADD(name, ctl) DI float name(float x) { float r; asm("s_nop 1\n\tv_add_f32_dpp %0, %1, %1 " ctl " row_mask:0xf bank_mask:0xf" : "=v"(r) : "v"(x)); return r; }
DPPADD(add_x1, "quad_perm:[1,0,3,2]") DPPADD(add_x2, "quad_perm:[2,3,0,1]") DPPADD(add_m8, "row_half_mirror") DPPADD(add_x8, "row_ror:8")
DI float fold32(float a, float b) {
  asm("s_nop 1\n\tv_permlane32_swap_b32 %0, %1\n\tv_add_f32 %0, %0, %1" : "+v"(a), "+v"(b)); return a;
}
DI float fold16(float a, float b) {
  asm("s_nop 1\n\tv_permlane16_swap_b32 %0, %1\n\tv_add_f32 %0, %0, %1" : "+v"(a), "+v"(b)); return a;
}
DI u32 vmov(u32 a) { u32 r; asm("v_mov_b32 %0, %1" : "=v"(r) : "v"(a)); return r; }
struct cpx { float r, i; };
struct cp2 { f32x2 r, i; };
DI cpx cmul(cpx a, cpx b) { return {a.r * b.r - a.i * b.i, a.r * b.i + a.i * b.r}; }
DI cpx csq(cpx a) { return {a.r * a.r - a.i * a.i, (a.r + a.r) * a.i}; }
DI cp2 cmul2(cp2 a, cpx b) { return {a.r * b.r - a.i * b.i, a.i * b.r + a.r * b.i}; }
DI cpx cexpl(float ar2, float air, float l) {
  float m = __builtin_amdgcn_exp2f(ar2 * l), f = __builtin_amdgcn_fractf(air * l);
  return {m * __builtin_amdgcn_cosf(f), m * __builtin_amdgcn_sinf(f)};
}

__global__ __launch_bounds__(512, 2) void s4ffn_kernel(Params p) {
  __shared__ __attribute__((aligned(16))) char smem[163840];
  LAS char* sm = (LAS char*)smem;
  const u32 smb = (u32)(size_t)sm;
  const int tid = threadIdx.x, lane = tid & 63;
  const int w = __builtin_amdgcn_readfirstlane(tid >> 6), v = w & 3;
  const int b = blockIdx.x, xcd = b & 7, o = b >> 3;
  const bool aux = o >= 28;
  const int ma = xcd >> 1, fh = xcd & 1;
  const int il = o / 7, jl = o - il * 7;
  const int mi = 4 * ma + il, fj = 7 * fh + jl;
  const int ti2 = 4 * xcd + (o >> 3), n2 = o & 7;
  const int tb = 32 * xcd + o, ch0 = 16 * o + xcd;
  const int hh = lane >> 5, blk = (lane >> 4) & 1, q4 = (lane >> 2) & 3, p4 = lane & 3, g4 = lane >> 4;
  STAMP_DECL; STAMP_RT(rt0_); STAMP_AT(0);
#define HOLD(id, ticks)
#define BURN(id)
  BURN(4)
  const rsrc_t rx = __builtin_amdgcn_make_buffer_rsrc((void*)p.x, 0, L * D * 2, 0x00020000);
  const rsrc_t rwg = __builtin_amdgcn_make_buffer_rsrc((void*)p.wg, 0, D * F * 2, 0x00020000);
#define ROT (2 * ma)
#define ROT2 0
  const rsrc_t rws = __builtin_amdgcn_make_buffer_rsrc((void*)p.ws, 0, WS_END, 0x00020000);
  u64 ev;
#define LOAD_EPOCH() asm volatile("s_load_dwordx2 %0, %1, 0\n\ts_waitcnt lgkmcnt(0)" : "=&s"(ev) : "s"(p.ws + FL_E) : "memory")
#define MAKE_TAG() const u32 tlo = (u32)(ev + 0x9E3779B97F4A7C15ull), thi = (u32)((ev + 0x9E3779B97F4A7C15ull) >> 32), tg32 = tlo ^ thi; const u32x2 tag = {tlo, thi}
  LAS char* const sy = sm + 4 * p4;

  if (w < 4) {
  if (w < 2) {
    if (!aux) {
      const rsrc_t rwu2 = __builtin_amdgcn_make_buffer_rsrc((void*)((const char*)p.wu - 2048), 0, D * F * 2 + 2048, 0x00020000);
      const u32 vx0 = (lane >> 3) * 1024 + (((lane & 7) ^ g4) << 4), vx1 = (vx0 ^ 64) + 7168, vx2 = vx0 + 14336, vx3 = vx1 + 14336;
      const u32 vg0 = g4 * (F * 2) + (((((lane & 15) >> 1) ^ g4) << 5) | ((lane & 1) << 4)), vg1 = vg0 + 4 * F * 2 - 1024;
      const u32 vu0 = vg0 ^ 128, vu1 = vu0 + 4 * F * 2 - 1024;
      const u32 sx0 = (mi * 128 + 64 * v) * 1024, sw0 = 16 * v * (F * 2) + fj * 256;
      u32 dst = 8192 * v;
      auto ux = [&](u32 c) {
        const u32 s = sx0 + ((ROT + c) & 7) * 128;
        dma4(rx, rx, smb + RB + dst, vx0, vx1, vx2, vx3, s); dma4(rx, rx, smb + RB + dst + 4096, vx0, vx1, vx2, vx3, s + 32768);
        dst = (dst + UNIT) & (RING - 1);
      };
      auto uw = [&](u32 c, u32 h) {
        const u32 s = sw0 + (2 * ((ROT + c) & 7) + h) * (32 * F * 2);
        dma4(rwg, rwu2, smb + RB + dst, vg0, vg1, vu0, vu1, s); dma4(rwg, rwu2, smb + RB + dst + 4096, vg0, vg1, vu0, vu1, s + 8 * F * 2);
        dst = (dst + UNIT) & (RING - 1);
      };
#define LDELAY 2
      __builtin_amdgcn_s_sleep(LDELAY);
      ux(0); uw(0, 0);
      LOAD_EPOCH();
#pragma unroll 1
      for (int c = 0; c < 7; ++c) {
        uw(c, 1); ux(c + 1); uw(c + 1, 0);
        WAIT_V(16);
        if (c == 0) STAMP_AT(1);
        __builtin_amdgcn_s_barrier();
      }
      uw(7, 1);
      dma1s(rws, smb + L_ST, lane * 16, WS_ST + mi * 1024);
      dma1s(rws, smb + L_CS + 1024 * v, lane * 16, WS_CS + (v * F + fj * 128) * 8);
      STAMP_AT(2);
    } else {
      LOAD_EPOCH();
      const rsrc_t rp = __builtin_amdgcn_make_buffer_rsrc((void*)p.wd, 0, D * F * 2, 0x00020000);
#define PFN 0
      u32 t = 0;
#pragma unroll 1
      for (int q = 0; q < 14; ++q) {
        if (q == 0 || q == 4) __builtin_amdgcn_s_barrier();
        const u32 so = (28 * (o - 28) + 4 * (q % 7) + 2 * (q / 7) + v) * (16 * D * 2);
        if (q < PFN) asm volatile("s_waitcnt vmcnt(2)\n\tbuffer_load_dword %0, %1, %2, %3 offen\n\tbuffer_load_dword %0, %1, %2, %4 offen" : "+v"(t) : "v"(lane * 128), "s"(rp), "s"(so), "s"(so + 8192));
      }
      asm volatile("s_waitcnt vmcnt(0)" : "+v"(t));
      STAMP_AT(2);
    }
  }
    const rsrc_t rwd = __builtin_amdgcn_make_buffer_rsrc((void*)p.wd, 0, D * F * 2, 0x00020000);
    const int rot2 = ROT2;
    if (v >= 2) {
      const int k = v - 2;
      const u32 dsk = p.dskip[ch0 + 8 * k];
      auto bar = [&] { SCHED(); if (!aux) __builtin_amdgcn_s_barrier(); FENCE(); SCHED(); };
      auto pull = [&](int i, int skip) {
        u32 d;
        asm volatile(".set pf_off, 0\n\t.rept 14\n\ts_load_dword %0, %1, pf_off\n\t.set pf_off, pf_off + 64\n\t.endr\n\t"
                     "s_cmp_ge_i32 %2, 28\n\ts_cbranch_scc1 1f\n\ts_barrier\n1:\n\ts_waitcnt lgkmcnt(0)"
                     : "=&s"(d) : "s"((const char*)p.wd + tb * 7168 + 896 * i), "s"(skip) : "scc", "memory");
      };
      if (k && lane >= 2 && lane < 32) *(LAS u32x4*)(sm + KCP * (lane >> 2) - 32 + 16 * p4) = (u32x4)(0u);
      if (k == 0) {
        u32x4 xr[8];
#pragma unroll
        for (int i = 0; i < 8; ++i) xr[i] = __builtin_amdgcn_raw_buffer_load_b128(rx, lane * 16 + 1024 * (i & 3), (8 * tb + (i & 4)) * 1024, 0);
        LOAD_EPOCH();
        float x[8][8], su[8], sq[8];
#pragma unroll
        for (int r = 0; r < 8; ++r) {
#pragma unroll
          for (int e = 0; e < 4; ++e) { x[r][2 * e] = bflo(xr[r][e]); x[r][2 * e + 1] = bfhi(xr[r][e]); }
          asm("v_add_f32 %0, %2, %3\n\tv_mul_f32 %1, %2, %2\n\tv_add_f32 %0, %0, %4\n\tv_fmac_f32 %1, %3, %3\n\tv_add_f32 %0, %0, %5\n\tv_fmac_f32 %1, %4, %4\n\t"
              "v_add_f32 %0, %0, %6\n\tv_fmac_f32 %1, %5, %5\n\tv_add_f32 %0, %0, %7\n\tv_fmac_f32 %1, %6, %6\n\tv_add_f32 %0, %0, %8\n\tv_fmac_f32 %1, %7, %7\n\t"
              "v_add_f32 %0, %0, %9\n\tv_fmac_f32 %1, %8, %8\n\tv_fmac_f32 %1, %9, %9"
              : "=&v"(su[r]), "=&v"(sq[r])
              : "v"(x[r][0]), "v"(x[r][1]), "v"(x[r][2]), "v"(x[r][3]), "v"(x[r][4]), "v"(x[r][5]), "v"(x[r][6]), "v"(x[r][7]));
        }
        STAMP_AT(1);
        bar();
        MAKE_TAG();
        auto total = [&](const float (&a)[8]) {
          const float lo = add_x8(fold16(fold32(a[0], a[2]), fold32(a[1], a[3]))), hi = add_x8(fold16(fold32(a[4], a[6]), fold32(a[5], a[7])));
          return add_m8(add_x2(add_x1(lane & 8 ? hi : lo)));
        };
        const float mean = total(su) * (1.f / D), nsq = -mean * mean;
        const float var = rbf(__builtin_fmaxf(__builtin_fmaf(total(sq), 1.f / D, nsq), 0.f));
        const u32 gran = pk(mean, __builtin_amdgcn_sqrtf(rbf(var + 1e-5f)));
        const float r = __builtin_amdgcn_rcpf(bfhi(gran)), mr = -bflo(gran) * r;
        if ((lane & 7) == 0) st8_sc1(rws, (u32x2){gran, tg32}, 8 * g4 + 4 * (lane & 8), WS_ST + 64 * tb);
        u32 n[8][4];
#pragma unroll
        for (int i = 0; i < 4; ++i) {
          if (i == 2) bar();
          const int l0 = 16 * (2 * i & 3) + 8 * (i >> 1);
          const u32 ra = __builtin_amdgcn_readlane(__builtin_bit_cast(u32, r), l0), rb = __builtin_amdgcn_readlane(__builtin_bit_cast(u32, r), l0 + 16);
          u32 ma_ = __builtin_amdgcn_readlane(__builtin_bit_cast(u32, mr), l0), mb_ = __builtin_amdgcn_readlane(__builtin_bit_cast(u32, mr), l0 + 16);
          asm volatile("s_nop 1" : "+v"(ma_), "+v"(mb_) : "s"(ra), "s"(rb));
#pragma unroll
          for (int e = 0; e < 8; ++e)
            asm("v_fma_f32 %1, %1, %3, %5\n\tv_fma_f32 %2, %2, %4, %6\n\tv_cvt_pk_bf16_f32 %0, %1, %2"
                : "=v"(n[e][i]), "+v"(x[2 * i][e]), "+v"(x[2 * i + 1][e]) : "s"(ra), "s"(rb), "v"(ma_), "v"(mb_));
        }
#pragma unroll
        for (int e = 0; e < 8; ++e) st16_sc1(rws, (u32x4){n[e][0], n[e][1], n[e][2], n[e][3]}, WS_U + tb * 8192 + lane * 16 + 1024 * e);
        SCHED(); __builtin_amdgcn_s_barrier();
        WAIT_V(0);
        if (lane == 0) st8_sc1(rws, tag, FL_1 + tb * 8, 0);
        STAMP_AT(2);
        __builtin_amdgcn_s_barrier();
      } else {
        LOAD_EPOCH();
#pragma unroll 1
        for (int i = 0; i < 4; ++i) pull(i, o - 16 * (i >> 1));
      }
      FENCE(); SCHED();
      const u32 sk = WS_K + (2 * b + k) * 8448 + 128;
      {
        float kv[16];
#pragma unroll
        for (int g = 0; g < 4; ++g) {
          LAS char* s = sm + 2 * KCP * k + 32 + 1024 * g + 16 * lane;
          const f32x4 a = *(LAS f32x4*)s, d = *(LAS f32x4*)(s + KCP);
#pragma unroll
          for (int e = 0; e < 4; ++e) asm("v_add_f32 %0, %1, %2" : "=v"(kv[4 * g + e]) : "v"(a[e]), "v"(d[e]));
        }
        const bool odd = lane & 1;
        if (lane == 0) kv[0] += bflo(dsk);
        u32x4 kc[4];
        kc[0] = (u32x4){pk(kv[0], kv[1]), pk(kv[2], kv[3]), pk(kv[4], kv[5]), pk(kv[6], kv[7])};
        kc[1] = (u32x4){pk(kv[8], kv[8]), pk(kv[8], kv[9]), pk(kv[9], kv[9]), pk(kv[10], kv[10])};
        kc[2] = (u32x4){pk(kv[11], kv[11]), pk(kv[11], kv[11]), pk(odd ? kv[12] : kv[11], kv[12]), pk(kv[12], kv[12])};
        kc[3] = (u32x4){pk(kv[13], kv[13]), pk(kv[13], kv[13]), pk(odd ? kv[14] : kv[13], kv[14]), pk(kv[14], kv[14])};
#pragma unroll
        for (int g = 0; g < 4; ++g) {
          const u32x4 cu = kc[g];
          int py = 0;
          if (g) py = __builtin_amdgcn_update_dpp(0, (int)kc[g ? g - 1 : 0].w, 0x13C, 0xf, 0xf, false);
          py = __builtin_amdgcn_update_dpp(py, (int)cu.w, 0x138, 0xf, 0xf, false);
          const u32x4 sh = {__builtin_amdgcn_alignbit(cu.x, py, 16), __builtin_amdgcn_alignbit(cu.y, cu.x, 16), __builtin_amdgcn_alignbit(cu.z, cu.y, 16), __builtin_amdgcn_alignbit(cu.w, cu.z, 16)};
          __builtin_amdgcn_raw_buffer_store_b128(cu, rws, sk + lane * 16 + 1024 * g, 0, 0);
          __builtin_amdgcn_raw_buffer_store_b128(sh, rws, sk + 4224 + lane * 16 + 1024 * g, 0, 0);
        }
        if (lane < 2) __builtin_amdgcn_raw_buffer_store_b32(0, rws, sk - 4 + 4224 * lane, 0, 0);
      }
      STAMP_AT(3);
      auto kcopy = [&](int q) { dma4s(rws, smb + (4 * k + q) * KCP + 32, lane * 16, lane * 16, lane * 16, lane * 16, sk + 4224 * (q & 1) - 2 * (q & 2)); };
      pull(4 + 2 * k, o);
      MAKE_TAG();
      if (aux) { post(sm + S_HP + 4 * k, 4); await(sm + S_HP + 4 * (k ^ 1), 0, 4); }
      WAIT_V(0);
      kcopy(0); kcopy(1);
      const u32 vf1 = FL_1 + (128 * k + 2 * lane) * 8;
      u32x4 f1 = ld16_sc1(rws, vf1, 0);
      pull(5 + 2 * k, o);
#pragma unroll 1
      for (u32 s = 0; s < SPIN_CAP && !__all(f1.x == tlo && f1.y == thi && f1.z == tlo && f1.w == thi); ++s) {
        __builtin_amdgcn_s_sleep(16);
        FENCE();
        f1 = ld16_sc1(rws, vf1, 0);
      }
      FENCE();
      STAMP_AT(4);
      kcopy(2);
      const u32 vu0 = (lane >> 1) * 8192 + ((lane ^ g4) & 1) * 16, us = 32 * 8192 - 1024;
      bar();
#define QDELAY 3
      __builtin_amdgcn_s_sleep(QDELAY);
      kcopy(3);
      dma4s(rws, smb + L_U + 1024 + 4096 * k, vu0, vu0 + us, vu0 + 2 * us, vu0 + 3 * us, WS_U + k * 128 * 8192 + tb * 32);
      if (k == 0) dma1s(rws, smb + L_U, lane * 16, 0x40000000u);
      STAMP_AT(5);
    }
    MAKE_TAG();
    const u32 dq = smb + RB + 4096 * v;
    const u32 vd0 = (lane >> 3) * (D * 2) + (((lane & 7) ^ ((lane >> 2) & 4)) << 4), vd1 = vd0 + 7168, vd2 = vd0 + 14336, vd3 = vd0 + 21504;
    auto ks_of = [&](int q) { int k = q % 7 + rot2; k = k >= 7 ? k - 7 : k; return 28 * v + 4 * k + 2 * (q / 7); };
    auto wdq = [&](int q, int slot) { dma4(rwd, rwd, dq + slot * UNIT, vd0, vd1, vd2, vd3, ks_of(q) * (16 * D * 2) + n2 * 128); };
    const u32 wo0 = RB + 4096 * v + (4 * hh + q4) * 128 + p4 * 8 + ((blk ^ (q4 & 2)) << 5), wo1 = wo0 ^ 64;
    auto wfrag = [&](int slot, int e, int ct) { LAS char* a = sm + slot * UNIT + 2048 * e + (ct ? wo1 : wo0); return trfrag(a, a + 1024); };
    bf16x8 wr_[14][2];
    auto take = [&](int q, int slot) {
#pragma unroll
      for (int e = 0; e < 4; ++e) wr_[2 * q + (e >> 1)][e & 1] = wfrag(slot, e >> 1, e & 1);
    };
    wdq(0, 1);
    if (v < 2) WAIT_V(4);
    STAMP_AT(6);
    if (!aux) __builtin_amdgcn_s_barrier();
    wdq(1, 2); wdq(2, 3); wdq(3, 4);
#define SWAP_UNIT(qa, slot, qb) do { WAIT_V(12); take(qa, slot); WAIT_L(); SCHED(); wdq(qb, slot); } while (0)
    SWAP_UNIT(0, 1, 4); SWAP_UNIT(1, 2, 5); SWAP_UNIT(2, 3, 6); SWAP_UNIT(3, 4, 10);
    SWAP_UNIT(4, 1, 7); SWAP_UNIT(5, 2, 8); SWAP_UNIT(6, 3, 9);
    STAMP_AT(7);
    if (!aux) await(sy + S_FRE, peek(sy + S_FRE), 8);
    if (v >= 2) post(sm + S_HP + 4 * (v - 2), 5);
    STAMP_AT(8);
    wdq(11, 5); wdq(12, 6); wdq(13, 7);
#define HDELAY 8
    if (!aux) { await(sy + S_GP, peek(sy + S_GP), 1); __builtin_amdgcn_s_sleep(HDELAY); }
    STAMP_AT(9);
    const u32 vf2 = FL_2 + (ti2 * 28 + 7 * v + (lane < 7 ? lane : 0)) * FS;
    wait_flags<8>(rws, vf2, tlo, thi);
    STAMP_AT(10);
    f32x16 acc[2][2];
#pragma unroll
    for (int a = 0; a < 4; ++a) acc[a >> 1][a & 1] = (f32x16)(0.f);
    constexpr int PD = 4;
    u32x4 hf[28][2];
    auto hload = [&](int i) {
      const u32 so = (ti2 * 112 + ks_of(i >> 1)) * 2048;
#define HAUX 16
      hf[i][0] = __builtin_amdgcn_raw_buffer_load_b128(rws, lane * 16 + 2048 * (i & 1), so, HAUX);
      hf[i][1] = __builtin_amdgcn_raw_buffer_load_b128(rws, lane * 16 + 2048 * (i & 1) + 1024, so, HAUX);
    };
#pragma unroll
    for (int i = 0; i < PD; ++i) hload(i);
    bf16x8 wl[2][2];
    u32x2 fb;
    SCHED();
#pragma unroll
    for (int i = 0; i < 28; ++i) {
      if (i + 1 >= 14 && i + 1 < 28) { wl[(i + 1) & 1][0] = wfrag((i - 11) >> 1, (i + 1) & 1, 0); wl[(i + 1) & 1][1] = wfrag((i - 11) >> 1, (i + 1) & 1, 1); }
      const bf16x8 a0 = i < 14 ? wr_[i < 14 ? i : 0][0] : wl[i & 1][0], a1 = i < 14 ? wr_[i < 14 ? i : 0][1] : wl[i & 1][1];
      const bf16x8 b0 = __builtin_bit_cast(bf16x8, hf[i][0]), b1 = __builtin_bit_cast(bf16x8, hf[i][1]);
      acc[0][0] = MFMA32(a0, b0, acc[0][0]);
      acc[1][0] = MFMA32(a1, b0, acc[1][0]);
      acc[0][1] = MFMA32(a0, b1, acc[0][1]);
      acc[1][1] = MFMA32(a1, b1, acc[1][1]);
      if (i == 6) STAMP_AT(15);
      if (i == 13) STAMP_AT(16);
      if (i == 20) STAMP_AT(17);
      SCHED();
#define ZAT 14
      if (i == ZAT) post(sm + S_H2 + 4 * v, 1);
      if (i == 2) fb = ld8_sc1(rws, vf2, FL_2B);
      if (i == 6) {
        if (__builtin_expect(!__all(fb.x == tlo && fb.y == thi), 0)) wait_flags<4>(rws, vf2 + FL_2B, tlo, thi);
        FENCE();
      }
      if (i < 4) { hload(PD + 2 * i); hload(PD + 2 * i + 1); }
      else if (i + PD + 4 < 28) hload(i + PD + 4);
      SCHED();
    }
    STAMP_AT(11);
#pragma unroll
    for (int g = 0; g < 4; ++g) {
      LAS char* t = sm + RB + UNIT + 4096 * v + (lane & 31) * 128 + (((2 * g + hh) ^ (lane & 7)) << 4);
#pragma unroll
      for (int a = 0; a < 4; ++a) {
        const f32x16& s = acc[a >> 1][a & 1];
        *(LAS f32x4*)(t + UNIT * a) = (f32x4){s[4 * g], s[4 * g + 1], s[4 * g + 2], s[4 * g + 3]};
      }
    }
    STAMP_AT(12);
    BARRIER();
  } else {
    __builtin_amdgcn_s_setprio(1);
    const int c = v >> 1, ah = v & 1, ch = ch0 + 8 * c;
    const int gi = ch * NM + 64 * ah + lane;
    const u32 r6 = p.logdt[ch], r0 = p.are[gi], r1 = p.aim[gi], r2 = p.bre[gi], r3 = p.bim[gi], r4 = p.cre[gi], r5 = p.cim[gi];
    SCHED();
    const int tg = c, fq = ah;
    u32 xa[4], wa[4], iw[4], ir[2];
    {
      const u32 row = 64 * tg + (lane & 31), f = (row >> 1) & 7, scr = RB + 5 * UNIT + 4096 * v, cki = 2 * blk + (p4 >> 1);
#pragma unroll
      for (int s = 0; s < 4; ++s) {
        xa[s] = RB + row * 128 + (((2 * s + hh) ^ f) << 4);
        wa[s] = RB + hh * 4096 + blk * 2048 + q4 * 256 + (((4 * fq + s) ^ (q4 + 4 * blk)) << 5) + p4 * 8;
        iw[s] = scr + lane * 64 + ((s ^ ((lane >> 1) & 3)) << 4);
        asm volatile("" : "+v"(xa[s]), "+v"(wa[s]), "+v"(iw[s]));
      }
      ir[0] = scr + (8 * hh + q4) * 64 + ((cki ^ (q4 >> 1)) << 4) + 8 * (p4 & 1);
      ir[1] = scr + (8 * hh + 4 + q4) * 64 + ((cki ^ (2 + (q4 >> 1))) << 4) + 8 * (p4 & 1);
      asm volatile("" : "+v"(ir[0]), "+v"(ir[1]));
    }
    LOAD_EPOCH();
    MAKE_TAG();
    SCHED();
    if (aux) {
      const int job = 4 * ma + (o - 28), mat = job >= 7, cg = 7 * fh + job - 7 * mat;
      float s[8];
#pragma unroll
      for (int e = 0; e < 8; ++e) s[e] = 0.f;
      if (job < 14) {
        const rsrc_t rw = __builtin_amdgcn_make_buffer_rsrc((void*)(mat ? p.wu : p.wg), 0, D * F * 2, 0x00020000);
        u32 k0 = 0x00003f80u, k1 = 0x3f800000u;
        asm("" : "+v"(k0), "+v"(k1));
        const bf16x2 s0 = __builtin_bit_cast(bf16x2, k0), s1 = __builtin_bit_cast(bf16x2, k1);
        u32x4 t[8];
        const u32 vw = g4 * (F * 2) + (lane & 15) * 16;
        u32 so = 128 * v * (F * 2) + cg * 256;
#pragma unroll
        for (int i = 0; i < 8; ++i) t[i] = __builtin_amdgcn_raw_buffer_load_b128(rw, vw, so + 4 * i * (F * 2), 0);
#pragma unroll 1
        for (int j = 0; j < 4; ++j) {
          so = j < 3 ? so + 32 * (F * 2) : 0x40000000u;
#pragma unroll
          for (int i = 0; i < 8; ++i) {
#pragma unroll
            for (int e = 0; e < 4; ++e) {
              const u32 d = t[i][e];
              s[2 * e] = __builtin_amdgcn_fdot2_f32_bf16(__builtin_bit_cast(bf16x2, d), s0, s[2 * e], false);
              s[2 * e + 1] = __builtin_amdgcn_fdot2_f32_bf16(__builtin_bit_cast(bf16x2, d), s1, s[2 * e + 1], false);
            }
            t[i] = __builtin_amdgcn_raw_buffer_load_b128(rw, vw, so + 4 * i * (F * 2), 0);
            SCHED();
          }
        }
      }
      const float a0 = fold16(fold32(s[0], s[4]), fold32(s[2], s[6])), a1 = fold16(fold32(s[1], s[5]), fold32(s[3], s[7]));
      *(LAS f32x2*)(sm + L_CS + 512 * v + 32 * (lane & 15) + 8 * g4) = (f32x2){a0, a1};
      BARRIER();
      LAS char* cx = sm + L_CS + 128 * v + 4 * (lane & 31);
      const float tot = (*(LAS float*)cx + *(LAS float*)(cx + 512)) + (*(LAS float*)(cx + 1024) + *(LAS float*)(cx + 1536)), th = rbf(tot);
      if (job < 14 && lane < 32) st8_sc1(rws, (u32x2){pk(th, tot - th), tg32}, lane * 8, WS_CS + (mat * F + cg * 128 + 32 * v) * 8);
      STAMP_AT(15);
    }
    bf16x8 yf[8], xf[8];
    {
      const float dt = rbf(__builtin_amdgcn_exp2f(bflo(r6) * 1.44269504088896f));
      const float ar2 = dt * bflo(r0) * 1.44269504088896f, air = dt * bflo(r1) * 0.159154943091895f;
      auto put = [&](int img, int g, u32x4 re, u32x4 im) {
        *(LAS u32x4*)(sm + iw[g] + 2 * img * UNIT) = re; *(LAS u32x4*)(sm + iw[g] + (1 - img) * UNIT) = im;
        SCHED();
      };
      auto get = [&](int img, int s) { const u32 d = (s < 4 ? 2 * img : 1 - img) * UNIT + 1024 * (s & 3); return trfrag(sm + ir[0] + d, sm + ir[1] + d); };
#define P2_(a) pk((a).x, (a).y)
#define P4_(a, b, c, d) (u32x4){P2_(a), P2_(b), P2_(c), P2_(d)}
      STAMP_AT(16);
      const cpx e8 = cexpl(ar2, air, 8.f);
      {
        const cpx s1 = {e8.r, -e8.i}, s2 = csq(s1), s4 = csq(s2), s8 = cexpl(ar2, -air, 64.f), s16 = csq(s8);
        const cp2 y0 = {{1.f, s1.r}, {0.f, s1.i}}, y1 = cmul2(y0, s2), y2 = cmul2(y0, s4), y3 = cmul2(y1, s4);
        put(0, 0, P4_(y0.r, y1.r, y2.r, y3.r), P4_(y0.i, y1.i, y2.i, y3.i));
        const cp2 y4 = cmul2(y0, s8), y5 = cmul2(y1, s8), y6 = cmul2(y2, s8), y7 = cmul2(y3, s8);
        put(0, 1, P4_(y4.r, y5.r, y6.r, y7.r), P4_(y4.i, y5.i, y6.i, y7.i));
        const cp2 y8 = cmul2(y0, s16), y9 = cmul2(y1, s16), ya = cmul2(y2, s16), yb = cmul2(y3, s16);
        put(0, 2, P4_(y8.r, y9.r, ya.r, yb.r), P4_(y8.i, y9.i, ya.i, yb.i));
        const cp2 yc = cmul2(y4, s16), yd = cmul2(y5, s16), ye = cmul2(y6, s16), yg = cmul2(y7, s16);
        put(0, 3, P4_(yc.r, yd.r, ye.r, yg.r), P4_(yc.i, yd.i, ye.i, yg.i));
      }
      {
        const cpx cf = cmul({bflo(r4), bflo(r5)}, {dt * bflo(r2), dt * bflo(r3)});
        const cpx e1 = cexpl(ar2, air, 1.f), e256 = cexpl(ar2, air, 256.f);
        const cpx e2 = csq(e1), e4 = csq(e2), e512 = csq(e256), e1024 = csq(e512);
        const cp2 cb = cmul2({{e1.r, e256.r}, {e1.i, e256.i}}, cf);
        const cp2 a0 = {{cf.r, cb.r.x}, {cf.i, cb.i.x}}, a1 = cmul2(a0, e2);
        const cp2 t0 = cmul2({{e2.r, e4.r}, {e2.i, e4.i}}, {cb.r.y, cb.i.y});
#define XA(m) (u32x4){P2_(a0.m), P2_(a1.m), pk(cb.m.y, cb.m.y), P2_(t0.m)}
#pragma unroll
        for (int s = 0; s < 4; ++s) yf[s] = get(0, s);
        put(1, 0, XA(r), XA(i));
        const cp2 a2 = cmul2(a0, e4), a3 = cmul2(a1, e4), t1 = cmul2(t0, e4);
#define XB(m) (u32x4){P2_(a2.m), P2_(a3.m), pk(t0.m.y, t0.m.y), P2_(t1.m)}
        put(1, 1, XB(r), XB(i));
#pragma unroll
        for (int s = 4; s < 8; ++s) yf[s] = get(0, s);
        const cp2 q0 = {{cf.r, cb.r.y}, {cf.i, cb.i.y}}, q1 = cmul2(q0, e512), q2 = cmul2(q0, e1024);
        const cp2 u4 = cmul2(q1, e4), u8 = cmul2(q1, e8);
#define XC(m) (u32x4){pk(q1.m.x, u4.m.x), pk(u8.m.x, q2.m.x), pk(q1.m.y, u4.m.y), pk(u8.m.y, q2.m.y)}
        put(1, 2, XC(r), XC(i));
        const cp2 q3 = cmul2(q1, e1024), v8 = cmul2(q2, e8), w8 = cmul2(q3, e8);
#define XD(m) (u32x4){pk(v8.m.x, q3.m.x), pk(w8.m.x, 0.f), pk(v8.m.y, q3.m.y), pk(w8.m.y, 0.f)}
        put(1, 3, XD(r), XD(i));
#pragma unroll
        for (int s = 0; s < 8; ++s) xf[s] = get(1, s);
      }
      STAMP_AT(17);
    }
    bf16x8 X0[2], W0[4];
    HOLD(2, 1500)
    if (!aux) BARRIER();
    X0[0] = ldfrag(sm + xa[0]); X0[1] = ldfrag(sm + xa[0] + 4096);
#pragma unroll
    for (int t = 0; t < 4; ++t) W0[t] = trfrag(sm + UNIT + wa[t], sm + UNIT + wa[t] + 1024);
    SCHED();
    {
      f32x16 kacc = (f32x16)(0.f);
#pragma unroll
      for (int s = 0; s < 8; ++s) kacc = MFMA32(xf[s], yf[s], kacc);
#pragma unroll
      for (int g = 0; g < 4; ++g) *(LAS f32x4*)(sm + KCP * v + 32 + 1024 * g + 16 * lane) = (f32x4){kacc[4 * g], kacc[4 * g + 1], kacc[4 * g + 2], kacc[4 * g + 3]};
    }
    SCHED();
    if (aux) BARRIER();
    else {
      f32x16 acc[2][4];
#pragma unroll
      for (int a = 0; a < 8; ++a) acc[a >> 2][a & 3] = (f32x16)(0.f);
      bf16x8 X1[2], W1[4];
      auto step = [&](const bf16x8 (&Xc)[2], const bf16x8 (&Wc)[4], bf16x8 (&Xn)[2], bf16x8 (&Wn)[4], u32 xb, u32 wb, int s, auto mid) {
        LAS char* xp = sm + (xb + xa[s]);
        auto wld = [&](int t) { LAS char* a = sm + (wb + wa[t]) + 8192 * (s & 1); Wn[t] = trfrag(a, a + 1024); };
        acc[0][0] = MFMA32(Wc[0], Xc[0], acc[0][0]); SCHED();
        mid(); wld(0); SCHED();
        acc[1][0] = MFMA32(Wc[0], Xc[1], acc[1][0]); SCHED();
        Xn[0] = ldfrag(xp); Xn[1] = ldfrag(xp + 4096); SCHED();
        acc[0][1] = MFMA32(Wc[1], Xc[0], acc[0][1]); SCHED();
        wld(1); SCHED();
        acc[1][1] = MFMA32(Wc[1], Xc[1], acc[1][1]); SCHED();
        wld(2); SCHED();
        acc[0][2] = MFMA32(Wc[2], Xc[0], acc[0][2]); SCHED();
        wld(3); SCHED();
        acc[1][2] = MFMA32(Wc[2], Xc[1], acc[1][2]); SCHED();
        acc[0][3] = MFMA32(Wc[3], Xc[0], acc[0][3]); SCHED();
        acc[1][3] = MFMA32(Wc[3], Xc[1], acc[1][3]); SCHED();
      };
      u32 xb = 0;
      step(X0, W0, X1, W1, xb, UNIT, 1, [&] {});
#pragma unroll 1
      for (int cc = 0;; ++cc) {
        const u32 w1 = (xb + 2 * UNIT) & (RING - 1);
        step(X1, W1, X0, W0, xb, w1, 2, [&] {});
        step(X0, W0, X1, W1, xb, w1, 3, [&] {});
        if (cc == 7) break;
        xb = (xb + 3 * UNIT) & (RING - 1);
        const u32 w0 = (xb + UNIT) & (RING - 1);
        step(X1, W1, X0, W0, xb, w0, 0, [&] { BARRIER(); });
        step(X0, W0, X1, W1, xb, w0, 1, [&] {});
      }
      STAMP_AT(2);
      HOLD(3, 3000)
      u32x2 st[2], cs[4];
      const u32 ost = 8 * (64 * tg + (lane & 31)), ocs = 8 * (64 * fq + (lane & 15));
#pragma unroll
      for (int t = 0; t < 2; ++t) st[t] = *(LAS u32x2*)(sm + L_ST + ost + 256 * t);
#pragma unroll
      for (int t = 0; t < 4; ++t) cs[t] = *(LAS u32x2*)(sm + L_CS + 1024 * blk + ocs + 128 * t);
#pragma unroll
      for (int a = 0; a < 8; ++a) acc[a & 1][a >> 1] = MFMA32(W1[a >> 1], X1[a & 1], acc[a & 1][a >> 1]);
#pragma unroll 1
      for (u32 s = 0; s < SPIN_CAP && __builtin_expect(!__all(st[0].y == tg32 && st[1].y == tg32 && cs[0].y == tg32 && cs[1].y == tg32 && cs[2].y == tg32 && cs[3].y == tg32), 0); ++s) {
        FENCE();
#pragma unroll
        for (int t = 0; t < 2; ++t) st[t] = ld8_sc1(rws, ost + 256 * t, WS_ST + mi * 1024);
#pragma unroll
        for (int t = 0; t < 4; ++t) cs[t] = ld8_sc1(rws, blk * F * 8 + ocs + 128 * t, WS_CS + fj * 1024);
        __builtin_amdgcn_s_sleep(8);
      }
      post(sm + S_FRE + 4 * v, 8);
      STAMP_NW(19);
      bf16x8 XM[2];
#pragma unroll
      for (int t = 0; t < 2; ++t) { const u32 m = hh ? 0u : (st[t].x & 0xffffu) ^ 0x8000u; XM[t] = __builtin_bit_cast(bf16x8, (u32x4){m | m << 16, 0, 0, 0}); }
#pragma unroll
      for (int a = 0; a < 8; ++a) acc[a & 1][a >> 1] = MFMA32(__builtin_bit_cast(bf16x8, (u32x4){cs[a >> 1].x, 0, 0, 0}), XM[a & 1], acc[a & 1][a >> 1]);
      const u32 vh = ((2 * mi + tg) * 112 + 8 * fj + 4 * fq) * 2048 + lane * 16;
      float ka[2], kb[2], kr[2];
#pragma unroll
      for (int t = 0; t < 2; ++t) {
        const float sd = bfhi(st[t].x), r = __builtin_amdgcn_rcpf(sd);
        kb[t] = -2.f * 0.7978845608028654f * 1.44269504088896f * r; ka[t] = kb[t] * 0.044715f * r * r; kr[t] = sd * sd;
      }
#pragma unroll
      for (int a = 0; a < 8; ++a) {
        const int tt = a & 1, ft = a >> 1;
        u32x4 hv;
#pragma unroll
        for (int e = 0; e < 4; ++e) {
          const f32x2 g = {acc[tt][ft][2 * e], acc[tt][ft][2 * e + 1]}, u = {acc[tt][ft][8 + 2 * e], acc[tt][ft][9 + 2 * e]};
          const f32x2 z = (g * g * ka[tt] + kb[tt]) * g;
          const f32x2 ex = {__builtin_amdgcn_exp2f(z.x), __builtin_amdgcn_exp2f(z.y)};
          const f32x2 dn = ex * kr[tt] + kr[tt];
          const f32x2 rc = {__builtin_amdgcn_rcpf(dn.x), __builtin_amdgcn_rcpf(dn.y)};
          const f32x2 hq = g * u * rc;
          hv[e] = pk(hq.x, hq.y);
        }
        st16_sc1(rws, hv, vh + 1024 * a);
#define FLAGA 5
        if (a == 1) STAMP_NW(20);
        if (a == 3) STAMP_NW(21);
        if (a == FLAGA) {
          STAMP_NW(22);
          asm volatile("s_waitcnt vmcnt(%0)" :: "i"(FLAGA - 3) : "memory");
          if (lane == 0) st8_sc1(rws, tag, FL_2 + ((2 * mi + tg) * 28 + 2 * fj + fq) * FS, 0);
          post(sm + S_GP + 4 * v, 1);
          STAMP_NW(18);
        }
        SCHED();
      }
      STAMP_NW(23); STAMP_NWEND();
      STAMP_AT(3);
    }
    LAS char* ka = sm + (4 * c + q4) * KCP + 2 * (28 + 128 * ah + 4 * p4 - 8 * g4);
    const u32 ub0 = 30 - 16 * ah + 2 * (lane & 15) + g4;
    f32x4 y[8];
#pragma unroll
    for (int i = 0; i < 8; ++i) y[i] = (f32x4)(0.f);
    await(sm + S_HP + 4 * (lane & 1), peek(sm + S_HP + 4 * (lane & 1)), 5);
    if (b == 255 && tid == 256) st8_sc1(rws, tag, FL_E, 0);
    const u32 vo = (64 * ti2 + 8 * v + (lane >> 3)) * (D * 2) + n2 * 128 + (lane & 7) * 16;
    u32x4 xres[2];
    STAMP_AT(4);
    bf16x8 A[2][8], B[2][8];
    auto cld = [&](int a, int i) {
      A[a & 1][i] = trfrag(ka + 64 * a + 512 * i + 8, ka + 64 * a + 512 * i);
      B[a & 1][i] = ldfrag(sm + L_U + 32 * (ub0 - 4 * a) + 16 * ((c ^ ((ub0 - 4 * a) >> 3)) & 1) + 1024 * i);
    };
#pragma unroll
    for (int i = 0; i < 8; ++i) cld(0, i);
#pragma unroll
    for (int a = 0; a < 4; ++a) {
#pragma unroll
      for (int s = 0; s < 8; ++s) {
#pragma unroll
        for (int i = 0; i <= s; ++i) {
          if (i + s < 8) y[i + s] = MFMA16(A[a & 1][i], B[a & 1][s], y[i + s]);
          if (i < s && i + s < 8) y[i + s] = MFMA16(A[a & 1][s], B[a & 1][i], y[i + s]);
        }
        SCHED();
        if (a < 3) cld(a + 1, s);
        SCHED();
      }
#define FLAG_AT 0
      if (a == FLAG_AT) {
        STAMP_AT(5);
        WAIT_V(0);
        if (!aux && lane == 0) st8_sc1(rws, tag, FL_2 + ((2 * mi + c) * 28 + 2 * fj + ah) * FS, FL_2B);
        xres[0] = __builtin_amdgcn_raw_buffer_load_b128(rx, vo, 0, 0);
        xres[1] = __builtin_amdgcn_raw_buffer_load_b128(rx, vo, 32 * D * 2, 0);
        STAMP_AT(6);
      }
    }
    STAMP_AT(7);
    LAS char* const pw = sm + S_GP + 4 * (v ^ 1);
    LAS char* ex = sm + 4 * c * KCP + 32;
    post(sm + S_GP + 4 * v, 2);
    await(pw, peek(pw), 2);
    if (ah) {
#pragma unroll
      for (int i = 0; i < 4; ++i) { *(LAS f32x4*)(ex + KCP + 1024 * i + 16 * lane) = y[i]; FENCE(); y[i] = y[4 + i]; }
    } else {
#pragma unroll
      for (int i = 0; i < 4; ++i) *(LAS f32x4*)(ex + 1024 * i + 16 * lane) = y[4 + i];
    }
    post(sm + S_GP + 4 * v, 3);
    await(pw, peek(pw), 3);
    STAMP_AT(8);
#pragma unroll
    for (int i = 0; i < 4; ++i) {
      const f32x4 t = *(LAS f32x4*)(ex + KCP * (ah ^ 1) + 1024 * i + 16 * lane) + y[i];
      st16_sc1(rws, (u32x4){pk(t[0], t[1]), tg32, pk(t[2], t[3]), tg32}, WS_ZT + ch * 8192 + 4096 * ah + 64 * (lane & 15) + 16 * g4 + 1024 * i);
    }
    STAMP_AT(9);
#define ZSLEEP 8
    const u32 vz = (64 * n2 + 16 * v + g4) * 8192 + 256 * ti2 + (lane & 15) * 16;
    u32x4 zz[4];
    await(sy + S_H2, peek(sy + S_H2), 1);
    STAMP_AT(10);
#pragma unroll 1
    for (u32 s = 0; s < SPIN_CAP; ++s) {
      if (s) __builtin_amdgcn_s_sleep(ZSLEEP);
      FENCE();
#pragma unroll
      for (int i = 0; i < 4; ++i) zz[i] = ld16_sc1(rws, vz, WS_ZT + 4 * 8192 * i);
      if (__all(zz[0].y == tg32 && zz[0].w == tg32 && zz[1].y == tg32 && zz[1].w == tg32 && zz[2].y == tg32 && zz[2].w == tg32 && zz[3].y == tg32 && zz[3].w == tg32)) break;
    }
    STAMP_AT(11);
    LAS char* stg = ex + 3 * KCP + 2048 * ah;
#pragma unroll
    for (int i = 0; i < 4; ++i) *(LAS u32x2*)(stg + 512 * i + 8 * lane) = (u32x2){zz[i].x, zz[i].z};
    LAS char* tr = stg + q4 * 128 + 32 * g4 + 8 * p4;
    const bf16x8 ra = trfrag(tr, tr + 512), rc = trfrag(tr + 1024, tr + 1536);
    LAS char* rb = ex + 2 * KCP + 2048 * ah + 32 * lane;
    *(LAS bf16x8*)rb = ra;
    *(LAS bf16x8*)(rb + 16) = rc;
    post(sm + S_GP + 4 * v, 4);
    await(sy + S_GP, peek(sy + S_GP), 4);
    const int tok = 8 * v + (lane >> 3), dc = lane & 7;
    f32x4 s0[2], s1[2];
#pragma unroll
    for (int i = 0; i < 2; ++i) {
      const u32x4 z = *(LAS u32x4*)(sm + (4 * (dc >> 2) + 2) * KCP + 32 + 2048 * ((dc >> 1) & 1) + 32 * tok + 16 * (dc & 1) + 1024 * i), x = xres[i];
      s0[i] = (f32x4){bflo(x[0]) + bflo(z[0]), bfhi(x[0]) + bfhi(z[0]), bflo(x[1]) + bflo(z[1]), bfhi(x[1]) + bfhi(z[1])};
      s1[i] = (f32x4){bflo(x[2]) + bflo(z[2]), bfhi(x[2]) + bfhi(z[2]), bflo(x[3]) + bflo(z[3]), bfhi(x[3]) + bfhi(z[3])};
    }
    u32 t0 = RB + UNIT + UNIT * 2 * (dc >> 2) + tok * 128 + (((2 * (dc & 3)) ^ (tok & 7)) << 4), t1 = t0 ^ 16u;
    char* op = (char*)p.out + vo;
    asm volatile("" : "+v"(s0[0]), "+v"(s0[1]), "+v"(s1[0]), "+v"(s1[1]), "+v"(t0), "+v"(t1), "+v"(op));
    STAMP_AT(12);
    BARRIER();
    STAMP_AT(13);
#pragma unroll
    for (int i = 0; i < 2; ++i) {
#pragma unroll
      for (int g = 0; g < 4; ++g) { s0[i] += *(LAS f32x4*)(sm + t0 + UNIT * i + 4096 * g); s1[i] += *(LAS f32x4*)(sm + t1 + UNIT * i + 4096 * g); }
      *(GAS u32x4*)(op + 32 * D * 2 * i) = (u32x4){pk(s0[i][0], s0[i][1]), pk(s0[i][2], s0[i][3]), pk(s1[i][0], s1[i][1]), pk(s1[i][2], s1[i][3])};
    }
  }
  HOLD(1, 4000) BURN(5)
  STAMP_AT(14); STAMP_OUT();
}

extern "C" void kernel_launch(void* const* d_in, const int* in_sizes, int n_in, void* d_out, int out_size, void* d_ws,
                              size_t ws_size, hipStream_t stream) {
  Params p;
  p.x = (const u16*)d_in[0]; p.are = (const u16*)d_in[1]; p.aim = (const u16*)d_in[2];
  p.bre = (const u16*)d_in[3]; p.bim = (const u16*)d_in[4]; p.cre = (const u16*)d_in[5];
  p.cim = (const u16*)d_in[6]; p.logdt = (const u16*)d_in[7]; p.dskip = (const u16*)d_in[8];
  p.wg = (const u16*)d_in[10]; p.wu = (const u16*)d_in[11]; p.wd = (const u16*)d_in[12];
  p.out = (u16*)d_out; p.ws = (char*)d_ws;
  s4ffn_kernel<<<256, 512, 0, stream>>>(p);
}
```

```cpp
#define PROBE 1
#include <hip/hip_runtime.h>
#include <stdint.h>

typedef unsigned short u16;
typedef unsigned u32;
typedef unsigned long long u64;
typedef __bf16 bf16x2 __attribute__((ext_vector_type(2)));
typedef __bf16 bf16x4 __attribute__((ext_vector_type(4)));
typedef __bf16 bf16x8 __attribute__((ext_vector_type(8)));
typedef float f32x2 __attribute__((ext_vector_type(2)));
typedef float f32x4 __attribute__((ext_vector_type(4)));
typedef float f32x16 __attribute__((ext_vector_type(16)));
typedef u32 u32x2 __attribute__((ext_vector_type(2)));
typedef u32 u32x4 __attribute__((ext_vector_type(4)));
#define LAS __attribute__((address_space(3)))
#define GAS __attribute__((address_space(1)))
#define DI __device__ __forceinline__
typedef __amdgpu_buffer_rsrc_t rsrc_t;

constexpr int L = 2048, D = 512, NM = 128, F = 1792;
constexpr u32 WS_H = 0, WS_U = 8u << 20, WS_ZT = 10u << 20, WS_ST = 14u << 20, WS_CS = WS_ST + 65536, WS_FL = 15u << 20, WS_K = 16u << 20, WS_END = 24u << 20;
constexpr u32 FL_E = WS_FL, FL_1 = WS_FL + 4096, FL_2 = WS_FL + 131072, FS = 128, FL_2B = 131072;
constexpr u32 KCP = 4160, UNIT = 16384, RB = 32768, RING = 8 * UNIT;
constexpr u32 L_U = 8 * KCP;
constexpr u32 L_ST = L_U + 9216;
constexpr u32 L_CS = L_ST + 1024;
constexpr u32 S_FRE = 4128, S_HP = 4144, S_GP = KCP + 4128, S_H2 = KCP + 4144;
constexpr u32 SPIN_CAP = 1u << 16;

struct Params {
  const u16 *x, *are, *aim, *bre, *bim, *cre, *cim, *logdt, *dskip, *wg, *wu, *wd;
  u16* out;
  char* ws;
};

#define STAMP_DECL
#define STAMP_AT(k)
#define STAMP_RT(v)
#define STAMP_NW(k)
#define STAMP_NWEND()
#define STAMP_OUT()

DI float bflo(u32 v) { return __builtin_bit_cast(float, v << 16); }
DI float bfhi(u32 v) { return __builtin_bit_cast(float, v & 0xffff0000u); }
DI u32 pk(float a, float b) { f32x2 f = {a, b}; return __builtin_bit_cast(u32, __builtin_convertvector(f, bf16x2)); }
DI float rbf(float a) { return bflo(pk(a, a)); }
DI void dma4(rsrc_t ra, rsrc_t rb, u32 lds, u32 v0, u32 v1, u32 v2, u32 v3, u32 soff) {
  asm volatile("s_mov_b32 m0, %0\n\ts_nop 0\n\tbuffer_load_dwordx4 %1, %5, %7 offen lds\n\tbuffer_load_dwordx4 %2, %5, %7 offen offset:1024 lds\n\t"
               "buffer_load_dwordx4 %3, %6, %7 offen offset:2048 lds\n\tbuffer_load_dwordx4 %4, %6, %7 offen offset:3072 lds"
               :: "s"(lds), "v"(v0), "v"(v1), "v"(v2), "v"(v3), "s"(ra), "s"(rb), "s"(soff));
}
DI void dma4s(rsrc_t r, u32 lds, u32 v0, u32 v1, u32 v2, u32 v3, u32 soff) {
  asm volatile("s_mov_b32 m0, %0\n\ts_nop 0\n\tbuffer_load_dwordx4 %1, %5, %6 offen sc1 lds\n\tbuffer_load_dwordx4 %2, %5, %6 offen offset:1024 sc1 lds\n\t"
               "buffer_load_dwordx4 %3, %5, %6 offen offset:2048 sc1 lds\n\tbuffer_load_dwordx4 %4, %5, %6 offen offset:3072 sc1 lds"
               :: "s"(lds), "v"(v0), "v"(v1), "v"(v2), "v"(v3), "s"(r), "s"(soff));
}
DI void dma1s(rsrc_t r, u32 lds, u32 v0, u32 soff) {
  asm volatile("s_mov_b32 m0, %0\n\ts_nop 0\n\tbuffer_load_dwordx4 %1, %2, %3 offen sc1 lds" :: "s"(lds), "v"(v0), "s"(r), "s"(soff));
}
#define WAIT_V(n) asm volatile("s_waitcnt vmcnt(" #n ")" ::: "memory")
#define WAIT_L() asm volatile("s_waitcnt lgkmcnt(0)" ::: "memory")
#define BARRIER() asm volatile("s_waitcnt lgkmcnt(0)\n\ts_barrier" ::: "memory")
#define FENCE() asm volatile("" ::: "memory")
#define SCHED() __builtin_amdgcn_sched_barrier(0)
DI bf16x8 ldfrag(LAS char* a) { return *(LAS bf16x8*)a; }
DI bf16x8 trfrag(LAS char* a0, LAS char* a1) {
  bf16x4 lo = __builtin_amdgcn_ds_read_tr16_b64_v4bf16((LAS bf16x4*)a0);
  bf16x4 hi = __builtin_amdgcn_ds_read_tr16_b64_v4bf16((LAS bf16x4*)a1);
  return __builtin_shufflevector(lo, hi, 0, 1, 2, 3, 4, 5, 6, 7);
}
#define MFMA32(a, b, c) __builtin_amdgcn_mfma_f32_32x32x16_bf16(a, b, c, 0, 0, 0)
#define MFMA16(a, b, c) __builtin_amdgcn_mfma_f32_16x16x32_bf16(a, b, c, 0, 0, 0)
DI u32x2 ld8_sc1(rsrc_t r, u32 voff, u32 soff) { return __builtin_amdgcn_raw_buffer_load_b64(r, voff, soff, 16); }
DI u32x4 ld16_sc1(rsrc_t r, u32 voff, u32 soff) { return __builtin_amdgcn_raw_buffer_load_b128(r, voff, soff, 16); }
DI void st8_sc1(rsrc_t r, u32x2 v, u32 voff, u32 soff) { __builtin_amdgcn_raw_buffer_store_b64(v, r, voff, soff, 16); }
DI void st16_sc1(rsrc_t r, u32x4 v, u32 voff) { __builtin_amdgcn_raw_buffer_store_b128(v, r, voff, 0, 16); }
template <int SLEEP> DI void wait_flags(rsrc_t r, u32 voff, u32 tlo, u32 thi) {
#pragma unroll 1
  for (u32 s = 0; s < SPIN_CAP; ++s) {
    FENCE();
    u32x2 f = ld8_sc1(r, voff, 0);
    if (__all(f.x == tlo && f.y == thi)) break;
    __builtin_amdgcn_s_sleep(SLEEP);
  }
  WAIT_V(0);
}
DI void post(LAS char* word, u32 v) {
  asm volatile("s_mov_b64 exec, 1\n\tds_write_b32 %0, %1\n\ts_mov_b64 exec, -1" :: "v"(word), "v"(v) : "memory");
}
DI u32 peek(LAS char* word) { return *(volatile LAS u32*)word; }
DI void await(LAS char* word, u32 seen, u32 need) {
  if (__builtin_expect(__any(seen < need), 0)) {
#pragma unroll 1
    for (u32 s = 0; s < SPIN_CAP * 16; ++s) { __builtin_amdgcn_s_sleep(1); if (!__any(peek(word) < need)) break; }
  }
  FENCE();
}
#define DPPADD(name, ctl) DI float name(float x) { float r; asm("s_nop 1\n\tv_add_f32_dpp %0, %1, %1 " ctl " row_mask:0xf bank_mask:0xf" : "=v"(r) : "v"(x)); return r; }
DPPADD(add_x1, "quad_perm:[1,0,3,2]") DPPADD(add_x2, "quad_perm:[2,3,0,1]") DPPADD(add_m8, "row_half_mirror") DPPADD(add_x8, "row_ror:8")
DI float fold32(float a, float b) {
  asm("s_nop 1\n\tv_permlane32_swap_b32 %0, %1\n\tv_add_f32 %0, %0, %1" : "+v"(a), "+v"(b)); return a;
}
DI float fold16(float a, float b) {
  asm("s_nop 1\n\tv_permlane16_swap_b32 %0, %1\n\tv_add_f32 %0, %0, %1" : "+v"(a), "+v"(b)); return a;
}
DI u32 vmov(u32 a) { u32 r; asm("v_mov_b32 %0, %1" : "=v"(r) : "v"(a)); return r; }
struct cpx { float r, i; };
struct cp2 { f32x2 r, i; };
DI cpx cmul(cpx a, cpx b) { return {a.r * b.r - a.i * b.i, a.r * b.i + a.i * b.r}; }
DI cpx csq(cpx a) { return {a.r * a.r - a.i * a.i, (a.r + a.r) * a.i}; }
DI cp2 cmul2(cp2 a, cpx b) { return {a.r * b.r - a.i * b.i, a.i * b.r + a.r * b.i}; }
DI cpx cexpl(float ar2, float air, float l) {
  float m = __builtin_amdgcn_exp2f(ar2 * l), f = __builtin_amdgcn_fractf(air * l);
  return {m * __builtin_amdgcn_cosf(f), m * __builtin_amdgcn_sinf(f)};
}

__global__ __launch_bounds__(512, 2) void s4ffn_kernel(Params p) {
  __shared__ __attribute__((aligned(16))) char smem[163840];
  LAS char* sm = (LAS char*)smem;
  const u32 smb = (u32)(size_t)sm;
  const int tid = threadIdx.x, lane = tid & 63;
  const int w = __builtin_amdgcn_readfirstlane(tid >> 6), v = w & 3;
  const int b = blockIdx.x, xcd = b & 7, o = b >> 3;
  const bool aux = o >= 28;
  const int ma = xcd >> 1, fh = xcd & 1;
  const int il = o / 7, jl = o - il * 7;
  const int mi = 4 * ma + il, fj = 7 * fh + jl;
  const int ti2 = 4 * xcd + (o >> 3), n2 = o & 7;
  const int tb = 32 * xcd + o, ch0 = 16 * o + xcd;
  const int hh = lane >> 5, blk = (lane >> 4) & 1, q4 = (lane >> 2) & 3, p4 = lane & 3, g4 = lane >> 4;
  STAMP_DECL; STAMP_RT(rt0_); STAMP_AT(0);
  const u64 pt0 = __builtin_amdgcn_s_memrealtime();
#define HOLD(id, ticks) if (PROBE == id) { while (__builtin_amdgcn_s_memrealtime() < pt0 + (ticks)) __builtin_amdgcn_s_sleep(1); }
#define BURN(id) if (PROBE == id) { for (int i_ = 0; i_ < 8; ++i_) __builtin_amdgcn_s_sleep(32); }
  BURN(4)
  const rsrc_t rx = __builtin_amdgcn_make_buffer_rsrc((void*)p.x, 0, L * D * 2, 0x00020000);
  const rsrc_t rwg = __builtin_amdgcn_make_buffer_rsrc((void*)p.wg, 0, D * F * 2, 0x00020000);
#define ROT (2 * ma)
#define ROT2 0
  const rsrc_t rws = __builtin_amdgcn_make_buffer_rsrc((void*)p.ws, 0, WS_END, 0x00020000);
  u64 ev;
#define LOAD_EPOCH() asm volatile("s_load_dwordx2 %0, %1, 0\n\ts_waitcnt lgkmcnt(0)" : "=&s"(ev) : "s"(p.ws + FL_E) : "memory")
#define MAKE_TAG() const u32 tlo = (u32)(ev + 0x9E3779B97F4A7C15ull), thi = (u32)((ev + 0x9E3779B97F4A7C15ull) >> 32), tg32 = tlo ^ thi; const u32x2 tag = {tlo, thi}
  LAS char* const sy = sm + 4 * p4;

  if (w < 4) {
  if (w < 2) {
    if (!aux) {
      const rsrc_t rwu2 = __builtin_amdgcn_make_buffer_rsrc((void*)((const char*)p.wu - 2048), 0, D * F * 2 + 2048, 0x00020000);
      const u32 vx0 = (lane >> 3) * 1024 + (((lane & 7) ^ g4) << 4), vx1 = (vx0 ^ 64) + 7168, vx2 = vx0 + 14336, vx3 = vx1 + 14336;
      const u32 vg0 = g4 * (F * 2) + (((((lane & 15) >> 1) ^ g4) << 5) | ((lane & 1) << 4)), vg1 = vg0 + 4 * F * 2 - 1024;
      const u32 vu0 = vg0 ^ 128, vu1 = vu0 + 4 * F * 2 - 1024;
      const u32 sx0 = (mi * 128 + 64 * v) * 1024, sw0 = 16 * v * (F * 2) + fj * 256;
      u32 dst = 8192 * v;
      auto ux = [&](u32 c) {
        const u32 s = sx0 + ((ROT + c) & 7) * 128;
        dma4(rx, rx, smb + RB + dst, vx0, vx1, vx2, vx3, s); dma4(rx, rx, smb + RB + dst + 4096, vx0, vx1, vx2, vx3, s + 32768);
        dst = (dst + UNIT) & (RING - 1);
      };
      auto uw = [&](u32 c, u32 h) {
        const u32 s = sw0 + (2 * ((ROT + c) & 7) + h) * (32 * F * 2);
        dma4(rwg, rwu2, smb + RB + dst, vg0, vg1, vu0, vu1, s); dma4(rwg, rwu2, smb + RB + dst + 4096, vg0, vg1, vu0, vu1, s + 8 * F * 2);
        dst = (dst + UNIT) & (RING - 1);
      };
#define LDELAY 2
      __builtin_amdgcn_s_sleep(LDELAY);
      ux(0); uw(0, 0);
      LOAD_EPOCH();
#pragma unroll 1
      for (int c = 0; c < 7; ++c) {
        uw(c, 1); ux(c + 1); uw(c + 1, 0);
        WAIT_V(16);
        if (c == 0) STAMP_AT(1);
        __builtin_amdgcn_s_barrier();
      }
      uw(7, 1);
      dma1s(rws, smb + L_ST, lane * 16, WS_ST + mi * 1024);
      dma1s(rws, smb + L_CS + 1024 * v, lane * 16, WS_CS + (v * F + fj * 128) * 8);
      STAMP_AT(2);
    } else {
      LOAD_EPOCH();
      const rsrc_t rp = __builtin_amdgcn_make_buffer_rsrc((void*)p.wd, 0, D * F * 2, 0x00020000);
#define PFN 0
      u32 t = 0;
#pragma unroll 1
      for (int q = 0; q < 14; ++q) {
        if (q == 0 || q == 4) __builtin_amdgcn_s_barrier();
        const u32 so = (28 * (o - 28) + 4 * (q % 7) + 2 * (q / 7) + v) * (16 * D * 2);
        if (q < PFN) asm volatile("s_waitcnt vmcnt(2)\n\tbuffer_load_dword %0, %1, %2, %3 offen\n\tbuffer_load_dword %0, %1, %2, %4 offen" : "+v"(t) : "v"(lane * 128), "s"(rp), "s"(so), "s"(so + 8192));
      }
      asm volatile("s_waitcnt vmcnt(0)" : "+v"(t));
      STAMP_AT(2);
    }
  }
    const rsrc_t rwd = __builtin_amdgcn_make_buffer_rsrc((void*)p.wd, 0, D * F * 2, 0x00020000);
    const int rot2 = ROT2;
    if (v >= 2) {
      const int k = v - 2;
      const u32 dsk = p.dskip[ch0 + 8 * k];
      auto bar = [&] { SCHED(); if (!aux) __builtin_amdgcn_s_barrier(); FENCE(); SCHED(); };
      auto pull = [&](int i, int skip) {
        u32 d;
        asm volatile(".set pf_off, 0\n\t.rept 14\n\ts_load_dword %0, %1, pf_off\n\t.set pf_off, pf_off + 64\n\t.endr\n\t"
                     "s_cmp_ge_i32 %2, 28\n\ts_cbranch_scc1 1f\n\ts_barrier\n1:\n\ts_waitcnt lgkmcnt(0)"
                     : "=&s"(d) : "s"((const char*)p.wd + tb * 7168 + 896 * i), "s"(skip) : "scc", "memory");
      };
      if (k && lane >= 2 && lane < 32) *(LAS u32x4*)(sm + KCP * (lane >> 2) - 32 + 16 * p4) = (u32x4)(0u);
      if (k == 0) {
        u32x4 xr[8];
#pragma unroll
        for (int i = 0; i < 8; ++i) xr[i] = __builtin_amdgcn_raw_buffer_load_b128(rx, lane * 16 + 1024 * (i & 3), (8 * tb + (i & 4)) * 1024, 0);
        LOAD_EPOCH();
        float x[8][8], su[8], sq[8];
#pragma unroll
        for (int r = 0; r < 8; ++r) {
#pragma unroll
          for (int e = 0; e < 4; ++e) { x[r][2 * e] = bflo(xr[r][e]); x[r][2 * e + 1] = bfhi(xr[r][e]); }
          asm("v_add_f32 %0, %2, %3\n\tv_mul_f32 %1, %2, %2\n\tv_add_f32 %0, %0, %4\n\tv_fmac_f32 %1, %3, %3\n\tv_add_f32 %0, %0, %5\n\tv_fmac_f32 %1, %4, %4\n\t"
              "v_add_f32 %0, %0, %6\n\tv_fmac_f32 %1, %5, %5\n\tv_add_f32 %0, %0, %7\n\tv_fmac_f32 %1, %6, %6\n\tv_add_f32 %0, %0, %8\n\tv_fmac_f32 %1, %7, %7\n\t"
              "v_add_f32 %0, %0, %9\n\tv_fmac_f32 %1, %8, %8\n\tv_fmac_f32 %1, %9, %9"
              : "=&v"(su[r]), "=&v"(sq[r])
              : "v"(x[r][0]), "v"(x[r][1]), "v"(x[r][2]), "v"(x[r][3]), "v"(x[r][4]), "v"(x[r][5]), "v"(x[r][6]), "v"(x[r][7]));
        }
        STAMP_AT(1);
        bar();
        MAKE_TAG();
        auto total = [&](const float (&a)[8]) {
          const float lo = add_x8(fold16(fold32(a[0], a[2]), fold32(a[1], a[3]))), hi = add_x8(fold16(fold32(a[4], a[6]), fold32(a[5], a[7])));
          return add_m8(add_x2(add_x1(lane & 8 ? hi : lo)));
        };
        const float mean = total(su) * (1.f / D), nsq = -mean * mean;
        const float var = rbf(__builtin_fmaxf(__builtin_fmaf(total(sq), 1.f / D, nsq), 0.f));
        const u32 gran = pk(mean, __builtin_amdgcn_sqrtf(rbf(var + 1e-5f)));
        const float r = __builtin_amdgcn_rcpf(bfhi(gran)), mr = -bflo(gran) * r;
        if ((lane & 7) == 0) st8_sc1(rws, (u32x2){gran, tg32}, 8 * g4 + 4 * (lane & 8), WS_ST + 64 * tb);
        u32 n[8][4];
#pragma unroll
        for (int i = 0; i < 4; ++i) {
          if (i == 2) bar();
          const int l0 = 16 * (2 * i & 3) + 8 * (i >> 1);
          const u32 ra = __builtin_amdgcn_readlane(__builtin_bit_cast(u32, r), l0), rb = __builtin_amdgcn_readlane(__builtin_bit_cast(u32, r), l0 + 16);
          u32 ma_ = __builtin_amdgcn_readlane(__builtin_bit_cast(u32, mr), l0), mb_ = __builtin_amdgcn_readlane(__builtin_bit_cast(u32, mr), l0 + 16);
          asm volatile("s_nop 1" : "+v"(ma_), "+v"(mb_) : "s"(ra), "s"(rb));
#pragma unroll
          for (int e = 0; e < 8; ++e)
            asm("v_fma_f32 %1, %1, %3, %5\n\tv_fma_f32 %2, %2, %4, %6\n\tv_cvt_pk_bf16_f32 %0, %1, %2"
                : "=v"(n[e][i]), "+v"(x[2 * i][e]), "+v"(x[2 * i + 1][e]) : "s"(ra), "s"(rb), "v"(ma_), "v"(mb_));
        }
#pragma unroll
        for (int e = 0; e < 8; ++e) st16_sc1(rws, (u32x4){n[e][0], n[e][1], n[e][2], n[e][3]}, WS_U + tb * 8192 + lane * 16 + 1024 * e);
        SCHED(); __builtin_amdgcn_s_barrier();
        WAIT_V(0);
        if (lane == 0) st8_sc1(rws, tag, FL_1 + tb * 8, 0);
        STAMP_AT(2);
        __builtin_amdgcn_s_barrier();
      } else {
        LOAD_EPOCH();
#pragma unroll 1
        for (int i = 0; i < 4; ++i) pull(i, o - 16 * (i >> 1));
      }
      FENCE(); SCHED();
      const u32 sk = WS_K + (2 * b + k) * 8448 + 128;
      {
        float kv[16];
#pragma unroll
        for (int g = 0; g < 4; ++g) {
          LAS char* s = sm + 2 * KCP * k + 32 + 1024 * g + 16 * lane;
          const f32x4 a = *(LAS f32x4*)s, d = *(LAS f32x4*)(s + KCP);
#pragma unroll
          for (int e = 0; e < 4; ++e) asm("v_add_f32 %0, %1, %2" : "=v"(kv[4 * g + e]) : "v"(a[e]), "v"(d[e]));
        }
        const bool odd = lane & 1;
        if (lane == 0) kv[0] += bflo(dsk);
        u32x4 kc[4];
        kc[0] = (u32x4){pk(kv[0], kv[1]), pk(kv[2], kv[3]), pk(kv[4], kv[5]), pk(kv[6], kv[7])};
        kc[1] = (u32x4){pk(kv[8], kv[8]), pk(kv[8], kv[9]), pk(kv[9], kv[9]), pk(kv[10], kv[10])};
        kc[2] = (u32x4){pk(kv[11], kv[11]), pk(kv[11], kv[11]), pk(odd ? kv[12] : kv[11], kv[12]), pk(kv[12], kv[12])};
        kc[3] = (u32x4){pk(kv[13], kv[13]), pk(kv[13], kv[13]), pk(odd ? kv[14] : kv[13], kv[14]), pk(kv[14], kv[14])};
#pragma unroll
        for (int g = 0; g < 4; ++g) {
          const u32x4 cu = kc[g];
          int py = 0;
          if (g) py = __builtin_amdgcn_update_dpp(0, (int)kc[g ? g - 1 : 0].w, 0x13C, 0xf, 0xf, false);
          py = __builtin_amdgcn_update_dpp(py, (int)cu.w, 0x138, 0xf, 0xf, false);
          const u32x4 sh = {__builtin_amdgcn_alignbit(cu.x, py, 16), __builtin_amdgcn_alignbit(cu.y, cu.x, 16), __builtin_amdgcn_alignbit(cu.z, cu.y, 16), __builtin_amdgcn_alignbit(cu.w, cu.z, 16)};
          __builtin_amdgcn_raw_buffer_store_b128(cu, rws, sk + lane * 16 + 1024 * g, 0, 0);
          __builtin_amdgcn_raw_buffer_store_b128(sh, rws, sk + 4224 + lane * 16 + 1024 * g, 0, 0);
        }
        if (lane < 2) __builtin_amdgcn_raw_buffer_store_b32(0, rws, sk - 4 + 4224 * lane, 0, 0);
      }
      STAMP_AT(3);
      auto kcopy = [&](int q) { dma4s(rws, smb + (4 * k + q) * KCP + 32, lane * 16, lane * 16, lane * 16, lane * 16, sk + 4224 * (q & 1) - 2 * (q & 2)); };
      pull(4 + 2 * k, o);
      MAKE_TAG();
      if (aux) { post(sm + S_HP + 4 * k, 4); await(sm + S_HP + 4 * (k ^ 1), 0, 4); }
      WAIT_V(0);
      kcopy(0); kcopy(1);
      const u32 vf1 = FL_1 + (128 * k + 2 * lane) * 8;
      u32x4 f1 = ld16_sc1(rws, vf1, 0);
      pull(5 + 2 * k, o);
#pragma unroll 1
      for (u32 s = 0; s < SPIN_CAP && !__all(f1.x == tlo && f1.y == thi && f1.z == tlo && f1.w == thi); ++s) {
        __builtin_amdgcn_s_sleep(16);
        FENCE();
        f1 = ld16_sc1(rws, vf1, 0);
      }
      FENCE();
      STAMP_AT(4);
      kcopy(2);
      const u32 vu0 = (lane >> 1) * 8192 + ((lane ^ g4) & 1) * 16, us = 32 * 8192 - 1024;
      bar();
#define QDELAY 3
      __builtin_amdgcn_s_sleep(QDELAY);
      kcopy(3);
      dma4s(rws, smb + L_U + 1024 + 4096 * k, vu0, vu0 + us, vu0 + 2 * us, vu0 + 3 * us, WS_U + k * 128 * 8192 + tb * 32);
      if (k == 0) dma1s(rws, smb + L_U, lane * 16, 0x40000000u);
      STAMP_AT(5);
    }
    MAKE_TAG();
    const u32 dq = smb + RB + 4096 * v;
    const u32 vd0 = (lane >> 3) * (D * 2) + (((lane & 7) ^ ((lane >> 2) & 4)) << 4), vd1 = vd0 + 7168, vd2 = vd0 + 14336, vd3 = vd0 + 21504;
    auto ks_of = [&](int q) { int k = q % 7 + rot2; k = k >= 7 ? k - 7 : k; return 28 * v + 4 * k + 2 * (q / 7); };
    auto wdq = [&](int q, int slot) { dma4(rwd, rwd, dq + slot * UNIT, vd0, vd1, vd2, vd3, ks_of(q) * (16 * D * 2) + n2 * 128); };
    const u32 wo0 = RB + 4096 * v + (4 * hh + q4) * 128 + p4 * 8 + ((blk ^ (q4 & 2)) << 5), wo1 = wo0 ^ 64;
    auto wfrag = [&](int slot, int e, int ct) { LAS char* a = sm + slot * UNIT + 2048 * e + (ct ? wo1 : wo0); return trfrag(a, a + 1024); };
    bf16x8 wr_[14][2];
    auto take = [&](int q, int slot) {
#pragma unroll
      for (int e = 0; e < 4; ++e) wr_[2 * q + (e >> 1)][e & 1] = wfrag(slot, e >> 1, e & 1);
    };
    wdq(0, 1);
    if (v < 2) WAIT_V(4);
    STAMP_AT(6);
    if (!aux) __builtin_amdgcn_s_barrier();
    wdq(1, 2); wdq(2, 3); wdq(3, 4);
#define SWAP_UNIT(qa, slot, qb) do { WAIT_V(12); take(qa, slot); WAIT_L(); SCHED(); wdq(qb, slot); } while (0)
    SWAP_UNIT(0, 1, 4); SWAP_UNIT(1, 2, 5); SWAP_UNIT(2, 3, 6); SWAP_UNIT(3, 4, 10);
    SWAP_UNIT(4, 1, 7); SWAP_UNIT(5, 2, 8); SWAP_UNIT(6, 3, 9);
    STAMP_AT(7);
    if (!aux) await(sy + S_FRE, peek(sy + S_FRE), 8);
    if (v >= 2) post(sm + S_HP + 4 * (v - 2), 5);
    STAMP_AT(8);
    wdq(11, 5); wdq(12, 6); wdq(13, 7);
#define HDELAY 8
    if (!aux) { await(sy + S_GP, peek(sy + S_GP), 1); __builtin_amdgcn_s_sleep(HDELAY); }
    STAMP_AT(9);
    const u32 vf2 = FL_2 + (ti2 * 28 + 7 * v + (lane < 7 ? lane : 0)) * FS;
    wait_flags<8>(rws, vf2, tlo, thi);
    STAMP_AT(10);
    f32x16 acc[2][2];
#pragma unroll
    for (int a = 0; a < 4; ++a) acc[a >> 1][a & 1] = (f32x16)(0.f);
    constexpr int PD = 4;
    u32x4 hf[28][2];
    auto hload = [&](int i) {
      const u32 so = (ti2 * 112 + ks_of(i >> 1)) * 2048;
#define HAUX 16
      hf[i][0] = __builtin_amdgcn_raw_buffer_load_b128(rws, lane * 16 + 2048 * (i & 1), so, HAUX);
      hf[i][1] = __builtin_amdgcn_raw_buffer_load_b128(rws, lane * 16 + 2048 * (i & 1) + 1024, so, HAUX);
    };
#pragma unroll
    for (int i = 0; i < PD; ++i) hload(i);
    bf16x8 wl[2][2];
    u32x2 fb;
    SCHED();
#pragma unroll
    for (int i = 0; i < 28; ++i) {
      if (i + 1 >= 14 && i + 1 < 28) { wl[(i + 1) & 1][0] = wfrag((i - 11) >> 1, (i + 1) & 1, 0); wl[(i + 1) & 1][1] = wfrag((i - 11) >> 1, (i + 1) & 1, 1); }
      const bf16x8 a0 = i < 14 ? wr_[i < 14 ? i : 0][0] : wl[i & 1][0], a1 = i < 14 ? wr_[i < 14 ? i : 0][1] : wl[i & 1][1];
      const bf16x8 b0 = __builtin_bit_cast(bf16x8, hf[i][0]), b1 = __builtin_bit_cast(bf16x8, hf[i][1]);
      acc[0][0] = MFMA32(a0, b0, acc[0][0]);
      acc[1][0] = MFMA32(a1, b0, acc[1][0]);
      acc[0][1] = MFMA32(a0, b1, acc[0][1]);
      acc[1][1] = MFMA32(a1, b1, acc[1][1]);
      if (i == 6) STAMP_AT(15);
      if (i == 13) STAMP_AT(16);
      if (i == 20) STAMP_AT(17);
      SCHED();
#define ZAT 14
      if (i == ZAT) post(sm + S_H2 + 4 * v, 1);
      if (i == 2) fb = ld8_sc1(rws, vf2, FL_2B);
      if (i == 6) {
        if (__builtin_expect(!__all(fb.x == tlo && fb.y == thi), 0)) wait_flags<4>(rws, vf2 + FL_2B, tlo, thi);
        FENCE();
      }
      if (i < 4) { hload(PD + 2 * i); hload(PD + 2 * i + 1); }
      else if (i + PD + 4 < 28) hload(i + PD + 4);
      SCHED();
    }
    STAMP_AT(11);
#pragma unroll
    for (int g = 0; g < 4; ++g) {
      LAS char* t = sm + RB + UNIT + 4096 * v + (lane & 31) * 128 + (((2 * g + hh) ^ (lane & 7)) << 4);
#pragma unroll
      for (int a = 0; a < 4; ++a) {
        const f32x16& s = acc[a >> 1][a & 1];
        *(LAS f32x4*)(t + UNIT * a) = (f32x4){s[4 * g], s[4 * g + 1], s[4 * g + 2], s[4 * g + 3]};
      }
    }
    STAMP_AT(12);
    BARRIER();
  } else {
    __builtin_amdgcn_s_setprio(1);
    const int c = v >> 1, ah = v & 1, ch = ch0 + 8 * c;
    const int gi = ch * NM + 64 * ah + lane;
    const u32 r6 = p.logdt[ch], r0 = p.are[gi], r1 = p.aim[gi], r2 = p.bre[gi], r3 = p.bim[gi], r4 = p.cre[gi], r5 = p.cim[gi];
    SCHED();
    const int tg = c, fq = ah;
    u32 xa[4], wa[4], iw[4], ir[2];
    {
      const u32 row = 64 * tg + (lane & 31), f = (row >> 1) & 7, scr = RB + 5 * UNIT + 4096 * v, cki = 2 * blk + (p4 >> 1);
#pragma unroll
      for (int s = 0; s < 4; ++s) {
        xa[s] = RB + row * 128 + (((2 * s + hh) ^ f) << 4);
        wa[s] = RB + hh * 4096 + blk * 2048 + q4 * 256 + (((4 * fq + s) ^ (q4 + 4 * blk)) << 5) + p4 * 8;
        iw[s] = scr + lane * 64 + ((s ^ ((lane >> 1) & 3)) << 4);
        asm volatile("" : "+v"(xa[s]), "+v"(wa[s]), "+v"(iw[s]));
      }
      ir[0] = scr + (8 * hh + q4) * 64 + ((cki ^ (q4 >> 1)) << 4) + 8 * (p4 & 1);
      ir[1] = scr + (8 * hh + 4 + q4) * 64 + ((cki ^ (2 + (q4 >> 1))) << 4) + 8 * (p4 & 1);
      asm volatile("" : "+v"(ir[0]), "+v"(ir[1]));
    }
    LOAD_EPOCH();
    MAKE_TAG();
    SCHED();
    if (aux) {
      const int job = 4 * ma + (o - 28), mat = job >= 7, cg = 7 * fh + job - 7 * mat;
      float s[8];
#pragma unroll
      for (int e = 0; e < 8; ++e) s[e] = 0.f;
      if (job < 14) {
        const rsrc_t rw = __builtin_amdgcn_make_buffer_rsrc((void*)(mat ? p.wu : p.wg), 0, D * F * 2, 0x00020000);
        u32 k0 = 0x00003f80u, k1 = 0x3f800000u;
        asm("" : "+v"(k0), "+v"(k1));
        const bf16x2 s0 = __builtin_bit_cast(bf16x2, k0), s1 = __builtin_bit_cast(bf16x2, k1);
        u32x4 t[8];
        const u32 vw = g4 * (F * 2) + (lane & 15) * 16;
        u32 so = 128 * v * (F * 2) + cg * 256;
#pragma unroll
        for (int i = 0; i < 8; ++i) t[i] = __builtin_amdgcn_raw_buffer_load_b128(rw, vw, so + 4 * i * (F * 2), 0);
#pragma unroll 1
        for (int j = 0; j < 4; ++j) {
          so = j < 3 ? so + 32 * (F * 2) : 0x40000000u;
#pragma unroll
          for (int i = 0; i < 8; ++i) {
#pragma unroll
            for (int e = 0; e < 4; ++e) {
              const u32 d = t[i][e];
              s[2 * e] = __builtin_amdgcn_fdot2_f32_bf16(__builtin_bit_cast(bf16x2, d), s0, s[2 * e], false);
              s[2 * e + 1] = __builtin_amdgcn_fdot2_f32_bf16(__builtin_bit_cast(bf16x2, d), s1, s[2 * e + 1], false);
            }
            t[i] = __builtin_amdgcn_raw_buffer_load_b128(rw, vw, so + 4 * i * (F * 2), 0);
            SCHED();
          }
        }
      }
      const float a0 = fold16(fold32(s[0], s[4]), fold32(s[2], s[6])), a1 = fold16(fold32(s[1], s[5]), fold32(s[3], s[7]));
      *(LAS f32x2*)(sm + L_CS + 512 * v + 32 * (lane & 15) + 8 * g4) = (f32x2){a0, a1};
      BARRIER();
      LAS char* cx = sm + L_CS + 128 * v + 4 * (lane & 31);
      const float tot = (*(LAS float*)cx + *(LAS float*)(cx + 512)) + (*(LAS float*)(cx + 1024) + *(LAS float*)(cx + 1536)), th = rbf(tot);
      if (job < 14 && lane < 32) st8_sc1(rws, (u32x2){pk(th, tot - th), tg32}, lane * 8, WS_CS + (mat * F + cg * 128 + 32 * v) * 8);
      STAMP_AT(15);
    }
    bf16x8 yf[8], xf[8];
    {
      const float dt = rbf(__builtin_amdgcn_exp2f(bflo(r6) * 1.44269504088896f));
      const float ar2 = dt * bflo(r0) * 1.44269504088896f, air = dt * bflo(r1) * 0.159154943091895f;
      auto put = [&](int img, int g, u32x4 re, u32x4 im) {
        *(LAS u32x4*)(sm + iw[g] + 2 * img * UNIT) = re; *(LAS u32x4*)(sm + iw[g] + (1 - img) * UNIT) = im;
        SCHED();
      };
      auto get = [&](int img, int s) { const u32 d = (s < 4 ? 2 * img : 1 - img) * UNIT + 1024 * (s & 3); return trfrag(sm + ir[0] + d, sm + ir[1] + d); };
#define P2_(a) pk((a).x, (a).y)
#define P4_(a, b, c, d) (u32x4){P2_(a), P2_(b), P2_(c), P2_(d)}
      STAMP_AT(16);
      const cpx e8 = cexpl(ar2, air, 8.f);
      {
        const cpx s1 = {e8.r, -e8.i}, s2 = csq(s1), s4 = csq(s2), s8 = cexpl(ar2, -air, 64.f), s16 = csq(s8);
        const cp2 y0 = {{1.f, s1.r}, {0.f, s1.i}}, y1 = cmul2(y0, s2), y2 = cmul2(y0, s4), y3 = cmul2(y1, s4);
        put(0, 0, P4_(y0.r, y1.r, y2.r, y3.r), P4_(y0.i, y1.i, y2.i, y3.i));
        const cp2 y4 = cmul2(y0, s8), y5 = cmul2(y1, s8), y6 = cmul2(y2, s8), y7 = cmul2(y3, s8);
        put(0, 1, P4_(y4.r, y5.r, y6.r, y7.r), P4_(y4.i, y5.i, y6.i, y7.i));
        const cp2 y8 = cmul2(y0, s16), y9 = cmul2(y1, s16), ya = cmul2(y2, s16), yb = cmul2(y3, s16);
        put(0, 2, P4_(y8.r, y9.r, ya.r, yb.r), P4_(y8.i, y9.i, ya.i, yb.i));
        const cp2 yc = cmul2(y4, s16), yd = cmul2(y5, s16), ye = cmul2(y6, s16), yg = cmul2(y7, s16);
        put(0, 3, P4_(yc.r, yd.r, ye.r, yg.r), P4_(yc.i, yd.i, ye.i, yg.i));
      }
      {
        const cpx cf = cmul({bflo(r4), bflo(r5)}, {dt * bflo(r2), dt * bflo(r3)});
        const cpx e1 = cexpl(ar2, air, 1.f), e256 = cexpl(ar2, air, 256.f);
        const cpx e2 = csq(e1), e4 = csq(e2), e512 = csq(e256), e1024 = csq(e512);
        const cp2 cb = cmul2({{e1.r, e256.r}, {e1.i, e256.i}}, cf);
        const cp2 a0 = {{cf.r, cb.r.x}, {cf.i, cb.i.x}}, a1 = cmul2(a0, e2);
        const cp2 t0 = cmul2({{e2.r, e4.r}, {e2.i, e4.i}}, {cb.r.y, cb.i.y});
#define XA(m) (u32x4){P2_(a0.m), P2_(a1.m), pk(cb.m.y, cb.m.y), P2_(t0.m)}
#pragma unroll
        for (int s = 0; s < 4; ++s) yf[s] = get(0, s);
        put(1, 0, XA(r), XA(i));
        const cp2 a2 = cmul2(a0, e4), a3 = cmul2(a1, e4), t1 = cmul2(t0, e4);
#define XB(m) (u32x4){P2_(a2.m), P2_(a3.m), pk(t0.m.y, t0.m.y), P2_(t1.m)}
        put(1, 1, XB(r), XB(i));
#pragma unroll
        for (int s = 4; s < 8; ++s) yf[s] = get(0, s);
        const cp2 q0 = {{cf.r, cb.r.y}, {cf.i, cb.i.y}}, q1 = cmul2(q0, e512), q2 = cmul2(q0, e1024);
        const cp2 u4 = cmul2(q1, e4), u8 = cmul2(q1, e8);
#define XC(m) (u32x4){pk(q1.m.x, u4.m.x), pk(u8.m.x, q2.m.x), pk(q1.m.y, u4.m.y), pk(u8.m.y, q2.m.y)}
        put(1, 2, XC(r), XC(i));
        const cp2 q3 = cmul2(q1, e1024), v8 = cmul2(q2, e8), w8 = cmul2(q3, e8);
#define XD(m) (u32x4){pk(v8.m.x, q3.m.x), pk(w8.m.x, 0.f), pk(v8.m.y, q3.m.y), pk(w8.m.y, 0.f)}
        put(1, 3, XD(r), XD(i));
#pragma unroll
        for (int s = 0; s < 8; ++s) xf[s] = get(1, s);
      }
      STAMP_AT(17);
    }
    bf16x8 X0[2], W0[4];
    HOLD(2, 1500)
    if (!aux) BARRIER();
    X0[0] = ldfrag(sm + xa[0]); X0[1] = ldfrag(sm + xa[0] + 4096);
#pragma unroll
    for (int t = 0; t < 4; ++t) W0[t] = trfrag(sm + UNIT + wa[t], sm + UNIT + wa[t] + 1024);
    SCHED();
    {
      f32x16 kacc = (f32x16)(0.f);
#pragma unroll
      for (int s = 0; s < 8; ++s) kacc = MFMA32(xf[s], yf[s], kacc);
#pragma unroll
      for (int g = 0; g < 4; ++g) *(LAS f32x4*)(sm + KCP * v + 32 + 1024 * g + 16 * lane) = (f32x4){kacc[4 * g], kacc[4 * g + 1], kacc[4 * g + 2], kacc[4 * g + 3]};
    }
    SCHED();
    if (aux) BARRIER();
    else {
      f32x16 acc[2][4];
#pragma unroll
      for (int a = 0; a < 8; ++a) acc[a >> 2][a & 3] = (f32x16)(0.f);
      bf16x8 X1[2], W1[4];
      auto step = [&](const bf16x8 (&Xc)[2], const bf16x8 (&Wc)[4], bf16x8 (&Xn)[2], bf16x8 (&Wn)[4], u32 xb, u32 wb, int s, auto mid) {
        LAS char* xp = sm + (xb + xa[s]);
        auto wld = [&](int t) { LAS char* a = sm + (wb + wa[t]) + 8192 * (s & 1); Wn[t] = trfrag(a, a + 1024); };
        acc[0][0] = MFMA32(Wc[0], Xc[0], acc[0][0]); SCHED();
        mid(); wld(0); SCHED();
        acc[1][0] = MFMA32(Wc[0], Xc[1], acc[1][0]); SCHED();
        Xn[0] = ldfrag(xp); Xn[1] = ldfrag(xp + 4096); SCHED();
        acc[0][1] = MFMA32(Wc[1], Xc[0], acc[0][1]); SCHED();
        wld(1); SCHED();
        acc[1][1] = MFMA32(Wc[1], Xc[1], acc[1][1]); SCHED();
        wld(2); SCHED();
        acc[0][2] = MFMA32(Wc[2], Xc[0], acc[0][2]); SCHED();
        wld(3); SCHED();
        acc[1][2] = MFMA32(Wc[2], Xc[1], acc[1][2]); SCHED();
        acc[0][3] = MFMA32(Wc[3], Xc[0], acc[0][3]); SCHED();
        acc[1][3] = MFMA32(Wc[3], Xc[1], acc[1][3]); SCHED();
      };
      u32 xb = 0;
      step(X0, W0, X1, W1, xb, UNIT, 1, [&] {});
#pragma unroll 1
      for (int cc = 0;; ++cc) {
        const u32 w1 = (xb + 2 * UNIT) & (RING - 1);
        step(X1, W1, X0, W0, xb, w1, 2, [&] {});
        step(X0, W0, X1, W1, xb, w1, 3, [&] {});
        if (cc == 7) break;
        xb = (xb + 3 * UNIT) & (RING - 1);
        const u32 w0 = (xb + UNIT) & (RING - 1);
        step(X1, W1, X0, W0, xb, w0, 0, [&] { BARRIER(); });
        step(X0, W0, X1, W1, xb, w0, 1, [&] {});
      }
      STAMP_AT(2);
      HOLD(3, 3000)
      u32x2 st[2], cs[4];
      const u32 ost = 8 * (64 * tg + (lane & 31)), ocs = 8 * (64 * fq + (lane & 15));
#pragma unroll
      for (int t = 0; t < 2; ++t) st[t] = *(LAS u32x2*)(sm + L_ST + ost + 256 * t);
#pragma unroll
      for (int t = 0; t < 4; ++t) cs[t] = *(LAS u32x2*)(sm + L_CS + 1024 * blk + ocs + 128 * t);
#pragma unroll
      for (int a = 0; a < 8; ++a) acc[a & 1][a >> 1] = MFMA32(W1[a >> 1], X1[a & 1], acc[a & 1][a >> 1]);
#pragma unroll 1
      for (u32 s = 0; s < SPIN_CAP && __builtin_expect(!__all(st[0].y == tg32 && st[1].y == tg32 && cs[0].y == tg32 && cs[1].y == tg32 && cs[2].y == tg32 && cs[3].y == tg32), 0); ++s) {
        FENCE();
#pragma unroll
        for (int t = 0; t < 2; ++t) st[t] = ld8_sc1(rws, ost + 256 * t, WS_ST + mi * 1024);
#pragma unroll
        for (int t = 0; t < 4; ++t) cs[t] = ld8_sc1(rws, blk * F * 8 + ocs + 128 * t, WS_CS + fj * 1024);
        __builtin_amdgcn_s_sleep(8);
      }
      post(sm + S_FRE + 4 * v, 8);
      STAMP_NW(19);
      bf16x8 XM[2];
#pragma unroll
      for (int t = 0; t < 2; ++t) { const u32 m = hh ? 0u : (st[t].x & 0xffffu) ^ 0x8000u; XM[t] = __builtin_bit_cast(bf16x8, (u32x4){m | m << 16, 0, 0, 0}); }
#pragma unroll
      for (int a = 0; a < 8; ++a) acc[a & 1][a >> 1] = MFMA32(__builtin_bit_cast(bf16x8, (u32x4){cs[a >> 1].x, 0, 0, 0}), XM[a & 1], acc[a & 1][a >> 1]);
      const u32 vh = ((2 * mi + tg) * 112 + 8 * fj + 4 * fq) * 2048 + lane * 16;
      float ka[2], kb[2], kr[2];
#pragma unroll
      for (int t = 0; t < 2; ++t) {
        const float sd = bfhi(st[t].x), r = __builtin_amdgcn_rcpf(sd);
        kb[t] = -2.f * 0.7978845608028654f * 1.44269504088896f * r; ka[t] = kb[t] * 0.044715f * r * r; kr[t] = sd * sd;
      }
#pragma unroll
      for (int a = 0; a < 8; ++a) {
        const int tt = a & 1, ft = a >> 1;
        u32x4 hv;
#pragma unroll
        for (int e = 0; e < 4; ++e) {
          const f32x2 g = {acc[tt][ft][2 * e], acc[tt][ft][2 * e + 1]}, u = {acc[tt][ft][8 + 2 * e], acc[tt][ft][9 + 2 * e]};
          const f32x2 z = (g * g * ka[tt] + kb[tt]) * g;
          const f32x2 ex = {__builtin_amdgcn_exp2f(z.x), __builtin_amdgcn_exp2f(z.y)};
          const f32x2 dn = ex * kr[tt] + kr[tt];
          const f32x2 rc = {__builtin_amdgcn_rcpf(dn.x), __builtin_amdgcn_rcpf(dn.y)};
          const f32x2 hq = g * u * rc;
          hv[e] = pk(hq.x, hq.y);
        }
        st16_sc1(rws, hv, vh + 1024 * a);
#define FLAGA 5
        if (a == 1) STAMP_NW(20);
        if (a == 3) STAMP_NW(21);
        if (a == FLAGA) {
          STAMP_NW(22);
          asm volatile("s_waitcnt vmcnt(%0)" :: "i"(FLAGA - 3) : "memory");
          if (lane == 0) st8_sc1(rws, tag, FL_2 + ((2 * mi + tg) * 28 + 2 * fj + fq) * FS, 0);
          post(sm + S_GP + 4 * v, 1);
          STAMP_NW(18);
        }
        SCHED();
      }
      STAMP_NW(23); STAMP_NWEND();
      STAMP_AT(3);
    }
    LAS char* ka = sm + (4 * c + q4) * KCP + 2 * (28 + 128 * ah + 4 * p4 - 8 * g4);
    const u32 ub0 = 30 - 16 * ah + 2 * (lane & 15) + g4;
    f32x4 y[8];
#pragma unroll
    for (int i = 0; i < 8; ++i) y[i] = (f32x4)(0.f);
    await(sm + S_HP + 4 * (lane & 1), peek(sm + S_HP + 4 * (lane & 1)), 5);
    if (b == 255 && tid == 256) st8_sc1(rws, tag, FL_E, 0);
    const u32 vo = (64 * ti2 + 8 * v + (lane >> 3)) * (D * 2) + n2 * 128 + (lane & 7) * 16;
    u32x4 xres[2];
    STAMP_AT(4);
    bf16x8 A[2][8], B[2][8];
    auto cld = [&](int a, int i) {
      A[a & 1][i] = trfrag(ka + 64 * a + 512 * i + 8, ka + 64 * a + 512 * i);
      B[a & 1][i] = ldfrag(sm + L_U + 32 * (ub0 - 4 * a) + 16 * ((c ^ ((ub0 - 4 * a) >> 3)) & 1) + 1024 * i);
    };
#pragma unroll
    for (int i = 0; i < 8; ++i) cld(0, i);
#pragma unroll
    for (int a = 0; a < 4; ++a) {
#pragma unroll
      for (int s = 0; s < 8; ++s) {
#pragma unroll
        for (int i = 0; i <= s; ++i) {
          if (i + s < 8) y[i + s] = MFMA16(A[a & 1][i], B[a & 1][s], y[i + s]);
          if (i < s && i + s < 8) y[i + s] = MFMA16(A[a & 1][s], B[a & 1][i], y[i + s]);
        }
        SCHED();
        if (a < 3) cld(a + 1, s);
        SCHED();
      }
#define FLAG_AT 0
      if (a == FLAG_AT) {
        STAMP_AT(5);
        WAIT_V(0);
        if (!aux && lane == 0) st8_sc1(rws, tag, FL_2 + ((2 * mi + c) * 28 + 2 * fj + ah) * FS, FL_2B);
        xres[0] = __builtin_amdgcn_raw_buffer_load_b128(rx, vo, 0, 0);
        xres[1] = __builtin_amdgcn_raw_buffer_load_b128(rx, vo, 32 * D * 2, 0);
        STAMP_AT(6);
      }
    }
    STAMP_AT(7);
    LAS char* const pw = sm + S_GP + 4 * (v ^ 1);
    LAS char* ex = sm + 4 * c * KCP + 32;
    post(sm + S_GP + 4 * v, 2);
    await(pw, peek(pw), 2);
    if (ah) {
#pragma unroll
      for (int i = 0; i < 4; ++i) { *(LAS f32x4*)(ex + KCP + 1024 * i + 16 * lane) = y[i]; FENCE(); y[i] = y[4 + i]; }
    } else {
#pragma unroll
      for (int i = 0; i < 4; ++i) *(LAS f32x4*)(ex + 1024 * i + 16 * lane) = y[4 + i];
    }
    post(sm + S_GP + 4 * v, 3);
    await(pw, peek(pw), 3);
    STAMP_AT(8);
#pragma unroll
    for (int i = 0; i < 4; ++i) {
      const f32x4 t = *(LAS f32x4*)(ex + KCP * (ah ^ 1) + 1024 * i + 16 * lane) + y[i];
      st16_sc1(rws, (u32x4){pk(t[0], t[1]), tg32, pk(t[2], t[3]), tg32}, WS_ZT + ch * 8192 + 4096 * ah + 64 * (lane & 15) + 16 * g4 + 1024 * i);
    }
    STAMP_AT(9);
#define ZSLEEP 8
    const u32 vz = (64 * n2 + 16 * v + g4) * 8192 + 256 * ti2 + (lane & 15) * 16;
    u32x4 zz[4];
    await(sy + S_H2, peek(sy + S_H2), 1);
    STAMP_AT(10);
#pragma unroll 1
    for (u32 s = 0; s < SPIN_CAP; ++s) {
      if (s) __builtin_amdgcn_s_sleep(ZSLEEP);
      FENCE();
#pragma unroll
      for (int i = 0; i < 4; ++i) zz[i] = ld16_sc1(rws, vz, WS_ZT + 4 * 8192 * i);
      if (__all(zz[0].y == tg32 && zz[0].w == tg32 && zz[1].y == tg32 && zz[1].w == tg32 && zz[2].y == tg32 && zz[2].w == tg32 && zz[3].y == tg32 && zz[3].w == tg32)) break;
    }
    STAMP_AT(11);
    LAS char* stg = ex + 3 * KCP + 2048 * ah;
#pragma unroll
    for (int i = 0; i < 4; ++i) *(LAS u32x2*)(stg + 512 * i + 8 * lane) = (u32x2){zz[i].x, zz[i].z};
    LAS char* tr = stg + q4 * 128 + 32 * g4 + 8 * p4;
    const bf16x8 ra = trfrag(tr, tr + 512), rc = trfrag(tr + 1024, tr + 1536);
    LAS char* rb = ex + 2 * KCP + 2048 * ah + 32 * lane;
    *(LAS bf16x8*)rb = ra;
    *(LAS bf16x8*)(rb + 16) = rc;
    post(sm + S_GP + 4 * v, 4);
    await(sy + S_GP, peek(sy + S_GP), 4);
    const int tok = 8 * v + (lane >> 3), dc = lane & 7;
    f32x4 s0[2], s1[2];
#pragma unroll
    for (int i = 0; i < 2; ++i) {
      const u32x4 z = *(LAS u32x4*)(sm + (4 * (dc >> 2) + 2) * KCP + 32 + 2048 * ((dc >> 1) & 1) + 32 * tok + 16 * (dc & 1) + 1024 * i), x = xres[i];
      s0[i] = (f32x4){bflo(x[0]) + bflo(z[0]), bfhi(x[0]) + bfhi(z[0]), bflo(x[1]) + bflo(z[1]), bfhi(x[1]) + bfhi(z[1])};
      s1[i] = (f32x4){bflo(x[2]) + bflo(z[2]), bfhi(x[2]) + bfhi(z[2]), bflo(x[3]) + bflo(z[3]), bfhi(x[3]) + bfhi(z[3])};
    }
    u32 t0 = RB + UNIT + UNIT * 2 * (dc >> 2) + tok * 128 + (((2 * (dc & 3)) ^ (tok & 7)) << 4), t1 = t0 ^ 16u;
    char* op = (char*)p.out + vo;
    asm volatile("" : "+v"(s0[0]), "+v"(s0[1]), "+v"(s1[0]), "+v"(s1[1]), "+v"(t0), "+v"(t1), "+v"(op));
    STAMP_AT(12);
    BARRIER();
    STAMP_AT(13);
#pragma unroll
    for (int i = 0; i < 2; ++i) {
#pragma unroll
      for (int g = 0; g < 4; ++g) { s0[i] += *(LAS f32x4*)(sm + t0 + UNIT * i + 4096 * g); s1[i] += *(LAS f32x4*)(sm + t1 + UNIT * i + 4096 * g); }
      *(GAS u32x4*)(op + 32 * D * 2 * i) = (u32x4){pk(s0[i][0], s0[i][1]), pk(s0[i][2], s0[i][3]), pk(s1[i][0], s1[i][1]), pk(s1[i][2], s1[i][3])};
    }
  }
  if (xcd == 0) HOLD(1, 4000) BURN(5)
  STAMP_AT(14); STAMP_OUT();
}

extern "C" void kernel_launch(void* const* d_in, const int* in_sizes, int n_in, void* d_out, int out_size, void* d_ws,
                              size_t ws_size, hipStream_t stream) {
  Params p;
  p.x = (const u16*)d_in[0]; p.are = (const u16*)d_in[1]; p.aim = (const u16*)d_in[2];
  p.bre = (const u16*)d_in[3]; p.bim = (const u16*)d_in[4]; p.cre = (const u16*)d_in[5];
  p.cim = (const u16*)d_in[6]; p.logdt = (const u16*)d_in[7]; p.dskip = (const u16*)d_in[8];
  p.wg = (const u16*)d_in[10]; p.wu = (const u16*)d_in[11]; p.wd = (const u16*)d_in[12];
  p.out = (u16*)d_out; p.ws = (char*)d_ws;
  s4ffn_kernel<<<256, 512, 0, stream>>>(p);
}
```

```cpp
#include <hip/hip_runtime.h>
#include <stdint.h>

typedef unsigned short u16;
typedef unsigned u32;
typedef unsigned long long u64;
typedef __bf16 bf16x2 __attribute__((ext_vector_type(2)));
typedef __bf16 bf16x4 __attribute__((ext_vector_type(4)));
typedef __bf16 bf16x8 __attribute__((ext_vector_type(8)));
typedef float f32x2 __attribute__((ext_vector_type(2)));
typedef float f32x4 __attribute__((ext_vector_type(4)));
typedef float f32x16 __attribute__((ext_vector_type(16)));
typedef u32 u32x2 __attribute__((ext_vector_type(2)));
typedef u32 u32x4 __attribute__((ext_vector_type(4)));
#define LAS __attribute__((address_space(3)))
#define GAS __attribute__((address_space(1)))
#define DI __device__ __forceinline__
typedef __amdgpu_buffer_rsrc_t rsrc_t;

constexpr int L = 2048, D = 512, NM = 128, F = 1792;
constexpr u32 WS_H = 0, WS_U = 8u << 20, WS_ZT = 10u << 20, WS_ST = 14u << 20, WS_CS = WS_ST + 65536, WS_FL = 15u << 20, WS_K = 16u << 20, WS_END = 24u << 20;
constexpr u32 FL_E = WS_FL, FL_1 = WS_FL + 4096, FL_2 = WS_FL + 131072, FS = 128, FL_2B = 131072;
constexpr u32 KCP = 4160, UNIT = 16384, RB = 32768, RING = 8 * UNIT;
constexpr u32 L_U = 8 * KCP;
constexpr u32 L_ST = L_U + 9216;
constexpr u32 L_CS = L_ST + 1024;
constexpr u32 S_FRE = 4128, S_HP = 4144, S_GP = KCP + 4128, S_H2 = KCP + 4144;
constexpr u32 CODE_BYTES = 24576;
constexpr u32 SPIN_CAP = 1u << 16;

struct Params {
  const u16 *x, *are, *aim, *bre, *bim, *cre, *cim, *logdt, *dskip, *wg, *wu, *wd;
  u16* out;
  char* ws;
};

#define STAMP_DECL
#define STAMP_AT(k)
#define STAMP_RT(v)
#define STAMP_NW(k)
#define STAMP_NWEND()
#define STAMP_OUT()

DI float bflo(u32 v) { return __builtin_bit_cast(float, v << 16); }
DI float bfhi(u32 v) { return __builtin_bit_cast(float, v & 0xffff0000u); }
DI u32 pk(float a, float b) { f32x2 f = {a, b}; return __builtin_bit_cast(u32, __builtin_convertvector(f, bf16x2)); }
DI float rbf(float a) { return bflo(pk(a, a)); }
DI void dma4(rsrc_t ra, rsrc_t rb, u32 lds, u32 v0, u32 v1, u32 v2, u32 v3, u32 soff) {
  asm volatile("s_mov_b32 m0, %0\n\ts_nop 0\n\tbuffer_load_dwordx4 %1, %5, %7 offen lds\n\tbuffer_load_dwordx4 %2, %5, %7 offen offset:1024 lds\n\t"
               "buffer_load_dwordx4 %3, %6, %7 offen offset:2048 lds\n\tbuffer_load_dwordx4 %4, %6, %7 offen offset:3072 lds"
               :: "s"(lds), "v"(v0), "v"(v1), "v"(v2), "v"(v3), "s"(ra), "s"(rb), "s"(soff));
}
DI void dma4s(rsrc_t r, u32 lds, u32 v0, u32 v1, u32 v2, u32 v3, u32 soff) {
  asm volatile("s_mov_b32 m0, %0\n\ts_nop 0\n\tbuffer_load_dwordx4 %1, %5, %6 offen sc1 lds\n\tbuffer_load_dwordx4 %2, %5, %6 offen offset:1024 sc1 lds\n\t"
               "buffer_load_dwordx4 %3, %5, %6 offen offset:2048 sc1 lds\n\tbuffer_load_dwordx4 %4, %5, %6 offen offset:3072 sc1 lds"
               :: "s"(lds), "v"(v0), "v"(v1), "v"(v2), "v"(v3), "s"(r), "s"(soff));
}
DI void dma1s(rsrc_t r, u32 lds, u32 v0, u32 soff) {
  asm volatile("s_mov_b32 m0, %0\n\ts_nop 0\n\tbuffer_load_dwordx4 %1, %2, %3 offen sc1 lds" :: "s"(lds), "v"(v0), "s"(r), "s"(soff));
}
#define WAIT_V(n) asm volatile("s_waitcnt vmcnt(" #n ")" ::: "memory")
#define WAIT_L() asm volatile("s_waitcnt lgkmcnt(0)" ::: "memory")
#define BARRIER() asm volatile("s_waitcnt lgkmcnt(0)\n\ts_barrier" ::: "memory")
#define FENCE() asm volatile("" ::: "memory")
#define SCHED() __builtin_amdgcn_sched_barrier(0)
DI bf16x8 ldfrag(LAS char* a) { return *(LAS bf16x8*)a; }
DI bf16x8 trfrag(LAS char* a0, LAS char* a1) {
  bf16x4 lo = __builtin_amdgcn_ds_read_tr16_b64_v4bf16((LAS bf16x4*)a0);
  bf16x4 hi = __builtin_amdgcn_ds_read_tr16_b64_v4bf16((LAS bf16x4*)a1);
  return __builtin_shufflevector(lo, hi, 0, 1, 2, 3, 4, 5, 6, 7);
}
#define MFMA32(a, b, c) __builtin_amdgcn_mfma_f32_32x32x16_bf16(a, b, c, 0, 0, 0)
#define MFMA16(a, b, c) __builtin_amdgcn_mfma_f32_16x16x32_bf16(a, b, c, 0, 0, 0)
DI u32x2 ld8_sc1(rsrc_t r, u32 voff, u32 soff) { return __builtin_amdgcn_raw_buffer_load_b64(r, voff, soff, 16); }
DI u32x4 ld16_sc1(rsrc_t r, u32 voff, u32 soff) { return __builtin_amdgcn_raw_buffer_load_b128(r, voff, soff, 16); }
DI void st8_sc1(rsrc_t r, u32x2 v, u32 voff, u32 soff) { __builtin_amdgcn_raw_buffer_store_b64(v, r, voff, soff, 16); }
DI void st16_sc1(rsrc_t r, u32x4 v, u32 voff) { __builtin_amdgcn_raw_buffer_store_b128(v, r, voff, 0, 16); }
template <int SLEEP> DI void wait_flags(rsrc_t r, u32 voff, u32 tlo, u32 thi) {
#pragma unroll 1
  for (u32 s = 0; s < SPIN_CAP; ++s) {
    FENCE();
    u32x2 f = ld8_sc1(r, voff, 0);
    if (__all(f.x == tlo && f.y == thi)) break;
    __builtin_amdgcn_s_sleep(SLEEP);
  }
  WAIT_V(0);
}
DI void post(LAS char* word, u32 v) {
  asm volatile("s_mov_b64 exec, 1\n\tds_write_b32 %0, %1\n\ts_mov_b64 exec, -1" :: "v"(word), "v"(v) : "memory");
}
DI u32 peek(LAS char* word) { return *(volatile LAS u32*)word; }
DI void await(LAS char* word, u32 seen, u32 need) {
  if (__builtin_expect(__any(seen < need), 0)) {
#pragma unroll 1
    for (u32 s = 0; s < SPIN_CAP * 16; ++s) { __builtin_amdgcn_s_sleep(1); if (!__any(peek(word) < need)) break; }
  }
  FENCE();
}
#define DPPADD(name, ctl) DI float name(float x) { float r; asm("s_nop 1\n\tv_add_f32_dpp %0, %1, %1 " ctl " row_mask:0xf bank_mask:0xf" : "=v"(r) : "v"(x)); return r; }
DPPADD(add_x1, "quad_perm:[1,0,3,2]") DPPADD(add_x2, "quad_perm:[2,3,0,1]") DPPADD(add_m8, "row_half_mirror") DPPADD(add_x8, "row_ror:8")
DI float fold32(float a, float b) {
  asm("s_nop 1\n\tv_permlane32_swap_b32 %0, %1\n\tv_add_f32 %0, %0, %1" : "+v"(a), "+v"(b)); return a;
}
DI float fold16(float a, float b) {
  asm("s_nop 1\n\tv_permlane16_swap_b32 %0, %1\n\tv_add_f32 %0, %0, %1" : "+v"(a), "+v"(b)); return a;
}
DI u32 vmov(u32 a) { u32 r; asm("v_mov_b32 %0, %1" : "=v"(r) : "v"(a)); return r; }
struct cpx { float r, i; };
struct cp2 { f32x2 r, i; };
DI cpx cmul(cpx a, cpx b) { return {a.r * b.r - a.i * b.i, a.r * b.i + a.i * b.r}; }
DI cpx csq(cpx a) { return {a.r * a.r - a.i * a.i, (a.r + a.r) * a.i}; }
DI cp2 cmul2(cp2 a, cpx b) { return {a.r * b.r - a.i * b.i, a.i * b.r + a.r * b.i}; }
DI cpx cexpl(float ar2, float air, float l) {
  float m = __builtin_amdgcn_exp2f(ar2 * l), f = __builtin_amdgcn_fractf(air * l);
  return {m * __builtin_amdgcn_cosf(f), m * __builtin_amdgcn_sinf(f)};
}

__global__ __launch_bounds__(512, 2) void s4ffn_kernel(Params p) {
  __shared__ __attribute__((aligned(16))) char smem[163840];
  LAS char* sm = (LAS char*)smem;
  const u32 smb = (u32)(size_t)sm;
  const int tid = threadIdx.x, lane = tid & 63;
  const int w = __builtin_amdgcn_readfirstlane(tid >> 6), v = w & 3;
  const int b = blockIdx.x, xcd = b & 7, o = b >> 3;
  u32 cpf;
  {
    u64 pc;
    asm volatile("s_getpc_b64 %0" : "=s"(pc));
    const u32 off = (8 * o + w) * 128;
    asm volatile("s_load_dword %0, %1, 0\n\ts_load_dword %0, %1, 64" : "=&s"(cpf) : "s"((pc & ~127ull) + (off < CODE_BYTES ? off : 0)));
    SCHED();
  }
  const bool aux = o >= 28;
  const int ma = xcd >> 1, fh = xcd & 1;
  const int il = o / 7, jl = o - il * 7;
  const int mi = 4 * ma + il, fj = 7 * fh + jl;
  const int ti2 = 4 * xcd + (o >> 3), n2 = o & 7;
  const int tb = 32 * xcd + o, ch0 = 16 * o + xcd;
  const int hh = lane >> 5, blk = (lane >> 4) & 1, q4 = (lane >> 2) & 3, p4 = lane & 3, g4 = lane >> 4;
  STAMP_DECL; STAMP_RT(rt0_); STAMP_AT(0);
#define HOLD(id, ticks)
#define BURN(id)
  BURN(4)
  const rsrc_t rx = __builtin_amdgcn_make_buffer_rsrc((void*)p.x, 0, L * D * 2, 0x00020000);
  const rsrc_t rwg = __builtin_amdgcn_make_buffer_rsrc((void*)p.wg, 0, D * F * 2, 0x00020000);
#define ROT (2 * ma)
#define ROT2 0
  const rsrc_t rws = __builtin_amdgcn_make_buffer_rsrc((void*)p.ws, 0, WS_END, 0x00020000);
  u64 ev;
#define LOAD_EPOCH() asm volatile("s_load_dwordx2 %0, %1, 0\n\ts_waitcnt lgkmcnt(0)" : "=&s"(ev) : "s"(p.ws + FL_E) : "memory")
#define MAKE_TAG() const u32 tlo = (u32)(ev + 0x9E3779B97F4A7C15ull), thi = (u32)((ev + 0x9E3779B97F4A7C15ull) >> 32), tg32 = tlo ^ thi; const u32x2 tag = {tlo, thi}
  asm volatile("s_waitcnt lgkmcnt(0)" :: "s"(cpf), "s"(mi), "s"(fj), "s"(ti2), "s"(ch0));
  LAS char* const sy = sm + 4 * p4;

  if (w < 4) {
  if (w < 2) {
    if (!aux) {
      const rsrc_t rwu2 = __builtin_amdgcn_make_buffer_rsrc((void*)((const char*)p.wu - 2048), 0, D * F * 2 + 2048, 0x00020000);
      const u32 vx0 = (lane >> 3) * 1024 + (((lane & 7) ^ g4) << 4), vx1 = (vx0 ^ 64) + 7168, vx2 = vx0 + 14336, vx3 = vx1 + 14336;
      const u32 vg0 = g4 * (F * 2) + (((((lane & 15) >> 1) ^ g4) << 5) | ((lane & 1) << 4)), vg1 = vg0 + 4 * F * 2 - 1024;
      const u32 vu0 = vg0 ^ 128, vu1 = vu0 + 4 * F * 2 - 1024;
      const u32 sx0 = (mi * 128 + 64 * v) * 1024, sw0 = 16 * v * (F * 2) + fj * 256;
      u32 dst = 8192 * v;
      auto ux = [&](u32 c) {
        const u32 s = sx0 + ((ROT + c) & 7) * 128;
        dma4(rx, rx, smb + RB + dst, vx0, vx1, vx2, vx3, s); dma4(rx, rx, smb + RB + dst + 4096, vx0, vx1, vx2, vx3, s + 32768);
        dst = (dst + UNIT) & (RING - 1);
      };
      auto uw = [&](u32 c, u32 h) {
        const u32 s = sw0 + (2 * ((ROT + c) & 7) + h) * (32 * F * 2);
        dma4(rwg, rwu2, smb + RB + dst, vg0, vg1, vu0, vu1, s); dma4(rwg, rwu2, smb + RB + dst + 4096, vg0, vg1, vu0, vu1, s + 8 * F * 2);
        dst = (dst + UNIT) & (RING - 1);
      };
#define LDELAY 2
      __builtin_amdgcn_s_sleep(LDELAY);
      ux(0); uw(0, 0);
      LOAD_EPOCH();
#pragma unroll 1
      for (int c = 0; c < 7; ++c) {
        uw(c, 1); ux(c + 1); uw(c + 1, 0);
        WAIT_V(16);
        if (c == 0) STAMP_AT(1);
        __builtin_amdgcn_s_barrier();
      }
      uw(7, 1);
      dma1s(rws, smb + L_ST, lane * 16, WS_ST + mi * 1024);
      dma1s(rws, smb + L_CS + 1024 * v, lane * 16, WS_CS + (v * F + fj * 128) * 8);
      STAMP_AT(2);
    } else {
      LOAD_EPOCH();
      const rsrc_t rp = __builtin_amdgcn_make_buffer_rsrc((void*)p.wd, 0, D * F * 2, 0x00020000);
#define PFN 0
      u32 t = 0;
#pragma unroll 1
      for (int q = 0; q < 14; ++q) {
        if (q == 0 || q == 4) __builtin_amdgcn_s_barrier();
        const u32 so = (28 * (o - 28) + 4 * (q % 7) + 2 * (q / 7) + v) * (16 * D * 2);
        if (q < PFN) asm volatile("s_waitcnt vmcnt(2)\n\tbuffer_load_dword %0, %1, %2, %3 offen\n\tbuffer_load_dword %0, %1, %2, %4 offen" : "+v"(t) : "v"(lane * 128), "s"(rp), "s"(so), "s"(so + 8192));
      }
      asm volatile("s_waitcnt vmcnt(0)" : "+v"(t));
      STAMP_AT(2);
    }
  }
    const rsrc_t rwd = __builtin_amdgcn_make_buffer_rsrc((void*)p.wd, 0, D * F * 2, 0x00020000);
    const int rot2 = ROT2;
    if (v >= 2) {
      const int k = v - 2;
      auto bar = [&] { SCHED(); if (!aux) __builtin_amdgcn_s_barrier(); FENCE(); SCHED(); };
      auto pull = [&](int i, int skip) {
        u32 d;
        asm volatile(".set pf_off, 0\n\t.rept 14\n\ts_load_dword %0, %1, pf_off\n\t.set pf_off, pf_off + 64\n\t.endr\n\t"
                     "s_cmp_ge_i32 %2, 28\n\ts_cbranch_scc1 1f\n\ts_barrier\n1:\n\ts_waitcnt lgkmcnt(0)"
                     : "=&s"(d) : "s"((const char*)p.wd + tb * 7168 + 896 * i), "s"(skip) : "scc", "memory");
      };
      auto kcopy = [&](int q) { dma4s(rws, smb + q * KCP + 32, lane * 16, lane * 16, lane * 16, lane * 16, WS_K + b * 16896 + 128 + 8448 * (q >> 2) + 4224 * (q & 1) - 2 * (q & 2)); };
      if (k && lane >= 2 && lane < 32) *(LAS u32x4*)(sm + KCP * (lane >> 2) - 32 + 16 * p4) = (u32x4)(0u);
      if (k == 0) {
        u32x4 xr[8];
#pragma unroll
        for (int i = 0; i < 8; ++i) xr[i] = __builtin_amdgcn_raw_buffer_load_b128(rx, lane * 16 + 1024 * (i & 3), (8 * tb + (i & 4)) * 1024, 0);
        LOAD_EPOCH();
        bar();
        float x[8][8], su[8], sq[8];
#pragma unroll
        for (int r = 0; r < 8; ++r) {
#pragma unroll
          for (int e = 0; e < 4; ++e) { x[r][2 * e] = bflo(xr[r][e]); x[r][2 * e + 1] = bfhi(xr[r][e]); }
          asm("v_add_f32 %0, %2, %3\n\tv_mul_f32 %1, %2, %2\n\tv_add_f32 %0, %0, %4\n\tv_fmac_f32 %1, %3, %3\n\tv_add_f32 %0, %0, %5\n\tv_fmac_f32 %1, %4, %4\n\t"
              "v_add_f32 %0, %0, %6\n\tv_fmac_f32 %1, %5, %5\n\tv_add_f32 %0, %0, %7\n\tv_fmac_f32 %1, %6, %6\n\tv_add_f32 %0, %0, %8\n\tv_fmac_f32 %1, %7, %7\n\t"
              "v_add_f32 %0, %0, %9\n\tv_fmac_f32 %1, %8, %8\n\tv_fmac_f32 %1, %9, %9"
              : "=&v"(su[r]), "=&v"(sq[r])
              : "v"(x[r][0]), "v"(x[r][1]), "v"(x[r][2]), "v"(x[r][3]), "v"(x[r][4]), "v"(x[r][5]), "v"(x[r][6]), "v"(x[r][7]));
        }
        STAMP_AT(1);
        bar();
        MAKE_TAG();
        auto total = [&](const float (&a)[8]) {
          const float lo = add_x8(fold16(fold32(a[0], a[2]), fold32(a[1], a[3]))), hi = add_x8(fold16(fold32(a[4], a[6]), fold32(a[5], a[7])));
          return add_m8(add_x2(add_x1(lane & 8 ? hi : lo)));
        };
        const float mean = total(su) * (1.f / D), nsq = -mean * mean;
        const float var = rbf(__builtin_fmaxf(__builtin_fmaf(total(sq), 1.f / D, nsq), 0.f));
        const u32 gran = pk(mean, __builtin_amdgcn_sqrtf(rbf(var + 1e-5f)));
        const float r = __builtin_amdgcn_rcpf(bfhi(gran)), mr = -bflo(gran) * r;
        if ((lane & 7) == 0) st8_sc1(rws, (u32x2){gran, tg32}, 8 * g4 + 4 * (lane & 8), WS_ST + 64 * tb);
        u32 n[8][4];
#pragma unroll
        for (int i = 0; i < 4; ++i) {
          if (i == 2) bar();
          const int l0 = 16 * (2 * i & 3) + 8 * (i >> 1);
          const u32 ra = __builtin_amdgcn_readlane(__builtin_bit_cast(u32, r), l0), rb = __builtin_amdgcn_readlane(__builtin_bit_cast(u32, r), l0 + 16);
          u32 ma_ = __builtin_amdgcn_readlane(__builtin_bit_cast(u32, mr), l0), mb_ = __builtin_amdgcn_readlane(__builtin_bit_cast(u32, mr), l0 + 16);
          asm volatile("s_nop 1" : "+v"(ma_), "+v"(mb_) : "s"(ra), "s"(rb));
#pragma unroll
          for (int e = 0; e < 8; ++e)
            asm("v_fma_f32 %1, %1, %3, %5\n\tv_fma_f32 %2, %2, %4, %6\n\tv_cvt_pk_bf16_f32 %0, %1, %2"
                : "=v"(n[e][i]), "+v"(x[2 * i][e]), "+v"(x[2 * i + 1][e]) : "s"(ra), "s"(rb), "v"(ma_), "v"(mb_));
        }
#pragma unroll
        for (int e = 0; e < 8; ++e) st16_sc1(rws, (u32x4){n[e][0], n[e][1], n[e][2], n[e][3]}, WS_U + tb * 8192 + lane * 16 + 1024 * e);
        SCHED(); __builtin_amdgcn_s_barrier();
        WAIT_V(0);
        if (lane == 0) st8_sc1(rws, tag, FL_1 + tb * 8, 0);
        STAMP_AT(2);
        pull(6, o - 16); pull(7, o);
      } else {
        const u32 dsk0 = p.dskip[ch0], dsk1 = p.dskip[ch0 + 8];
        LOAD_EPOCH();
        pull(0, o - 16); pull(1, o - 16);
        const u32 sk0 = WS_K + b * 16896 + 128;
#pragma unroll 1
        for (int j = 0; j < 2; ++j) {
        const u32 sk = sk0 + 8448 * j, dsk = j ? dsk1 : dsk0;
        float kv[16];
#pragma unroll
        for (int g = 0; g < 4; ++g) {
          LAS char* s = sm + 2 * KCP * j + 32 + 1024 * g + 16 * lane;
          const f32x4 a = *(LAS f32x4*)s, d = *(LAS f32x4*)(s + KCP);
#pragma unroll
          for (int e = 0; e < 4; ++e) asm("v_add_f32 %0, %1, %2" : "=v"(kv[4 * g + e]) : "v"(a[e]), "v"(d[e]));
        }
        const bool odd = lane & 1;
        if (lane == 0) kv[0] += bflo(dsk);
        u32x4 kc[4];
        kc[0] = (u32x4){pk(kv[0], kv[1]), pk(kv[2], kv[3]), pk(kv[4], kv[5]), pk(kv[6], kv[7])};
        kc[1] = (u32x4){pk(kv[8], kv[8]), pk(kv[8], kv[9]), pk(kv[9], kv[9]), pk(kv[10], kv[10])};
        kc[2] = (u32x4){pk(kv[11], kv[11]), pk(kv[11], kv[11]), pk(odd ? kv[12] : kv[11], kv[12]), pk(kv[12], kv[12])};
        kc[3] = (u32x4){pk(kv[13], kv[13]), pk(kv[13], kv[13]), pk(odd ? kv[14] : kv[13], kv[14]), pk(kv[14], kv[14])};
#pragma unroll
        for (int g = 0; g < 4; ++g) {
          const u32x4 cu = kc[g];
          int py = 0;
          if (g) py = __builtin_amdgcn_update_dpp(0, (int)kc[g ? g - 1 : 0].w, 0x13C, 0xf, 0xf, false);
          py = __builtin_amdgcn_update_dpp(py, (int)cu.w, 0x138, 0xf, 0xf, false);
          const u32x4 sh = {__builtin_amdgcn_alignbit(cu.x, py, 16), __builtin_amdgcn_alignbit(cu.y, cu.x, 16), __builtin_amdgcn_alignbit(cu.z, cu.y, 16), __builtin_amdgcn_alignbit(cu.w, cu.z, 16)};
          __builtin_amdgcn_raw_buffer_store_b128(cu, rws, sk + lane * 16 + 1024 * g, 0, 0);
          __builtin_amdgcn_raw_buffer_store_b128(sh, rws, sk + 4224 + lane * 16 + 1024 * g, 0, 0);
        }
        if (lane < 2) __builtin_amdgcn_raw_buffer_store_b32(0, rws, sk - 4 + 4224 * lane, 0, 0);
        pull(2 + j, o);
        }
        STAMP_AT(3);
        WAIT_V(0);
        kcopy(0); kcopy(1); kcopy(2);
        pull(4, o);
        kcopy(3); kcopy(4);
        pull(5, o);
        kcopy(5); kcopy(6);
      }
      MAKE_TAG();
      const u32 vf1 = FL_1 + (128 * k + 2 * lane) * 8;
      u32x4 f1 = ld16_sc1(rws, vf1, 0);
      const u32 vu0 = (lane >> 1) * 8192 + ((lane ^ g4) & 1) * 16, us = 32 * 8192 - 1024;
      bar();
#define QDELAY 3
      __builtin_amdgcn_s_sleep(QDELAY);
#pragma unroll 1
      for (u32 s = 0; s < SPIN_CAP && !__all(f1.x == tlo && f1.y == thi && f1.z == tlo && f1.w == thi); ++s) {
        FENCE();
        f1 = ld16_sc1(rws, vf1, 0);
        __builtin_amdgcn_s_sleep(16);
      }
      FENCE();
      STAMP_AT(4);
      dma4s(rws, smb + L_U + 1024 + 4096 * k, vu0, vu0 + us, vu0 + 2 * us, vu0 + 3 * us, WS_U + k * 128 * 8192 + tb * 32);
      if (k == 0) dma1s(rws, smb + L_U, lane * 16, 0x40000000u);
      else kcopy(7);
      STAMP_AT(5);
    }
    MAKE_TAG();
    const u32 dq = smb + RB + 4096 * v;
    const u32 vd0 = (lane >> 3) * (D * 2) + (((lane & 7) ^ ((lane >> 2) & 4)) << 4), vd1 = vd0 + 7168, vd2 = vd0 + 14336, vd3 = vd0 + 21504;
    auto ks_of = [&](int q) { int k = q % 7 + rot2; k = k >= 7 ? k - 7 : k; return 28 * v + 4 * k + 2 * (q / 7); };
    auto wdq = [&](int q, int slot) { dma4(rwd, rwd, dq + slot * UNIT, vd0, vd1, vd2, vd3, ks_of(q) * (16 * D * 2) + n2 * 128); };
    const u32 wo0 = RB + 4096 * v + (4 * hh + q4) * 128 + p4 * 8 + ((blk ^ (q4 & 2)) << 5), wo1 = wo0 ^ 64;
    auto wfrag = [&](int slot, int e, int ct) { LAS char* a = sm + slot * UNIT + 2048 * e + (ct ? wo1 : wo0); return trfrag(a, a + 1024); };
    bf16x8 wr_[14][2];
    auto take = [&](int q, int slot) {
#pragma unroll
      for (int e = 0; e < 4; ++e) wr_[2 * q + (e >> 1)][e & 1] = wfrag(slot, e >> 1, e & 1);
    };
    wdq(0, 1);
    if (v < 2) WAIT_V(4);
    STAMP_AT(6);
    if (!aux) __builtin_amdgcn_s_barrier();
    wdq(1, 2); wdq(2, 3); wdq(3, 4);
#define SWAP_UNIT(qa, slot, qb) do { WAIT_V(12); take(qa, slot); WAIT_L(); SCHED(); wdq(qb, slot); } while (0)
    SWAP_UNIT(0, 1, 4); SWAP_UNIT(1, 2, 5); SWAP_UNIT(2, 3, 6); SWAP_UNIT(3, 4, 10);
    SWAP_UNIT(4, 1, 7); SWAP_UNIT(5, 2, 8); SWAP_UNIT(6, 3, 9);
    STAMP_AT(7);
    if (!aux) await(sy + S_FRE, peek(sy + S_FRE), 8);
    if (v >= 2) post(sm + S_HP + 4 * (v - 2), 5);
    STAMP_AT(8);
    wdq(11, 5); wdq(12, 6); wdq(13, 7);
#define HDELAY 8
    if (!aux) { await(sy + S_GP, peek(sy + S_GP), 1); __builtin_amdgcn_s_sleep(HDELAY); }
    STAMP_AT(9);
    const u32 vf2 = FL_2 + (ti2 * 28 + 7 * v + (lane < 7 ? lane : 0)) * FS;
    wait_flags<8>(rws, vf2, tlo, thi);
    STAMP_AT(10);
    f32x16 acc[2][2];
#pragma unroll
    for (int a = 0; a < 4; ++a) acc[a >> 1][a & 1] = (f32x16)(0.f);
    constexpr int PD = 4;
    u32x4 hf[28][2];
    auto hload = [&](int i) {
      const u32 so = (ti2 * 112 + ks_of(i >> 1)) * 2048;
#define HAUX 16
      hf[i][0] = __builtin_amdgcn_raw_buffer_load_b128(rws, lane * 16 + 2048 * (i & 1), so, HAUX);
      hf[i][1] = __builtin_amdgcn_raw_buffer_load_b128(rws, lane * 16 + 2048 * (i & 1) + 1024, so, HAUX);
    };
#pragma unroll
    for (int i = 0; i < PD; ++i) hload(i);
    bf16x8 wl[2][2];
    u32x2 fb;
    SCHED();
#pragma unroll
    for (int i = 0; i < 28; ++i) {
      if (i + 1 >= 14 && i + 1 < 28) { wl[(i + 1) & 1][0] = wfrag((i - 11) >> 1, (i + 1) & 1, 0); wl[(i + 1) & 1][1] = wfrag((i - 11) >> 1, (i + 1) & 1, 1); }
      const bf16x8 a0 = i < 14 ? wr_[i < 14 ? i : 0][0] : wl[i & 1][0], a1 = i < 14 ? wr_[i < 14 ? i : 0][1] : wl[i & 1][1];
      const bf16x8 b0 = __builtin_bit_cast(bf16x8, hf[i][0]), b1 = __builtin_bit_cast(bf16x8, hf[i][1]);
      acc[0][0] = MFMA32(a0, b0, acc[0][0]);
      acc[1][0] = MFMA32(a1, b0, acc[1][0]);
      acc[0][1] = MFMA32(a0, b1, acc[0][1]);
      acc[1][1] = MFMA32(a1, b1, acc[1][1]);
      if (i == 6) STAMP_AT(15);
      if (i == 13) STAMP_AT(16);
      if (i == 20) STAMP_AT(17);
      SCHED();
#define ZAT 14
      if (i == ZAT) post(sm + S_H2 + 4 * v, 1);
      if (i == 2) fb = ld8_sc1(rws, vf2, FL_2B);
      if (i == 6) {
        if (__builtin_expect(!__all(fb.x == tlo && fb.y == thi), 0)) wait_flags<4>(rws, vf2 + FL_2B, tlo, thi);
        FENCE();
      }
      if (i < 4) { hload(PD + 2 * i); hload(PD + 2 * i + 1); }
      else if (i + PD + 4 < 28) hload(i + PD + 4);
      SCHED();
    }
    STAMP_AT(11);
#pragma unroll
    for (int g = 0; g < 4; ++g) {
      LAS char* t = sm + RB + UNIT + 4096 * v + (lane & 31) * 128 + (((2 * g + hh) ^ (lane & 7)) << 4);
#pragma unroll
      for (int a = 0; a < 4; ++a) {
        const f32x16& s = acc[a >> 1][a & 1];
        *(LAS f32x4*)(t + UNIT * a) = (f32x4){s[4 * g], s[4 * g + 1], s[4 * g + 2], s[4 * g + 3]};
      }
    }
    STAMP_AT(12);
    BARRIER();
  } else {
    __builtin_amdgcn_s_setprio(1);
    const int c = v >> 1, ah = v & 1, ch = ch0 + 8 * c;
    const int gi = ch * NM + 64 * ah + lane;
    const u32 r6 = p.logdt[ch], r0 = p.are[gi], r1 = p.aim[gi], r2 = p.bre[gi], r3 = p.bim[gi], r4 = p.cre[gi], r5 = p.cim[gi];
    SCHED();
    const int tg = c, fq = ah;
    u32 xa[4], wa[4], iw[4], ir[2];
    {
      const u32 row = 64 * tg + (lane & 31), f = (row >> 1) & 7, scr = RB + 5 * UNIT + 4096 * v, cki = 2 * blk + (p4 >> 1);
#pragma unroll
      for (int s = 0; s < 4; ++s) {
        xa[s] = RB + row * 128 + (((2 * s + hh) ^ f) << 4);
        wa[s] = RB + hh * 4096 + blk * 2048 + q4 * 256 + (((4 * fq + s) ^ (q4 + 4 * blk)) << 5) + p4 * 8;
        iw[s] = scr + lane * 64 + ((s ^ ((lane >> 1) & 3)) << 4);
        asm volatile("" : "+v"(xa[s]), "+v"(wa[s]), "+v"(iw[s]));
      }
      ir[0] = scr + (8 * hh + q4) * 64 + ((cki ^ (q4 >> 1)) << 4) + 8 * (p4 & 1);
      ir[1] = scr + (8 * hh + 4 + q4) * 64 + ((cki ^ (2 + (q4 >> 1))) << 4) + 8 * (p4 & 1);
      asm volatile("" : "+v"(ir[0]), "+v"(ir[1]));
    }
    LOAD_EPOCH();
    MAKE_TAG();
    SCHED();
    if (aux) {
      const int job = 4 * ma + (o - 28), mat = job >= 7, cg = 7 * fh + job - 7 * mat;
      float s[8];
#pragma unroll
      for (int e = 0; e < 8; ++e) s[e] = 0.f;
      if (job < 14) {
        const rsrc_t rw = __builtin_amdgcn_make_buffer_rsrc((void*)(mat ? p.wu : p.wg), 0, D * F * 2, 0x00020000);
        u32 k0 = 0x00003f80u, k1 = 0x3f800000u;
        asm("" : "+v"(k0), "+v"(k1));
        const bf16x2 s0 = __builtin_bit_cast(bf16x2, k0), s1 = __builtin_bit_cast(bf16x2, k1);
        u32x4 t[8];
        const u32 vw = g4 * (F * 2) + (lane & 15) * 16;
        u32 so = 128 * v * (F * 2) + cg * 256;
#pragma unroll
        for (int i = 0; i < 8; ++i) t[i] = __builtin_amdgcn_raw_buffer_load_b128(rw, vw, so + 4 * i * (F * 2), 0);
#pragma unroll 1
        for (int j = 0; j < 4; ++j) {
          so = j < 3 ? so + 32 * (F * 2) : 0x40000000u;
#pragma unroll
          for (int i = 0; i < 8; ++i) {
#pragma unroll
            for (int e = 0; e < 4; ++e) {
              const u32 d = t[i][e];
              s[2 * e] = __builtin_amdgcn_fdot2_f32_bf16(__builtin_bit_cast(bf16x2, d), s0, s[2 * e], false);
              s[2 * e + 1] = __builtin_amdgcn_fdot2_f32_bf16(__builtin_bit_cast(bf16x2, d), s1, s[2 * e + 1], false);
            }
            t[i] = __builtin_amdgcn_raw_buffer_load_b128(rw, vw, so + 4 * i * (F * 2), 0);
            SCHED();
          }
        }
      }
      const float a0 = fold16(fold32(s[0], s[4]), fold32(s[2], s[6])), a1 = fold16(fold32(s[1], s[5]), fold32(s[3], s[7]));
      *(LAS f32x2*)(sm + L_CS + 512 * v + 32 * (lane & 15) + 8 * g4) = (f32x2){a0, a1};
      BARRIER();
      LAS char* cx = sm + L_CS + 128 * v + 4 * (lane & 31);
      const float tot = (*(LAS float*)cx + *(LAS float*)(cx + 512)) + (*(LAS float*)(cx + 1024) + *(LAS float*)(cx + 1536)), th = rbf(tot);
      if (job < 14 && lane < 32) st8_sc1(rws, (u32x2){pk(th, tot - th), tg32}, lane * 8, WS_CS + (mat * F + cg * 128 + 32 * v) * 8);
      STAMP_AT(15);
    }
    bf16x8 yf[8], xf[8];
    {
      const float dt = rbf(__builtin_amdgcn_exp2f(bflo(r6) * 1.44269504088896f));
      const float ar2 = dt * bflo(r0) * 1.44269504088896f, air = dt * bflo(r1) * 0.159154943091895f;
      auto put = [&](int img, int g, u32x4 re, u32x4 im) {
        *(LAS u32x4*)(sm + iw[g] + 2 * img * UNIT) = re; *(LAS u32x4*)(sm + iw[g] + (1 - img) * UNIT) = im;
        SCHED();
      };
      auto get = [&](int img, int s) { const u32 d = (s < 4 ? 2 * img : 1 - img) * UNIT + 1024 * (s & 3); return trfrag(sm + ir[0] + d, sm + ir[1] + d); };
#define P2_(a) pk((a).x, (a).y)
#define P4_(a, b, c, d) (u32x4){P2_(a), P2_(b), P2_(c), P2_(d)}
      STAMP_AT(16);
      const cpx e8 = cexpl(ar2, air, 8.f);
      {
        const cpx s1 = {e8.r, -e8.i}, s2 = csq(s1), s4 = csq(s2), s8 = cexpl(ar2, -air, 64.f), s16 = csq(s8);
        const cp2 y0 = {{1.f, s1.r}, {0.f, s1.i}}, y1 = cmul2(y0, s2), y2 = cmul2(y0, s4), y3 = cmul2(y1, s4);
        put(0, 0, P4_(y0.r, y1.r, y2.r, y3.r), P4_(y0.i, y1.i, y2.i, y3.i));
        const cp2 y4 = cmul2(y0, s8), y5 = cmul2(y1, s8), y6 = cmul2(y2, s8), y7 = cmul2(y3, s8);
        put(0, 1, P4_(y4.r, y5.r, y6.r, y7.r), P4_(y4.i, y5.i, y6.i, y7.i));
        const cp2 y8 = cmul2(y0, s16), y9 = cmul2(y1, s16), ya = cmul2(y2, s16), yb = cmul2(y3, s16);
        put(0, 2, P4_(y8.r, y9.r, ya.r, yb.r), P4_(y8.i, y9.i, ya.i, yb.i));
        const cp2 yc = cmul2(y4, s16), yd = cmul2(y5, s16), ye = cmul2(y6, s16), yg = cmul2(y7, s16);
        put(0, 3, P4_(yc.r, yd.r, ye.r, yg.r), P4_(yc.i, yd.i, ye.i, yg.i));
      }
      {
        const cpx cf = cmul({bflo(r4), bflo(r5)}, {dt * bflo(r2), dt * bflo(r3)});
        const cpx e1 = cexpl(ar2, air, 1.f), e256 = cexpl(ar2, air, 256.f);
        const cpx e2 = csq(e1), e4 = csq(e2), e512 = csq(e256), e1024 = csq(e512);
        const cp2 cb = cmul2({{e1.r, e256.r}, {e1.i, e256.i}}, cf);
        const cp2 a0 = {{cf.r, cb.r.x}, {cf.i, cb.i.x}}, a1 = cmul2(a0, e2);
        const cp2 t0 = cmul2({{e2.r, e4.r}, {e2.i, e4.i}}, {cb.r.y, cb.i.y});
#define XA(m) (u32x4){P2_(a0.m), P2_(a1.m), pk(cb.m.y, cb.m.y), P2_(t0.m)}
#pragma unroll
        for (int s = 0; s < 4; ++s) yf[s] = get(0, s);
        put(1, 0, XA(r), XA(i));
        const cp2 a2 = cmul2(a0, e4), a3 = cmul2(a1, e4), t1 = cmul2(t0, e4);
#define XB(m) (u32x4){P2_(a2.m), P2_(a3.m), pk(t0.m.y, t0.m.y), P2_(t1.m)}
        put(1, 1, XB(r), XB(i));
#pragma unroll
        for (int s = 4; s < 8; ++s) yf[s] = get(0, s);
        const cp2 q0 = {{cf.r, cb.r.y}, {cf.i, cb.i.y}}, q1 = cmul2(q0, e512), q2 = cmul2(q0, e1024);
        const cp2 u4 = cmul2(q1, e4), u8 = cmul2(q1, e8);
#define XC(m) (u32x4){pk(q1.m.x, u4.m.x), pk(u8.m.x, q2.m.x), pk(q1.m.y, u4.m.y), pk(u8.m.y, q2.m.y)}
        put(1, 2, XC(r), XC(i));
        const cp2 q3 = cmul2(q1, e1024), v8 = cmul2(q2, e8), w8 = cmul2(q3, e8);
#define XD(m) (u32x4){pk(v8.m.x, q3.m.x), pk(w8.m.x, 0.f), pk(v8.m.y, q3.m.y), pk(w8.m.y, 0.f)}
        put(1, 3, XD(r), XD(i));
#pragma unroll
        for (int s = 0; s < 8; ++s) xf[s] = get(1, s);
      }
      STAMP_AT(17);
    }
    bf16x8 X0[2], W0[4];
    HOLD(2, 1500)
    if (!aux) BARRIER();
    X0[0] = ldfrag(sm + xa[0]); X0[1] = ldfrag(sm + xa[0] + 4096);
#pragma unroll
    for (int t = 0; t < 4; ++t) W0[t] = trfrag(sm + UNIT + wa[t], sm + UNIT + wa[t] + 1024);
    SCHED();
    {
      f32x16 kacc = (f32x16)(0.f);
#pragma unroll
      for (int s = 0; s < 8; ++s) kacc = MFMA32(xf[s], yf[s], kacc);
#pragma unroll
      for (int g = 0; g < 4; ++g) *(LAS f32x4*)(sm + KCP * v + 32 + 1024 * g + 16 * lane) = (f32x4){kacc[4 * g], kacc[4 * g + 1], kacc[4 * g + 2], kacc[4 * g + 3]};
    }
    SCHED();
    if (aux) BARRIER();
    else {
      f32x16 acc[2][4];
#pragma unroll
      for (int a = 0; a < 8; ++a) acc[a >> 2][a & 3] = (f32x16)(0.f);
      bf16x8 X1[2], W1[4];
      auto step = [&](const bf16x8 (&Xc)[2], const bf16x8 (&Wc)[4], bf16x8 (&Xn)[2], bf16x8 (&Wn)[4], u32 xb, u32 wb, int s, auto mid) {
        LAS char* xp = sm + (xb + xa[s]);
        auto wld = [&](int t) { LAS char* a = sm + (wb + wa[t]) + 8192 * (s & 1); Wn[t] = trfrag(a, a + 1024); };
        acc[0][0] = MFMA32(Wc[0], Xc[0], acc[0][0]); SCHED();
        mid(); wld(0); SCHED();
        acc[1][0] = MFMA32(Wc[0], Xc[1], acc[1][0]); SCHED();
        Xn[0] = ldfrag(xp); Xn[1] = ldfrag(xp + 4096); SCHED();
        acc[0][1] = MFMA32(Wc[1], Xc[0], acc[0][1]); SCHED();
        wld(1); SCHED();
        acc[1][1] = MFMA32(Wc[1], Xc[1], acc[1][1]); SCHED();
        wld(2); SCHED();
        acc[0][2] = MFMA32(Wc[2], Xc[0], acc[0][2]); SCHED();
        wld(3); SCHED();
        acc[1][2] = MFMA32(Wc[2], Xc[1], acc[1][2]); SCHED();
        acc[0][3] = MFMA32(Wc[3], Xc[0], acc[0][3]); SCHED();
        acc[1][3] = MFMA32(Wc[3], Xc[1], acc[1][3]); SCHED();
      };
      u32 xb = 0;
      step(X0, W0, X1, W1, xb, UNIT, 1, [&] {});
#pragma unroll 1
      for (int cc = 0;; ++cc) {
        const u32 w1 = (xb + 2 * UNIT) & (RING - 1);
        step(X1, W1, X0, W0, xb, w1, 2, [&] {});
        step(X0, W0, X1, W1, xb, w1, 3, [&] {});
        if (cc == 7) break;
        xb = (xb + 3 * UNIT) & (RING - 1);
        const u32 w0 = (xb + UNIT) & (RING - 1);
        step(X1, W1, X0, W0, xb, w0, 0, [&] { BARRIER(); });
        step(X0, W0, X1, W1, xb, w0, 1, [&] {});
      }
      STAMP_AT(2);
      HOLD(3, 3000)
      u32x2 st[2], cs[4];
      const u32 ost = 8 * (64 * tg + (lane & 31)), ocs = 8 * (64 * fq + (lane & 15));
#pragma unroll
      for (int t = 0; t < 2; ++t) st[t] = *(LAS u32x2*)(sm + L_ST + ost + 256 * t);
#pragma unroll
      for (int t = 0; t < 4; ++t) cs[t] = *(LAS u32x2*)(sm + L_CS + 1024 * blk + ocs + 128 * t);
      SCHED();
#pragma unroll
      for (int a = 0; a < 5; ++a) acc[a & 1][a >> 1] = MFMA32(W1[a >> 1], X1[a & 1], acc[a & 1][a >> 1]);
      SCHED();
#pragma unroll 1
      for (u32 s = 0; s < SPIN_CAP && __builtin_expect(!__all(st[0].y == tg32 && st[1].y == tg32 && cs[0].y == tg32 && cs[1].y == tg32 && cs[2].y == tg32 && cs[3].y == tg32), 0); ++s) {
        FENCE();
#pragma unroll
        for (int t = 0; t < 2; ++t) st[t] = ld8_sc1(rws, ost + 256 * t, WS_ST + mi * 1024);
#pragma unroll
        for (int t = 0; t < 4; ++t) cs[t] = ld8_sc1(rws, blk * F * 8 + ocs + 128 * t, WS_CS + fj * 1024);
        __builtin_amdgcn_s_sleep(8);
      }
      post(sm + S_FRE + 4 * v, 8);
      STAMP_NW(19);
      bf16x8 XM[2];
      float ka[2], kb[2], kr[2];
      acc[1][2] = MFMA32(W1[2], X1[1], acc[1][2]);
#pragma unroll
      for (int t = 0; t < 2; ++t) { const u32 m = hh ? 0u : (st[t].x & 0xffffu) ^ 0x8000u; XM[t] = __builtin_bit_cast(bf16x8, (u32x4){m | m << 16, 0, 0, 0}); }
      auto corr = [&](int a) { acc[a & 1][a >> 1] = MFMA32(__builtin_bit_cast(bf16x8, (u32x4){cs[a >> 1].x, 0, 0, 0}), XM[a & 1], acc[a & 1][a >> 1]); };
      SCHED();
      corr(0);
      const u32 vh = ((2 * mi + tg) * 112 + 8 * fj + 4 * fq) * 2048 + lane * 16;
#pragma unroll
      for (int t = 0; t < 2; ++t) {
        const float sd = bfhi(st[t].x), r = __builtin_amdgcn_rcpf(sd);
        kb[t] = -2.f * 0.7978845608028654f * 1.44269504088896f * r; ka[t] = kb[t] * 0.044715f * r * r; kr[t] = sd * sd;
      }
      SCHED();
#pragma unroll
      for (int a = 0; a < 8; ++a) {
        const int tt = a & 1, ft = a >> 1;
        f32x2 z[4], gu[4];
#pragma unroll
        for (int e = 0; e < 4; ++e) {
          const f32x2 g = {acc[tt][ft][2 * e], acc[tt][ft][2 * e + 1]}, u = {acc[tt][ft][8 + 2 * e], acc[tt][ft][9 + 2 * e]};
          z[e] = (g * g * ka[tt] + kb[tt]) * g; gu[e] = g * u;
        }
        SCHED();
        if (a < 7) corr(a + 1);
        SCHED();
#pragma unroll
        for (int e = 0; e < 4; ++e) z[e] = (f32x2){__builtin_amdgcn_exp2f(z[e].x), __builtin_amdgcn_exp2f(z[e].y)};
        SCHED();
#pragma unroll
        for (int e = 0; e < 4; ++e) z[e] = z[e] * kr[tt] + kr[tt];
        SCHED();
        if (a < 2) acc[a][3] = MFMA32(W1[3], X1[a], acc[a][3]);
        SCHED();
#pragma unroll
        for (int e = 0; e < 4; ++e) z[e] = (f32x2){__builtin_amdgcn_rcpf(z[e].x), __builtin_amdgcn_rcpf(z[e].y)};
        SCHED();
        u32x4 hv;
#pragma unroll
        for (int e = 0; e < 4; ++e) { const f32x2 hq = gu[e] * z[e]; hv[e] = pk(hq.x, hq.y); }
        st16_sc1(rws, hv, vh + 1024 * a);
#define FLAGA 5
        if (a == 1) STAMP_NW(20);
        if (a == 3) STAMP_NW(21);
        if (a == FLAGA) {
          STAMP_NW(22);
          asm volatile("s_waitcnt vmcnt(%0)" :: "i"(FLAGA - 3) : "memory");
          if (lane == 0) st8_sc1(rws, tag, FL_2 + ((2 * mi + tg) * 28 + 2 * fj + fq) * FS, 0);
          post(sm + S_GP + 4 * v, 1);
          STAMP_NW(18);
        }
        SCHED();
      }
      STAMP_NW(23); STAMP_NWEND();
      STAMP_AT(3);
    }
    LAS char* ka = sm + (4 * c + q4) * KCP + 2 * (28 + 128 * ah + 4 * p4 - 8 * g4);
    const u32 ub0 = 30 - 16 * ah + 2 * (lane & 15) + g4;
    f32x4 y[8];
#pragma unroll
    for (int i = 0; i < 8; ++i) y[i] = (f32x4)(0.f);
    await(sm + S_HP + 4 * (lane & 1), peek(sm + S_HP + 4 * (lane & 1)), 5);
    if (b == 255 && tid == 256) st8_sc1(rws, tag, FL_E, 0);
    const u32 vo = (64 * ti2 + 8 * v + (lane >> 3)) * (D * 2) + n2 * 128 + (lane & 7) * 16;
    u32x4 xres[2];
    STAMP_AT(4);
    bf16x8 A[2][8], B[2][8];
    auto cld = [&](int a, int i) {
      A[a & 1][i] = trfrag(ka + 64 * a + 512 * i + 8, ka + 64 * a + 512 * i);
      B[a & 1][i] = ldfrag(sm + L_U + 32 * (ub0 - 4 * a) + 16 * ((c ^ ((ub0 - 4 * a) >> 3)) & 1) + 1024 * i);
    };
#pragma unroll
    for (int i = 0; i < 8; ++i) cld(0, i);
#pragma unroll
    for (int a = 0; a < 4; ++a) {
#pragma unroll
      for (int s = 0; s < 8; ++s) {
#pragma unroll
        for (int i = 0; i <= s; ++i) {
          if (i + s < 8) y[i + s] = MFMA16(A[a & 1][i], B[a & 1][s], y[i + s]);
          if (i < s && i + s < 8) y[i + s] = MFMA16(A[a & 1][s], B[a & 1][i], y[i + s]);
        }
        SCHED();
        if (a < 3) cld(a + 1, s);
        SCHED();
      }
#define FLAG_AT 0
      if (a == FLAG_AT) {
        STAMP_AT(5);
        WAIT_V(0);
        if (!aux && lane == 0) st8_sc1(rws, tag, FL_2 + ((2 * mi + c) * 28 + 2 * fj + ah) * FS, FL_2B);
        xres[0] = __builtin_amdgcn_raw_buffer_load_b128(rx, vo, 0, 0);
        xres[1] = __builtin_amdgcn_raw_buffer_load_b128(rx, vo, 32 * D * 2, 0);
        STAMP_AT(6);
      }
    }
    STAMP_AT(7);
    LAS char* const pw = sm + S_GP + 4 * (v ^ 1);
    LAS char* ex = sm + 4 * c * KCP + 32;
    post(sm + S_GP + 4 * v, 2);
    await(pw, peek(pw), 2);
    if (ah) {
#pragma unroll
      for (int i = 0; i < 4; ++i) { *(LAS f32x4*)(ex + KCP + 1024 * i + 16 * lane) = y[i]; FENCE(); y[i] = y[4 + i]; }
    } else {
#pragma unroll
      for (int i = 0; i < 4; ++i) *(LAS f32x4*)(ex + 1024 * i + 16 * lane) = y[4 + i];
    }
    post(sm + S_GP + 4 * v, 3);
    await(pw, peek(pw), 3);
    STAMP_AT(8);
#pragma unroll
    for (int i = 0; i < 4; ++i) {
      const f32x4 t = *(LAS f32x4*)(ex + KCP * (ah ^ 1) + 1024 * i + 16 * lane) + y[i];
      st16_sc1(rws, (u32x4){pk(t[0], t[1]), tg32, pk(t[2], t[3]), tg32}, WS_ZT + ch * 8192 + 4096 * ah + 64 * (lane & 15) + 16 * g4 + 1024 * i);
    }
    STAMP_AT(9);
#define ZSLEEP 8
    const u32 vz = (64 * n2 + 16 * v + g4) * 8192 + 256 * ti2 + (lane & 15) * 16;
    u32x4 zz[4];
    await(sy + S_H2, peek(sy + S_H2), 1);
    STAMP_AT(10);
#pragma unroll 1
    for (u32 s = 0; s < SPIN_CAP; ++s) {
      if (s) __builtin_amdgcn_s_sleep(ZSLEEP);
      FENCE();
#pragma unroll
      for (int i = 0; i < 4; ++i) zz[i] = ld16_sc1(rws, vz, WS_ZT + 4 * 8192 * i);
      if (__all(zz[0].y == tg32 && zz[0].w == tg32 && zz[1].y == tg32 && zz[1].w == tg32 && zz[2].y == tg32 && zz[2].w == tg32 && zz[3].y == tg32 && zz[3].w == tg32)) break;
    }
    STAMP_AT(11);
    LAS char* stg = ex + 3 * KCP + 2048 * ah;
#pragma unroll
    for (int i = 0; i < 4; ++i) *(LAS u32x2*)(stg + 512 * i + 8 * lane) = (u32x2){zz[i].x, zz[i].z};
    LAS char* tr = stg + q4 * 128 + 32 * g4 + 8 * p4;
    const bf16x8 ra = trfrag(tr, tr + 512), rc = trfrag(tr + 1024, tr + 1536);
    LAS char* rb = ex + 2 * KCP + 2048 * ah + 32 * lane;
    *(LAS bf16x8*)rb = ra;
    *(LAS bf16x8*)(rb + 16) = rc;
    post(sm + S_GP + 4 * v, 4);
    await(sy + S_GP, peek(sy + S_GP), 4);
    const int tok = 8 * v + (lane >> 3), dc = lane & 7;
    f32x4 s0[2], s1[2];
#pragma unroll
    for (int i = 0; i < 2; ++i) {
      const u32x4 z = *(LAS u32x4*)(sm + (4 * (dc >> 2) + 2) * KCP + 32 + 2048 * ((dc >> 1) & 1) + 32 * tok + 16 * (dc & 1) + 1024 * i), x = xres[i];
      s0[i] = (f32x4){bflo(x[0]) + bflo(z[0]), bfhi(x[0]) + bfhi(z[0]), bflo(x[1]) + bflo(z[1]), bfhi(x[1]) + bfhi(z[1])};
      s1[i] = (f32x4){bflo(x[2]) + bflo(z[2]), bfhi(x[2]) + bfhi(z[2]), bflo(x[3]) + bflo(z[3]), bfhi(x[3]) + bfhi(z[3])};
    }
    u32 t0 = RB + UNIT + UNIT * 2 * (dc >> 2) + tok * 128 + (((2 * (dc & 3)) ^ (tok & 7)) << 4), t1 = t0 ^ 16u;
    char* op = (char*)p.out + vo;
    asm volatile("" : "+v"(s0[0]), "+v"(s0[1]), "+v"(s1[0]), "+v"(s1[1]), "+v"(t0), "+v"(t1), "+v"(op));
    STAMP_AT(12);
    BARRIER();
    STAMP_AT(13);
#pragma unroll
    for (int i = 0; i < 2; ++i) {
#pragma unroll
      for (int g = 0; g < 4; ++g) { s0[i] += *(LAS f32x4*)(sm + t0 + UNIT * i + 4096 * g); s1[i] += *(LAS f32x4*)(sm + t1 + UNIT * i + 4096 * g); }
      *(GAS u32x4*)(op + 32 * D * 2 * i) = (u32x4){pk(s0[i][0], s0[i][1]), pk(s0[i][2], s0[i][3]), pk(s1[i][0], s1[i][1]), pk(s1[i][2], s1[i][3])};
    }
  }
  HOLD(1, 4000) BURN(5)
  STAMP_AT(14); STAMP_OUT();
}

extern "C" void kernel_launch(void* const* d_in, const int* in_sizes, int n_in, void* d_out, int out_size, void* d_ws,
                              size_t ws_size, hipStream_t stream) {
  Params p;
  p.x = (const u16*)d_in[0]; p.are = (const u16*)d_in[1]; p.aim = (const u16*)d_in[2];
  p.bre = (const u16*)d_in[3]; p.bim = (const u16*)d_in[4]; p.cre = (const u16*)d_in[5];
  p.cim = (const u16*)d_in[6]; p.logdt = (const u16*)d_in[7]; p.dskip = (const u16*)d_in[8];
  p.wg = (const u16*)d_in[10]; p.wu = (const u16*)d_in[11]; p.wd = (const u16*)d_in[12];
  p.out = (u16*)d_out; p.ws = (char*)d_ws;
  s4ffn_kernel<<<256, 512, 0, stream>>>(p);
}
```

```cpp
#include <hip/hip_runtime.h>
#include <stdint.h>

typedef unsigned short u16;
typedef unsigned u32;
typedef unsigned long long u64;
typedef __bf16 bf16x2 __attribute__((ext_vector_type(2)));
typedef __bf16 bf16x4 __attribute__((ext_vector_type(4)));
typedef __bf16 bf16x8 __attribute__((ext_vector_type(8)));
typedef float f32x2 __attribute__((ext_vector_type(2)));
typedef float f32x4 __attribute__((ext_vector_type(4)));
typedef float f32x16 __attribute__((ext_vector_type(16)));
typedef u32 u32x2 __attribute__((ext_vector_type(2)));
typedef u32 u32x4 __attribute__((ext_vector_type(4)));
#define LAS __attribute__((address_space(3)))
#define GAS __attribute__((address_space(1)))
#define DI __device__ __forceinline__
typedef __amdgpu_buffer_rsrc_t rsrc_t;

constexpr int L = 2048, D = 512, NM = 128, F = 1792;
constexpr u32 WS_H = 0, WS_U = 8u << 20, WS_ZT = 10u << 20, WS_ST = 14u << 20, WS_CS = WS_ST + 65536, WS_FL = 15u << 20, WS_K = 16u << 20, WS_END = 24u << 20;
constexpr u32 FL_E = WS_FL, FL_1 = WS_FL + 4096, FL_2 = WS_FL + 131072, FS = 128, FL_2B = 131072;
constexpr u32 KCP = 4160, UNIT = 16384, RB = 32768, RING = 8 * UNIT;
constexpr u32 L_U = 8 * KCP;
constexpr u32 L_ST = L_U + 9216;
constexpr u32 L_CS = L_ST + 1024;
constexpr u32 S_FRE = 4128, S_HP = 4144, S_GP = KCP + 4128, S_H2 = KCP + 4144;
constexpr u32 CODE_BYTES = 24576;
constexpr u32 SPIN_CAP = 1u << 16;

struct Params {
  const u16 *x, *are, *aim, *bre, *bim, *cre, *cim, *logdt, *dskip, *wg, *wu, *wd;
  u16* out;
  char* ws;
};

#define STAMP_DECL u64 pa_ = 0, pb_ = 0; const u64 rt0_ = __builtin_amdgcn_s_memrealtime()
#define STAMP_AT(k) do { if (k == 0) asm volatile("s_memrealtime %0\n\ts_waitcnt lgkmcnt(0)" : "=s"(pa_) :: "memory"); if (k == 0) asm volatile("s_memrealtime %0\n\ts_waitcnt lgkmcnt(0)" : "=s"(pb_) :: "memory"); } while (0)
#define STAMP_NW(k) STAMP_AT(k)
#define STAMP_NWEND()
#define STAMP_RT(v)
#define STAMP_OUT() do { if (w == 4 && b == 101) { const u64 e_ = rt0_ + 3000 + (u32)(pb_ - pa_) / 1; while (__builtin_amdgcn_s_memrealtime() < e_) __builtin_amdgcn_s_sleep(1); } } while (0)

DI float bflo(u32 v) { return __builtin_bit_cast(float, v << 16); }
DI float bfhi(u32 v) { return __builtin_bit_cast(float, v & 0xffff0000u); }
DI u32 pk(float a, float b) { f32x2 f = {a, b}; return __builtin_bit_cast(u32, __builtin_convertvector(f, bf16x2)); }
DI float rbf(float a) { return bflo(pk(a, a)); }
DI void dma4(rsrc_t ra, rsrc_t rb, u32 lds, u32 v0, u32 v1, u32 v2, u32 v3, u32 soff) {
  asm volatile("s_mov_b32 m0, %0\n\ts_nop 0\n\tbuffer_load_dwordx4 %1, %5, %7 offen lds\n\tbuffer_load_dwordx4 %2, %5, %7 offen offset:1024 lds\n\t"
               "buffer_load_dwordx4 %3, %6, %7 offen offset:2048 lds\n\tbuffer_load_dwordx4 %4, %6, %7 offen offset:3072 lds"
               :: "s"(lds), "v"(v0), "v"(v1), "v"(v2), "v"(v3), "s"(ra), "s"(rb), "s"(soff));
}
DI void dma4s(rsrc_t r, u32 lds, u32 v0, u32 v1, u32 v2, u32 v3, u32 soff) {
  asm volatile("s_mov_b32 m0, %0\n\ts_nop 0\n\tbuffer_load_dwordx4 %1, %5, %6 offen sc1 lds\n\tbuffer_load_dwordx4 %2, %5, %6 offen offset:1024 sc1 lds\n\t"
               "buffer_load_dwordx4 %3, %5, %6 offen offset:2048 sc1 lds\n\tbuffer_load_dwordx4 %4, %5, %6 offen offset:3072 sc1 lds"
               :: "s"(lds), "v"(v0), "v"(v1), "v"(v2), "v"(v3), "s"(r), "s"(soff));
}
DI void dma1s(rsrc_t r, u32 lds, u32 v0, u32 soff) {
  asm volatile("s_mov_b32 m0, %0\n\ts_nop 0\n\tbuffer_load_dwordx4 %1, %2, %3 offen sc1 lds" :: "s"(lds), "v"(v0), "s"(r), "s"(soff));
}
#define WAIT_V(n) asm volatile("s_waitcnt vmcnt(" #n ")" ::: "memory")
#define WAIT_L() asm volatile("s_waitcnt lgkmcnt(0)" ::: "memory")
#define BARRIER() asm volatile("s_waitcnt lgkmcnt(0)\n\ts_barrier" ::: "memory")
#define FENCE() asm volatile("" ::: "memory")
#define SCHED() __builtin_amdgcn_sched_barrier(0)
DI bf16x8 ldfrag(LAS char* a) { return *(LAS bf16x8*)a; }
DI bf16x8 trfrag(LAS char* a0, LAS char* a1) {
  bf16x4 lo = __builtin_amdgcn_ds_read_tr16_b64_v4bf16((LAS bf16x4*)a0);
  bf16x4 hi = __builtin_amdgcn_ds_read_tr16_b64_v4bf16((LAS bf16x4*)a1);
  return __builtin_shufflevector(lo, hi, 0, 1, 2, 3, 4, 5, 6, 7);
}
#define MFMA32(a, b, c) __builtin_amdgcn_mfma_f32_32x32x16_bf16(a, b, c, 0, 0, 0)
#define MFMA16(a, b, c) __builtin_amdgcn_mfma_f32_16x16x32_bf16(a, b, c, 0, 0, 0)
DI u32x2 ld8_sc1(rsrc_t r, u32 voff, u32 soff) { return __builtin_amdgcn_raw_buffer_load_b64(r, voff, soff, 16); }
DI u32x4 ld16_sc1(rsrc_t r, u32 voff, u32 soff) { return __builtin_amdgcn_raw_buffer_load_b128(r, voff, soff, 16); }
DI void st8_sc1(rsrc_t r, u32x2 v, u32 voff, u32 soff) { __builtin_amdgcn_raw_buffer_store_b64(v, r, voff, soff, 16); }
DI void st16_sc1(rsrc_t r, u32x4 v, u32 voff) { __builtin_amdgcn_raw_buffer_store_b128(v, r, voff, 0, 16); }
template <int SLEEP> DI void wait_flags(rsrc_t r, u32 voff, u32 tlo, u32 thi) {
#pragma unroll 1
  for (u32 s = 0; s < SPIN_CAP; ++s) {
    FENCE();
    u32x2 f = ld8_sc1(r, voff, 0);
    if (__all(f.x == tlo && f.y == thi)) break;
    __builtin_amdgcn_s_sleep(SLEEP);
  }
  WAIT_V(0);
}
DI void post(LAS char* word, u32 v) {
  asm volatile("s_mov_b64 exec, 1\n\tds_write_b32 %0, %1\n\ts_mov_b64 exec, -1" :: "v"(word), "v"(v) : "memory");
}
DI u32 peek(LAS char* word) { return *(volatile LAS u32*)word; }
DI void await(LAS char* word, u32 seen, u32 need) {
  if (__builtin_expect(__any(seen < need), 0)) {
#pragma unroll 1
    for (u32 s = 0; s < SPIN_CAP * 16; ++s) { __builtin_amdgcn_s_sleep(1); if (!__any(peek(word) < need)) break; }
  }
  FENCE();
}
#define DPPADD(name, ctl) DI float name(float x) { float r; asm("s_nop 1\n\tv_add_f32_dpp %0, %1, %1 " ctl " row_mask:0xf bank_mask:0xf" : "=v"(r) : "v"(x)); return r; }
DPPADD(add_x1, "quad_perm:[1,0,3,2]") DPPADD(add_x2, "quad_perm:[2,3,0,1]") DPPADD(add_m8, "row_half_mirror") DPPADD(add_x8, "row_ror:8")
DI float fold32(float a, float b) {
  asm("s_nop 1\n\tv_permlane32_swap_b32 %0, %1\n\tv_add_f32 %0, %0, %1" : "+v"(a), "+v"(b)); return a;
}
DI float fold16(float a, float b) {
  asm("s_nop 1\n\tv_permlane16_swap_b32 %0, %1\n\tv_add_f32 %0, %0, %1" : "+v"(a), "+v"(b)); return a;
}
DI u32 vmov(u32 a) { u32 r; asm("v_mov_b32 %0, %1" : "=v"(r) : "v"(a)); return r; }
struct cpx { float r, i; };
struct cp2 { f32x2 r, i; };
DI cpx cmul(cpx a, cpx b) { return {a.r * b.r - a.i * b.i, a.r * b.i + a.i * b.r}; }
DI cpx csq(cpx a) { return {a.r * a.r - a.i * a.i, (a.r + a.r) * a.i}; }
DI cp2 cmul2(cp2 a, cpx b) { return {a.r * b.r - a.i * b.i, a.i * b.r + a.r * b.i}; }
DI cpx cexpl(float ar2, float air, float l) {
  float m = __builtin_amdgcn_exp2f(ar2 * l), f = __builtin_amdgcn_fractf(air * l);
  return {m * __builtin_amdgcn_cosf(f), m * __builtin_amdgcn_sinf(f)};
}

__global__ __launch_bounds__(512, 2) void s4ffn_kernel(Params p) {
  __shared__ __attribute__((aligned(16))) char smem[163840];
  LAS char* sm = (LAS char*)smem;
  const u32 smb = (u32)(size_t)sm;
  const int tid = threadIdx.x, lane = tid & 63;
  const int w = __builtin_amdgcn_readfirstlane(tid >> 6), v = w & 3;
  const int b = blockIdx.x, xcd = b & 7, o = b >> 3;
  u32 cpf;
  {
    u64 pc;
    asm volatile("s_getpc_b64 %0" : "=s"(pc));
    const u32 off = (8 * o + w) * 128;
    asm volatile("s_load_dword %0, %1, 0\n\ts_load_dword %0, %1, 64" : "=&s"(cpf) : "s"((pc & ~127ull) + (off < CODE_BYTES ? off : 0)));
    SCHED();
  }
  const bool aux = o >= 28;
  const int ma = xcd >> 1, fh = xcd & 1;
  const int il = o / 7, jl = o - il * 7;
  const int mi = 4 * ma + il, fj = 7 * fh + jl;
  const int ti2 = 4 * xcd + (o >> 3), n2 = o & 7;
  const int tb = 32 * xcd + o, ch0 = 16 * o + xcd;
  const int hh = lane >> 5, blk = (lane >> 4) & 1, q4 = (lane >> 2) & 3, p4 = lane & 3, g4 = lane >> 4;
  STAMP_DECL; STAMP_RT(rt0_); STAMP_AT(0);
#define HOLD(id, ticks)
#define BURN(id)
  BURN(4)
  const rsrc_t rx = __builtin_amdgcn_make_buffer_rsrc((void*)p.x, 0, L * D * 2, 0x00020000);
  const rsrc_t rwg = __builtin_amdgcn_make_buffer_rsrc((void*)p.wg, 0, D * F * 2, 0x00020000);
#define ROT (2 * ma)
#define ROT2 0
  const rsrc_t rws = __builtin_amdgcn_make_buffer_rsrc((void*)p.ws, 0, WS_END, 0x00020000);
  u64 ev;
#define LOAD_EPOCH() asm volatile("s_load_dwordx2 %0, %1, 0\n\ts_waitcnt lgkmcnt(0)" : "=&s"(ev) : "s"(p.ws + FL_E) : "memory")
#define MAKE_TAG() const u32 tlo = (u32)(ev + 0x9E3779B97F4A7C15ull), thi = (u32)((ev + 0x9E3779B97F4A7C15ull) >> 32), tg32 = tlo ^ thi; const u32x2 tag = {tlo, thi}
  asm volatile("s_waitcnt lgkmcnt(0)" :: "s"(cpf), "s"(mi), "s"(fj), "s"(ti2), "s"(ch0));
  LAS char* const sy = sm + 4 * p4;

  if (w < 4) {
  if (w < 2) {
    if (!aux) {
      const rsrc_t rwu2 = __builtin_amdgcn_make_buffer_rsrc((void*)((const char*)p.wu - 2048), 0, D * F * 2 + 2048, 0x00020000);
      const u32 vx0 = (lane >> 3) * 1024 + (((lane & 7) ^ g4) << 4), vx1 = (vx0 ^ 64) + 7168, vx2 = vx0 + 14336, vx3 = vx1 + 14336;
      const u32 vg0 = g4 * (F * 2) + (((((lane & 15) >> 1) ^ g4) << 5) | ((lane & 1) << 4)), vg1 = vg0 + 4 * F * 2 - 1024;
      const u32 vu0 = vg0 ^ 128, vu1 = vu0 + 4 * F * 2 - 1024;
      const u32 sx0 = (mi * 128 + 64 * v) * 1024, sw0 = 16 * v * (F * 2) + fj * 256;
      u32 dst = 8192 * v;
      auto ux = [&](u32 c) {
        const u32 s = sx0 + ((ROT + c) & 7) * 128;
        dma4(rx, rx, smb + RB + dst, vx0, vx1, vx2, vx3, s); dma4(rx, rx, smb + RB + dst + 4096, vx0, vx1, vx2, vx3, s + 32768);
        dst = (dst + UNIT) & (RING - 1);
      };
      auto uw = [&](u32 c, u32 h) {
        const u32 s = sw0 + (2 * ((ROT + c) & 7) + h) * (32 * F * 2);
        dma4(rwg, rwu2, smb + RB + dst, vg0, vg1, vu0, vu1, s); dma4(rwg, rwu2, smb + RB + dst + 4096, vg0, vg1, vu0, vu1, s + 8 * F * 2);
        dst = (dst + UNIT) & (RING - 1);
      };
#define LDELAY 2
      __builtin_amdgcn_s_sleep(LDELAY);
      ux(0); uw(0, 0);
      LOAD_EPOCH();
#pragma unroll 1
      for (int c = 0; c < 7; ++c) {
        uw(c, 1); ux(c + 1); uw(c + 1, 0);
        WAIT_V(16);
        if (c == 0) STAMP_AT(1);
        __builtin_amdgcn_s_barrier();
      }
      uw(7, 1);
      dma1s(rws, smb + L_ST, lane * 16, WS_ST + mi * 1024);
      dma1s(rws, smb + L_CS + 1024 * v, lane * 16, WS_CS + (v * F + fj * 128) * 8);
      STAMP_AT(2);
    } else {
      LOAD_EPOCH();
      const rsrc_t rp = __builtin_amdgcn_make_buffer_rsrc((void*)p.wd, 0, D * F * 2, 0x00020000);
#define PFN 0
      u32 t = 0;
#pragma unroll 1
      for (int q = 0; q < 14; ++q) {
        if (q == 0 || q == 4) __builtin_amdgcn_s_barrier();
        const u32 so = (28 * (o - 28) + 4 * (q % 7) + 2 * (q / 7) + v) * (16 * D * 2);
        if (q < PFN) asm volatile("s_waitcnt vmcnt(2)\n\tbuffer_load_dword %0, %1, %2, %3 offen\n\tbuffer_load_dword %0, %1, %2, %4 offen" : "+v"(t) : "v"(lane * 128), "s"(rp), "s"(so), "s"(so + 8192));
      }
      asm volatile("s_waitcnt vmcnt(0)" : "+v"(t));
      STAMP_AT(2);
    }
  }
    const rsrc_t rwd = __builtin_amdgcn_make_buffer_rsrc((void*)p.wd, 0, D * F * 2, 0x00020000);
    const int rot2 = ROT2;
    if (v >= 2) {
      const int k = v - 2;
      auto bar = [&] { SCHED(); if (!aux) __builtin_amdgcn_s_barrier(); FENCE(); SCHED(); };
      auto pull = [&](int i, int skip) {
        u32 d;
        asm volatile(".set pf_off, 0\n\t.rept 14\n\ts_load_dword %0, %1, pf_off\n\t.set pf_off, pf_off + 64\n\t.endr\n\t"
                     "s_cmp_ge_i32 %2, 28\n\ts_cbranch_scc1 1f\n\ts_barrier\n1:\n\ts_waitcnt lgkmcnt(0)"
                     : "=&s"(d) : "s"((const char*)p.wd + tb * 7168 + 896 * i), "s"(skip) : "scc", "memory");
      };
      auto kcopy = [&](int q) { dma4s(rws, smb + q * KCP + 32, lane * 16, lane * 16, lane * 16, lane * 16, WS_K + b * 16896 + 128 + 8448 * (q >> 2) + 4224 * (q & 1) - 2 * (q & 2)); };
      if (k && lane >= 2 && lane < 32) *(LAS u32x4*)(sm + KCP * (lane >> 2) - 32 + 16 * p4) = (u32x4)(0u);
      if (k == 0) {
        u32x4 xr[8];
#pragma unroll
        for (int i = 0; i < 8; ++i) xr[i] = __builtin_amdgcn_raw_buffer_load_b128(rx, lane * 16 + 1024 * (i & 3), (8 * tb + (i & 4)) * 1024, 0);
        LOAD_EPOCH();
        bar();
        float x[8][8], su[8], sq[8];
#pragma unroll
        for (int r = 0; r < 8; ++r) {
#pragma unroll
          for (int e = 0; e < 4; ++e) { x[r][2 * e] = bflo(xr[r][e]); x[r][2 * e + 1] = bfhi(xr[r][e]); }
          asm("v_add_f32 %0, %2, %3\n\tv_mul_f32 %1, %2, %2\n\tv_add_f32 %0, %0, %4\n\tv_fmac_f32 %1, %3, %3\n\tv_add_f32 %0, %0, %5\n\tv_fmac_f32 %1, %4, %4\n\t"
              "v_add_f32 %0, %0, %6\n\tv_fmac_f32 %1, %5, %5\n\tv_add_f32 %0, %0, %7\n\tv_fmac_f32 %1, %6, %6\n\tv_add_f32 %0, %0, %8\n\tv_fmac_f32 %1, %7, %7\n\t"
              "v_add_f32 %0, %0, %9\n\tv_fmac_f32 %1, %8, %8\n\tv_fmac_f32 %1, %9, %9"
              : "=&v"(su[r]), "=&v"(sq[r])
              : "v"(x[r][0]), "v"(x[r][1]), "v"(x[r][2]), "v"(x[r][3]), "v"(x[r][4]), "v"(x[r][5]), "v"(x[r][6]), "v"(x[r][7]));
        }
        STAMP_AT(1);
        bar();
        MAKE_TAG();
        auto total = [&](const float (&a)[8]) {
          const float lo = add_x8(fold16(fold32(a[0], a[2]), fold32(a[1], a[3]))), hi = add_x8(fold16(fold32(a[4], a[6]), fold32(a[5], a[7])));
          return add_m8(add_x2(add_x1(lane & 8 ? hi : lo)));
        };
        const float mean = total(su) * (1.f / D), nsq = -mean * mean;
        const float var = rbf(__builtin_fmaxf(__builtin_fmaf(total(sq), 1.f / D, nsq), 0.f));
        const u32 gran = pk(mean, __builtin_amdgcn_sqrtf(rbf(var + 1e-5f)));
        const float r = __builtin_amdgcn_rcpf(bfhi(gran)), mr = -bflo(gran) * r;
        if ((lane & 7) == 0) st8_sc1(rws, (u32x2){gran, tg32}, 8 * g4 + 4 * (lane & 8), WS_ST + 64 * tb);
        u32 n[8][4];
#pragma unroll
        for (int i = 0; i < 4; ++i) {
          if (i == 2) bar();
          const int l0 = 16 * (2 * i & 3) + 8 * (i >> 1);
          const u32 ra = __builtin_amdgcn_readlane(__builtin_bit_cast(u32, r), l0), rb = __builtin_amdgcn_readlane(__builtin_bit_cast(u32, r), l0 + 16);
          u32 ma_ = __builtin_amdgcn_readlane(__builtin_bit_cast(u32, mr), l0), mb_ = __builtin_amdgcn_readlane(__builtin_bit_cast(u32, mr), l0 + 16);
          asm volatile("s_nop 1" : "+v"(ma_), "+v"(mb_) : "s"(ra), "s"(rb));
#pragma unroll
          for (int e = 0; e < 8; ++e)
            asm("v_fma_f32 %1, %1, %3, %5\n\tv_fma_f32 %2, %2, %4, %6\n\tv_cvt_pk_bf16_f32 %0, %1, %2"
                : "=v"(n[e][i]), "+v"(x[2 * i][e]), "+v"(x[2 * i + 1][e]) : "s"(ra), "s"(rb), "v"(ma_), "v"(mb_));
        }
#pragma unroll
        for (int e = 0; e < 8; ++e) st16_sc1(rws, (u32x4){n[e][0], n[e][1], n[e][2], n[e][3]}, WS_U + tb * 8192 + lane * 16 + 1024 * e);
        SCHED(); __builtin_amdgcn_s_barrier();
        WAIT_V(0);
        if (lane == 0) st8_sc1(rws, tag, FL_1 + tb * 8, 0);
        STAMP_AT(2);
        pull(6, o - 16); pull(7, o);
      } else {
        const u32 dsk0 = p.dskip[ch0], dsk1 = p.dskip[ch0 + 8];
        LOAD_EPOCH();
        pull(0, o - 16); pull(1, o - 16);
        const u32 sk0 = WS_K + b * 16896 + 128;
#pragma unroll 1
        for (int j = 0; j < 2; ++j) {
        const u32 sk = sk0 + 8448 * j, dsk = j ? dsk1 : dsk0;
        float kv[16];
#pragma unroll
        for (int g = 0; g < 4; ++g) {
          LAS char* s = sm + 2 * KCP * j + 32 + 1024 * g + 16 * lane;
          const f32x4 a = *(LAS f32x4*)s, d = *(LAS f32x4*)(s + KCP);
#pragma unroll
          for (int e = 0; e < 4; ++e) asm("v_add_f32 %0, %1, %2" : "=v"(kv[4 * g + e]) : "v"(a[e]), "v"(d[e]));
        }
        const bool odd = lane & 1;
        if (lane == 0) kv[0] += bflo(dsk);
        u32x4 kc[4];
        kc[0] = (u32x4){pk(kv[0], kv[1]), pk(kv[2], kv[3]), pk(kv[4], kv[5]), pk(kv[6], kv[7])};
        kc[1] = (u32x4){pk(kv[8], kv[8]), pk(kv[8], kv[9]), pk(kv[9], kv[9]), pk(kv[10], kv[10])};
        kc[2] = (u32x4){pk(kv[11], kv[11]), pk(kv[11], kv[11]), pk(odd ? kv[12] : kv[11], kv[12]), pk(kv[12], kv[12])};
        kc[3] = (u32x4){pk(kv[13], kv[13]), pk(kv[13], kv[13]), pk(odd ? kv[14] : kv[13], kv[14]), pk(kv[14], kv[14])};
#pragma unroll
        for (int g = 0; g < 4; ++g) {
          const u32x4 cu = kc[g];
          int py = 0;
          if (g) py = __builtin_amdgcn_update_dpp(0, (int)kc[g ? g - 1 : 0].w, 0x13C, 0xf, 0xf, false);
          py = __builtin_amdgcn_update_dpp(py, (int)cu.w, 0x138, 0xf, 0xf, false);
          const u32x4 sh = {__builtin_amdgcn_alignbit(cu.x, py, 16), __builtin_amdgcn_alignbit(cu.y, cu.x, 16), __builtin_amdgcn_alignbit(cu.z, cu.y, 16), __builtin_amdgcn_alignbit(cu.w, cu.z, 16)};
          __builtin_amdgcn_raw_buffer_store_b128(cu, rws, sk + lane * 16 + 1024 * g, 0, 0);
          __builtin_amdgcn_raw_buffer_store_b128(sh, rws, sk + 4224 + lane * 16 + 1024 * g, 0, 0);
        }
        if (lane < 2) __builtin_amdgcn_raw_buffer_store_b32(0, rws, sk - 4 + 4224 * lane, 0, 0);
        pull(2 + j, o);
        }
        STAMP_AT(3);
        WAIT_V(0);
        kcopy(0); kcopy(1); kcopy(2);
        pull(4, o);
        kcopy(3); kcopy(4);
        pull(5, o);
        kcopy(5); kcopy(6);
      }
      MAKE_TAG();
      const u32 vf1 = FL_1 + (128 * k + 2 * lane) * 8;
      u32x4 f1 = ld16_sc1(rws, vf1, 0);
      const u32 vu0 = (lane >> 1) * 8192 + ((lane ^ g4) & 1) * 16, us = 32 * 8192 - 1024;
      bar();
#define QDELAY 3
      __builtin_amdgcn_s_sleep(QDELAY);
#pragma unroll 1
      for (u32 s = 0; s < SPIN_CAP && !__all(f1.x == tlo && f1.y == thi && f1.z == tlo && f1.w == thi); ++s) {
        FENCE();
        f1 = ld16_sc1(rws, vf1, 0);
        __builtin_amdgcn_s_sleep(16);
      }
      FENCE();
      STAMP_AT(4);
      dma4s(rws, smb + L_U + 1024 + 4096 * k, vu0, vu0 + us, vu0 + 2 * us, vu0 + 3 * us, WS_U + k * 128 * 8192 + tb * 32);
      if (k == 0) dma1s(rws, smb + L_U, lane * 16, 0x40000000u);
      else kcopy(7);
      STAMP_AT(5);
    }
    MAKE_TAG();
    const u32 dq = smb + RB + 4096 * v;
    const u32 vd0 = (lane >> 3) * (D * 2) + (((lane & 7) ^ ((lane >> 2) & 4)) << 4), vd1 = vd0 + 7168, vd2 = vd0 + 14336, vd3 = vd0 + 21504;
    auto ks_of = [&](int q) { int k = q % 7 + rot2; k = k >= 7 ? k - 7 : k; return 28 * v + 4 * k + 2 * (q / 7); };
    auto wdq = [&](int q, int slot) { dma4(rwd, rwd, dq + slot * UNIT, vd0, vd1, vd2, vd3, ks_of(q) * (16 * D * 2) + n2 * 128); };
    const u32 wo0 = RB + 4096 * v + (4 * hh + q4) * 128 + p4 * 8 + ((blk ^ (q4 & 2)) << 5), wo1 = wo0 ^ 64;
    auto wfrag = [&](int slot, int e, int ct) { LAS char* a = sm + slot * UNIT + 2048 * e + (ct ? wo1 : wo0); return trfrag(a, a + 1024); };
    bf16x8 wr_[14][2];
    auto take = [&](int q, int slot) {
#pragma unroll
      for (int e = 0; e < 4; ++e) wr_[2 * q + (e >> 1)][e & 1] = wfrag(slot, e >> 1, e & 1);
    };
    wdq(0, 1);
    if (v < 2) WAIT_V(4);
    STAMP_AT(6);
    if (!aux) __builtin_amdgcn_s_barrier();
    wdq(1, 2); wdq(2, 3); wdq(3, 4);
#define SWAP_UNIT(qa, slot, qb) do { WAIT_V(12); take(qa, slot); WAIT_L(); SCHED(); wdq(qb, slot); } while (0)
    SWAP_UNIT(0, 1, 4); SWAP_UNIT(1, 2, 5); SWAP_UNIT(2, 3, 6); SWAP_UNIT(3, 4, 10);
    SWAP_UNIT(4, 1, 7); SWAP_UNIT(5, 2, 8); SWAP_UNIT(6, 3, 9);
    STAMP_AT(7);
    if (!aux) await(sy + S_FRE, peek(sy + S_FRE), 8);
    if (v >= 2) post(sm + S_HP + 4 * (v - 2), 5);
    STAMP_AT(8);
    wdq(11, 5); wdq(12, 6); wdq(13, 7);
#define HDELAY 8
    if (!aux) { await(sy + S_GP, peek(sy + S_GP), 1); __builtin_amdgcn_s_sleep(HDELAY); }
    STAMP_AT(9);
    const u32 vf2 = FL_2 + (ti2 * 28 + 7 * v + (lane < 7 ? lane : 0)) * FS;
    wait_flags<8>(rws, vf2, tlo, thi);
    STAMP_AT(10);
    f32x16 acc[2][2];
#pragma unroll
    for (int a = 0; a < 4; ++a) acc[a >> 1][a & 1] = (f32x16)(0.f);
    constexpr int PD = 4;
    u32x4 hf[28][2];
    auto hload = [&](int i) {
      const u32 so = (ti2 * 112 + ks_of(i >> 1)) * 2048;
#define HAUX 16
      hf[i][0] = __builtin_amdgcn_raw_buffer_load_b128(rws, lane * 16 + 2048 * (i & 1), so, HAUX);
      hf[i][1] = __builtin_amdgcn_raw_buffer_load_b128(rws, lane * 16 + 2048 * (i & 1) + 1024, so, HAUX);
    };
#pragma unroll
    for (int i = 0; i < PD; ++i) hload(i);
    bf16x8 wl[2][2];
    u32x2 fb;
    SCHED();
#pragma unroll
    for (int i = 0; i < 28; ++i) {
      if (i + 1 >= 14 && i + 1 < 28) { wl[(i + 1) & 1][0] = wfrag((i - 11) >> 1, (i + 1) & 1, 0); wl[(i + 1) & 1][1] = wfrag((i - 11) >> 1, (i + 1) & 1, 1); }
      const bf16x8 a0 = i < 14 ? wr_[i < 14 ? i : 0][0] : wl[i & 1][0], a1 = i < 14 ? wr_[i < 14 ? i : 0][1] : wl[i & 1][1];
      const bf16x8 b0 = __builtin_bit_cast(bf16x8, hf[i][0]), b1 = __builtin_bit_cast(bf16x8, hf[i][1]);
      acc[0][0] = MFMA32(a0, b0, acc[0][0]);
      acc[1][0] = MFMA32(a1, b0, acc[1][0]);
      acc[0][1] = MFMA32(a0, b1, acc[0][1]);
      acc[1][1] = MFMA32(a1, b1, acc[1][1]);
      if (i == 6) STAMP_AT(15);
      if (i == 13) STAMP_AT(16);
      if (i == 20) STAMP_AT(17);
      SCHED();
#define ZAT 14
      if (i == ZAT) post(sm + S_H2 + 4 * v, 1);
      if (i == 2) fb = ld8_sc1(rws, vf2, FL_2B);
      if (i == 6) {
        if (__builtin_expect(!__all(fb.x == tlo && fb.y == thi), 0)) wait_flags<4>(rws, vf2 + FL_2B, tlo, thi);
        FENCE();
      }
      if (i < 4) { hload(PD + 2 * i); hload(PD + 2 * i + 1); }
      else if (i + PD + 4 < 28) hload(i + PD + 4);
      SCHED();
    }
    STAMP_AT(11);
#pragma unroll
    for (int g = 0; g < 4; ++g) {
      LAS char* t = sm + RB + UNIT + 4096 * v + (lane & 31) * 128 + (((2 * g + hh) ^ (lane & 7)) << 4);
#pragma unroll
      for (int a = 0; a < 4; ++a) {
        const f32x16& s = acc[a >> 1][a & 1];
        *(LAS f32x4*)(t + UNIT * a) = (f32x4){s[4 * g], s[4 * g + 1], s[4 * g + 2], s[4 * g + 3]};
      }
    }
    STAMP_AT(12);
    BARRIER();
  } else {
    __builtin_amdgcn_s_setprio(1);
    const int c = v >> 1, ah = v & 1, ch = ch0 + 8 * c;
    const int gi = ch * NM + 64 * ah + lane;
    const u32 r6 = p.logdt[ch], r0 = p.are[gi], r1 = p.aim[gi], r2 = p.bre[gi], r3 = p.bim[gi], r4 = p.cre[gi], r5 = p.cim[gi];
    SCHED();
    const int tg = c, fq = ah;
    u32 xa[4], wa[4], iw[4], ir[2];
    {
      const u32 row = 64 * tg + (lane & 31), f = (row >> 1) & 7, scr = RB + 5 * UNIT + 4096 * v, cki = 2 * blk + (p4 >> 1);
#pragma unroll
      for (int s = 0; s < 4; ++s) {
        xa[s] = RB + row * 128 + (((2 * s + hh) ^ f) << 4);
        wa[s] = RB + hh * 4096 + blk * 2048 + q4 * 256 + (((4 * fq + s) ^ (q4 + 4 * blk)) << 5) + p4 * 8;
        iw[s] = scr + lane * 64 + ((s ^ ((lane >> 1) & 3)) << 4);
        asm volatile("" : "+v"(xa[s]), "+v"(wa[s]), "+v"(iw[s]));
      }
      ir[0] = scr + (8 * hh + q4) * 64 + ((cki ^ (q4 >> 1)) << 4) + 8 * (p4 & 1);
      ir[1] = scr + (8 * hh + 4 + q4) * 64 + ((cki ^ (2 + (q4 >> 1))) << 4) + 8 * (p4 & 1);
      asm volatile("" : "+v"(ir[0]), "+v"(ir[1]));
    }
    LOAD_EPOCH();
    MAKE_TAG();
    SCHED();
    if (aux) {
      const int job = 4 * ma + (o - 28), mat = job >= 7, cg = 7 * fh + job - 7 * mat;
      float s[8];
#pragma unroll
      for (int e = 0; e < 8; ++e) s[e] = 0.f;
      if (job < 14) {
        const rsrc_t rw = __builtin_amdgcn_make_buffer_rsrc((void*)(mat ? p.wu : p.wg), 0, D * F * 2, 0x00020000);
        u32 k0 = 0x00003f80u, k1 = 0x3f800000u;
        asm("" : "+v"(k0), "+v"(k1));
        const bf16x2 s0 = __builtin_bit_cast(bf16x2, k0), s1 = __builtin_bit_cast(bf16x2, k1);
        u32x4 t[8];
        const u32 vw = g4 * (F * 2) + (lane & 15) * 16;
        u32 so = 128 * v * (F * 2) + cg * 256;
#pragma unroll
        for (int i = 0; i < 8; ++i) t[i] = __builtin_amdgcn_raw_buffer_load_b128(rw, vw, so + 4 * i * (F * 2), 0);
#pragma unroll 1
        for (int j = 0; j < 4; ++j) {
          so = j < 3 ? so + 32 * (F * 2) : 0x40000000u;
#pragma unroll
          for (int i = 0; i < 8; ++i) {
#pragma unroll
            for (int e = 0; e < 4; ++e) {
              const u32 d = t[i][e];
              s[2 * e] = __builtin_amdgcn_fdot2_f32_bf16(__builtin_bit_cast(bf16x2, d), s0, s[2 * e], false);
              s[2 * e + 1] = __builtin_amdgcn_fdot2_f32_bf16(__builtin_bit_cast(bf16x2, d), s1, s[2 * e + 1], false);
            }
            t[i] = __builtin_amdgcn_raw_buffer_load_b128(rw, vw, so + 4 * i * (F * 2), 0);
            SCHED();
          }
        }
      }
      const float a0 = fold16(fold32(s[0], s[4]), fold32(s[2], s[6])), a1 = fold16(fold32(s[1], s[5]), fold32(s[3], s[7]));
      *(LAS f32x2*)(sm + L_CS + 512 * v + 32 * (lane & 15) + 8 * g4) = (f32x2){a0, a1};
      BARRIER();
      LAS char* cx = sm + L_CS + 128 * v + 4 * (lane & 31);
      const float tot = (*(LAS float*)cx + *(LAS float*)(cx + 512)) + (*(LAS float*)(cx + 1024) + *(LAS float*)(cx + 1536)), th = rbf(tot);
      if (job < 14 && lane < 32) st8_sc1(rws, (u32x2){pk(th, tot - th), tg32}, lane * 8, WS_CS + (mat * F + cg * 128 + 32 * v) * 8);
      STAMP_AT(15);
    }
    bf16x8 yf[8], xf[8];
    {
      const float dt = rbf(__builtin_amdgcn_exp2f(bflo(r6) * 1.44269504088896f));
      const float ar2 = dt * bflo(r0) * 1.44269504088896f, air = dt * bflo(r1) * 0.159154943091895f;
      auto put = [&](int img, int g, u32x4 re, u32x4 im) {
        *(LAS u32x4*)(sm + iw[g] + 2 * img * UNIT) = re; *(LAS u32x4*)(sm + iw[g] + (1 - img) * UNIT) = im;
        SCHED();
      };
      auto get = [&](int img, int s) { const u32 d = (s < 4 ? 2 * img : 1 - img) * UNIT + 1024 * (s & 3); return trfrag(sm + ir[0] + d, sm + ir[1] + d); };
#define P2_(a) pk((a).x, (a).y)
#define P4_(a, b, c, d) (u32x4){P2_(a), P2_(b), P2_(c), P2_(d)}
      STAMP_AT(16);
      const cpx e8 = cexpl(ar2, air, 8.f);
      {
        const cpx s1 = {e8.r, -e8.i}, s2 = csq(s1), s4 = csq(s2), s8 = cexpl(ar2, -air, 64.f), s16 = csq(s8);
        const cp2 y0 = {{1.f, s1.r}, {0.f, s1.i}}, y1 = cmul2(y0, s2), y2 = cmul2(y0, s4), y3 = cmul2(y1, s4);
        put(0, 0, P4_(y0.r, y1.r, y2.r, y3.r), P4_(y0.i, y1.i, y2.i, y3.i));
        const cp2 y4 = cmul2(y0, s8), y5 = cmul2(y1, s8), y6 = cmul2(y2, s8), y7 = cmul2(y3, s8);
        put(0, 1, P4_(y4.r, y5.r, y6.r, y7.r), P4_(y4.i, y5.i, y6.i, y7.i));
        const cp2 y8 = cmul2(y0, s16), y9 = cmul2(y1, s16), ya = cmul2(y2, s16), yb = cmul2(y3, s16);
        put(0, 2, P4_(y8.r, y9.r, ya.r, yb.r), P4_(y8.i, y9.i, ya.i, yb.i));
        const cp2 yc = cmul2(y4, s16), yd = cmul2(y5, s16), ye = cmul2(y6, s16), yg = cmul2(y7, s16);
        put(0, 3, P4_(yc.r, yd.r, ye.r, yg.r), P4_(yc.i, yd.i, ye.i, yg.i));
      }
      {
        const cpx cf = cmul({bflo(r4), bflo(r5)}, {dt * bflo(r2), dt * bflo(r3)});
        const cpx e1 = cexpl(ar2, air, 1.f), e256 = cexpl(ar2, air, 256.f);
        const cpx e2 = csq(e1), e4 = csq(e2), e512 = csq(e256), e1024 = csq(e512);
        const cp2 cb = cmul2({{e1.r, e256.r}, {e1.i, e256.i}}, cf);
        const cp2 a0 = {{cf.r, cb.r.x}, {cf.i, cb.i.x}}, a1 = cmul2(a0, e2);
        const cp2 t0 = cmul2({{e2.r, e4.r}, {e2.i, e4.i}}, {cb.r.y, cb.i.y});
#define XA(m) (u32x4){P2_(a0.m), P2_(a1.m), pk(cb.m.y, cb.m.y), P2_(t0.m)}
#pragma unroll
        for (int s = 0; s < 4; ++s) yf[s] = get(0, s);
        put(1, 0, XA(r), XA(i));
        const cp2 a2 = cmul2(a0, e4), a3 = cmul2(a1, e4), t1 = cmul2(t0, e4);
#define XB(m) (u32x4){P2_(a2.m), P2_(a3.m), pk(t0.m.y, t0.m.y), P2_(t1.m)}
        put(1, 1, XB(r), XB(i));
#pragma unroll
        for (int s = 4; s < 8; ++s) yf[s] = get(0, s);
        const cp2 q0 = {{cf.r, cb.r.y}, {cf.i, cb.i.y}}, q1 = cmul2(q0, e512), q2 = cmul2(q0, e1024);
        const cp2 u4 = cmul2(q1, e4), u8 = cmul2(q1, e8);
#define XC(m) (u32x4){pk(q1.m.x, u4.m.x), pk(u8.m.x, q2.m.x), pk(q1.m.y, u4.m.y), pk(u8.m.y, q2.m.y)}
        put(1, 2, XC(r), XC(i));
        const cp2 q3 = cmul2(q1, e1024), v8 = cmul2(q2, e8), w8 = cmul2(q3, e8);
#define XD(m) (u32x4){pk(v8.m.x, q3.m.x), pk(w8.m.x, 0.f), pk(v8.m.y, q3.m.y), pk(w8.m.y, 0.f)}
        put(1, 3, XD(r), XD(i));
#pragma unroll
        for (int s = 0; s < 8; ++s) xf[s] = get(1, s);
      }
      STAMP_AT(17);
    }
    bf16x8 X0[2], W0[4];
    HOLD(2, 1500)
    if (!aux) BARRIER();
    X0[0] = ldfrag(sm + xa[0]); X0[1] = ldfrag(sm + xa[0] + 4096);
#pragma unroll
    for (int t = 0; t < 4; ++t) W0[t] = trfrag(sm + UNIT + wa[t], sm + UNIT + wa[t] + 1024);
    SCHED();
    {
      f32x16 kacc = (f32x16)(0.f);
#pragma unroll
      for (int s = 0; s < 8; ++s) kacc = MFMA32(xf[s], yf[s], kacc);
#pragma unroll
      for (int g = 0; g < 4; ++g) *(LAS f32x4*)(sm + KCP * v + 32 + 1024 * g + 16 * lane) = (f32x4){kacc[4 * g], kacc[4 * g + 1], kacc[4 * g + 2], kacc[4 * g + 3]};
    }
    SCHED();
    if (aux) BARRIER();
    else {
      f32x16 acc[2][4];
#pragma unroll
      for (int a = 0; a < 8; ++a) acc[a >> 2][a & 3] = (f32x16)(0.f);
      bf16x8 X1[2], W1[4];
      auto step = [&](const bf16x8 (&Xc)[2], const bf16x8 (&Wc)[4], bf16x8 (&Xn)[2], bf16x8 (&Wn)[4], u32 xb, u32 wb, int s, auto mid) {
        LAS char* xp = sm + (xb + xa[s]);
        auto wld = [&](int t) { LAS char* a = sm + (wb + wa[t]) + 8192 * (s & 1); Wn[t] = trfrag(a, a + 1024); };
        acc[0][0] = MFMA32(Wc[0], Xc[0], acc[0][0]); SCHED();
        mid(); wld(0); SCHED();
        acc[1][0] = MFMA32(Wc[0], Xc[1], acc[1][0]); SCHED();
        Xn[0] = ldfrag(xp); Xn[1] = ldfrag(xp + 4096); SCHED();
        acc[0][1] = MFMA32(Wc[1], Xc[0], acc[0][1]); SCHED();
        wld(1); SCHED();
        acc[1][1] = MFMA32(Wc[1], Xc[1], acc[1][1]); SCHED();
        wld(2); SCHED();
        acc[0][2] = MFMA32(Wc[2], Xc[0], acc[0][2]); SCHED();
        wld(3); SCHED();
        acc[1][2] = MFMA32(Wc[2], Xc[1], acc[1][2]); SCHED();
        acc[0][3] = MFMA32(Wc[3], Xc[0], acc[0][3]); SCHED();
        acc[1][3] = MFMA32(Wc[3], Xc[1], acc[1][3]); SCHED();
      };
      u32 xb = 0;
      step(X0, W0, X1, W1, xb, UNIT, 1, [&] {});
#pragma unroll 1
      for (int cc = 0;; ++cc) {
        const u32 w1 = (xb + 2 * UNIT) & (RING - 1);
        step(X1, W1, X0, W0, xb, w1, 2, [&] {});
        step(X0, W0, X1, W1, xb, w1, 3, [&] {});
        if (cc == 7) break;
        xb = (xb + 3 * UNIT) & (RING - 1);
        const u32 w0 = (xb + UNIT) & (RING - 1);
        step(X1, W1, X0, W0, xb, w0, 0, [&] { BARRIER(); });
        step(X0, W0, X1, W1, xb, w0, 1, [&] {});
      }
      STAMP_AT(2);
      HOLD(3, 3000)
      u32x2 st[2], cs[4];
      const u32 ost = 8 * (64 * tg + (lane & 31)), ocs = 8 * (64 * fq + (lane & 15));
#pragma unroll
      for (int t = 0; t < 2; ++t) st[t] = *(LAS u32x2*)(sm + L_ST + ost + 256 * t);
#pragma unroll
      for (int t = 0; t < 4; ++t) cs[t] = *(LAS u32x2*)(sm + L_CS + 1024 * blk + ocs + 128 * t);
      SCHED();
#pragma unroll
      for (int a = 0; a < 5; ++a) acc[a & 1][a >> 1] = MFMA32(W1[a >> 1], X1[a & 1], acc[a & 1][a >> 1]);
      SCHED();
#pragma unroll 1
      for (u32 s = 0; s < SPIN_CAP && __builtin_expect(!__all(st[0].y == tg32 && st[1].y == tg32 && cs[0].y == tg32 && cs[1].y == tg32 && cs[2].y == tg32 && cs[3].y == tg32), 0); ++s) {
        FENCE();
#pragma unroll
        for (int t = 0; t < 2; ++t) st[t] = ld8_sc1(rws, ost + 256 * t, WS_ST + mi * 1024);
#pragma unroll
        for (int t = 0; t < 4; ++t) cs[t] = ld8_sc1(rws, blk * F * 8 + ocs + 128 * t, WS_CS + fj * 1024);
        __builtin_amdgcn_s_sleep(8);
      }
      post(sm + S_FRE + 4 * v, 8);
      STAMP_NW(19);
      bf16x8 XM[2];
      float ka[2], kb[2], kr[2];
      acc[1][2] = MFMA32(W1[2], X1[1], acc[1][2]);
#pragma unroll
      for (int t = 0; t < 2; ++t) { const u32 m = hh ? 0u : (st[t].x & 0xffffu) ^ 0x8000u; XM[t] = __builtin_bit_cast(bf16x8, (u32x4){m | m << 16, 0, 0, 0}); }
      auto corr = [&](int a) { acc[a & 1][a >> 1] = MFMA32(__builtin_bit_cast(bf16x8, (u32x4){cs[a >> 1].x, 0, 0, 0}), XM[a & 1], acc[a & 1][a >> 1]); };
      SCHED();
      corr(0);
      const u32 vh = ((2 * mi + tg) * 112 + 8 * fj + 4 * fq) * 2048 + lane * 16;
#pragma unroll
      for (int t = 0; t < 2; ++t) {
        const float sd = bfhi(st[t].x), r = __builtin_amdgcn_rcpf(sd);
        kb[t] = -2.f * 0.7978845608028654f * 1.44269504088896f * r; ka[t] = kb[t] * 0.044715f * r * r; kr[t] = sd * sd;
      }
      SCHED();
#pragma unroll
      for (int a = 0; a < 8; ++a) {
        const int tt = a & 1, ft = a >> 1;
        f32x2 z[4], gu[4];
#pragma unroll
        for (int e = 0; e < 4; ++e) {
          const f32x2 g = {acc[tt][ft][2 * e], acc[tt][ft][2 * e + 1]}, u = {acc[tt][ft][8 + 2 * e], acc[tt][ft][9 + 2 * e]};
          z[e] = (g * g * ka[tt] + kb[tt]) * g; gu[e] = g * u;
        }
        SCHED();
        if (a < 7) corr(a + 1);
        SCHED();
#pragma unroll
        for (int e = 0; e < 4; ++e) z[e] = (f32x2){__builtin_amdgcn_exp2f(z[e].x), __builtin_amdgcn_exp2f(z[e].y)};
        SCHED();
#pragma unroll
        for (int e = 0; e < 4; ++e) z[e] = z[e] * kr[tt] + kr[tt];
        SCHED();
        if (a < 2) acc[a][3] = MFMA32(W1[3], X1[a], acc[a][3]);
        SCHED();
#pragma unroll
        for (int e = 0; e < 4; ++e) z[e] = (f32x2){__builtin_amdgcn_rcpf(z[e].x), __builtin_amdgcn_rcpf(z[e].y)};
        SCHED();
        u32x4 hv;
#pragma unroll
        for (int e = 0; e < 4; ++e) { const f32x2 hq = gu[e] * z[e]; hv[e] = pk(hq.x, hq.y); }
        st16_sc1(rws, hv, vh + 1024 * a);
#define FLAGA 5
        if (a == 1) STAMP_NW(20);
        if (a == 3) STAMP_NW(21);
        if (a == FLAGA) {
          STAMP_NW(22);
          asm volatile("s_waitcnt vmcnt(%0)" :: "i"(FLAGA - 3) : "memory");
          if (lane == 0) st8_sc1(rws, tag, FL_2 + ((2 * mi + tg) * 28 + 2 * fj + fq) * FS, 0);
          post(sm + S_GP + 4 * v, 1);
          STAMP_NW(18);
        }
        SCHED();
      }
      STAMP_NW(23); STAMP_NWEND();
      STAMP_AT(3);
    }
    LAS char* ka = sm + (4 * c + q4) * KCP + 2 * (28 + 128 * ah + 4 * p4 - 8 * g4);
    const u32 ub0 = 30 - 16 * ah + 2 * (lane & 15) + g4;
    f32x4 y[8];
#pragma unroll
    for (int i = 0; i < 8; ++i) y[i] = (f32x4)(0.f);
    await(sm + S_HP + 4 * (lane & 1), peek(sm + S_HP + 4 * (lane & 1)), 5);
    if (b == 255 && tid == 256) st8_sc1(rws, tag, FL_E, 0);
    const u32 vo = (64 * ti2 + 8 * v + (lane >> 3)) * (D * 2) + n2 * 128 + (lane & 7) * 16;
    u32x4 xres[2];
    STAMP_AT(4);
    bf16x8 A[2][8], B[2][8];
    auto cld = [&](int a, int i) {
      A[a & 1][i] = trfrag(ka + 64 * a + 512 * i + 8, ka + 64 * a + 512 * i);
      B[a & 1][i] = ldfrag(sm + L_U + 32 * (ub0 - 4 * a) + 16 * ((c ^ ((ub0 - 4 * a) >> 3)) & 1) + 1024 * i);
    };
#pragma unroll
    for (int i = 0; i < 8; ++i) cld(0, i);
#pragma unroll
    for (int a = 0; a < 4; ++a) {
#pragma unroll
      for (int s = 0; s < 8; ++s) {
#pragma unroll
        for (int i = 0; i <= s; ++i) {
          if (i + s < 8) y[i + s] = MFMA16(A[a & 1][i], B[a & 1][s], y[i + s]);
          if (i < s && i + s < 8) y[i + s] = MFMA16(A[a & 1][s], B[a & 1][i], y[i + s]);
        }
        SCHED();
        if (a < 3) cld(a + 1, s);
        SCHED();
      }
#define FLAG_AT 0
      if (a == FLAG_AT) {
        STAMP_AT(5);
        WAIT_V(0);
        if (!aux && lane == 0) st8_sc1(rws, tag, FL_2 + ((2 * mi + c) * 28 + 2 * fj + ah) * FS, FL_2B);
        xres[0] = __builtin_amdgcn_raw_buffer_load_b128(rx, vo, 0, 0);
        xres[1] = __builtin_amdgcn_raw_buffer_load_b128(rx, vo, 32 * D * 2, 0);
        STAMP_AT(6);
      }
    }
    STAMP_AT(7);
    LAS char* const pw = sm + S_GP + 4 * (v ^ 1);
    LAS char* ex = sm + 4 * c * KCP + 32;
    post(sm + S_GP + 4 * v, 2);
    await(pw, peek(pw), 2);
    if (ah) {
#pragma unroll
      for (int i = 0; i < 4; ++i) { *(LAS f32x4*)(ex + KCP + 1024 * i + 16 * lane) = y[i]; FENCE(); y[i] = y[4 + i]; }
    } else {
#pragma unroll
      for (int i = 0; i < 4; ++i) *(LAS f32x4*)(ex + 1024 * i + 16 * lane) = y[4 + i];
    }
    post(sm + S_GP + 4 * v, 3);
    await(pw, peek(pw), 3);
    STAMP_AT(8);
#pragma unroll
    for (int i = 0; i < 4; ++i) {
      const f32x4 t = *(LAS f32x4*)(ex + KCP * (ah ^ 1) + 1024 * i + 16 * lane) + y[i];
      st16_sc1(rws, (u32x4){pk(t[0], t[1]), tg32, pk(t[2], t[3]), tg32}, WS_ZT + ch * 8192 + 4096 * ah + 64 * (lane & 15) + 16 * g4 + 1024 * i);
    }
    STAMP_AT(9);
#define ZSLEEP 8
    const u32 vz = (64 * n2 + 16 * v + g4) * 8192 + 256 * ti2 + (lane & 15) * 16;
    u32x4 zz[4];
    await(sy + S_H2, peek(sy + S_H2), 1);
    STAMP_AT(10);
#pragma unroll 1
    for (u32 s = 0; s < SPIN_CAP; ++s) {
      if (s) __builtin_amdgcn_s_sleep(ZSLEEP);
      FENCE();
#pragma unroll
      for (int i = 0; i < 4; ++i) zz[i] = ld16_sc1(rws, vz, WS_ZT + 4 * 8192 * i);
      if (__all(zz[0].y == tg32 && zz[0].w == tg32 && zz[1].y == tg32 && zz[1].w == tg32 && zz[2].y == tg32 && zz[2].w == tg32 && zz[3].y == tg32 && zz[3].w == tg32)) break;
    }
    STAMP_AT(11);
    LAS char* stg = ex + 3 * KCP + 2048 * ah;
#pragma unroll
    for (int i = 0; i < 4; ++i) *(LAS u32x2*)(stg + 512 * i + 8 * lane) = (u32x2){zz[i].x, zz[i].z};
    LAS char* tr = stg + q4 * 128 + 32 * g4 + 8 * p4;
    const bf16x8 ra = trfrag(tr, tr + 512), rc = trfrag(tr + 1024, tr + 1536);
    LAS char* rb = ex + 2 * KCP + 2048 * ah + 32 * lane;
    *(LAS bf16x8*)rb = ra;
    *(LAS bf16x8*)(rb + 16) = rc;
    post(sm + S_GP + 4 * v, 4);
    await(sy + S_GP, peek(sy + S_GP), 4);
    const int tok = 8 * v + (lane >> 3), dc = lane & 7;
    f32x4 s0[2], s1[2];
#pragma unroll
    for (int i = 0; i < 2; ++i) {
      const u32x4 z = *(LAS u32x4*)(sm + (4 * (dc >> 2) + 2) * KCP + 32 + 2048 * ((dc >> 1) & 1) + 32 * tok + 16 * (dc & 1) + 1024 * i), x = xres[i];
      s0[i] = (f32x4){bflo(x[0]) + bflo(z[0]), bfhi(x[0]) + bfhi(z[0]), bflo(x[1]) + bflo(z[1]), bfhi(x[1]) + bfhi(z[1])};
      s1[i] = (f32x4){bflo(x[2]) + bflo(z[2]), bfhi(x[2]) + bfhi(z[2]), bflo(x[3]) + bflo(z[3]), bfhi(x[3]) + bfhi(z[3])};
    }
    u32 t0 = RB + UNIT + UNIT * 2 * (dc >> 2) + tok * 128 + (((2 * (dc & 3)) ^ (tok & 7)) << 4), t1 = t0 ^ 16u;
    char* op = (char*)p.out + vo;
    asm volatile("" : "+v"(s0[0]), "+v"(s0[1]), "+v"(s1[0]), "+v"(s1[1]), "+v"(t0), "+v"(t1), "+v"(op));
    STAMP_AT(12);
    BARRIER();
    STAMP_AT(13);
#pragma unroll
    for (int i = 0; i < 2; ++i) {
#pragma unroll
      for (int g = 0; g < 4; ++g) { s0[i] += *(LAS f32x4*)(sm + t0 + UNIT * i + 4096 * g); s1[i] += *(LAS f32x4*)(sm + t1 + UNIT * i + 4096 * g); }
      *(GAS u32x4*)(op + 32 * D * 2 * i) = (u32x4){pk(s0[i][0], s0[i][1]), pk(s0[i][2], s0[i][3]), pk(s1[i][0], s1[i][1]), pk(s1[i][2], s1[i][3])};
    }
  }
  HOLD(1, 4000) BURN(5)
  STAMP_AT(14); STAMP_OUT();
}

extern "C" void kernel_launch(void* const* d_in, const int* in_sizes, int n_in, void* d_out, int out_size, void* d_ws,
                              size_t ws_size, hipStream_t stream) {
  Params p;
  p.x = (const u16*)d_in[0]; p.are = (const u16*)d_in[1]; p.aim = (const u16*)d_in[2];
  p.bre = (const u16*)d_in[3]; p.bim = (const u16*)d_in[4]; p.cre = (const u16*)d_in[5];
  p.cim = (const u16*)d_in[6]; p.logdt = (const u16*)d_in[7]; p.dskip = (const u16*)d_in[8];
  p.wg = (const u16*)d_in[10]; p.wu = (const u16*)d_in[11]; p.wd = (const u16*)d_in[12];
  p.out = (u16*)d_out; p.ws = (char*)d_ws;
  s4ffn_kernel<<<256, 512, 0, stream>>>(p);
}
```

```cpp
#include <hip/hip_runtime.h>
#include <stdint.h>

typedef unsigned short u16;
typedef unsigned u32;
typedef unsigned long long u64;
typedef __bf16 bf16x2 __attribute__((ext_vector_type(2)));
typedef __bf16 bf16x4 __attribute__((ext_vector_type(4)));
typedef __bf16 bf16x8 __attribute__((ext_vector_type(8)));
typedef float f32x2 __attribute__((ext_vector_type(2)));
typedef float f32x4 __attribute__((ext_vector_type(4)));
typedef float f32x16 __attribute__((ext_vector_type(16)));
typedef u32 u32x2 __attribute__((ext_vector_type(2)));
typedef u32 u32x4 __attribute__((ext_vector_type(4)));
#define LAS __attribute__((address_space(3)))
#define GAS __attribute__((address_space(1)))
#define DI __device__ __forceinline__
typedef __amdgpu_buffer_rsrc_t rsrc_t;

constexpr int L = 2048, D = 512, NM = 128, F = 1792;
constexpr u32 WS_H = 0, WS_U = 8u << 20, WS_ZT = 10u << 20, WS_ST = 14u << 20, WS_CS = WS_ST + 65536, WS_FL = 15u << 20, WS_K = 16u << 20, WS_END = 24u << 20;
constexpr u32 FL_E = WS_FL, FL_1 = WS_FL + 4096, FL_2 = WS_FL + 131072, FS = 128, FL_2B = 131072;
constexpr u32 KCP = 4160, UNIT = 16384, RB = 32768, RING = 8 * UNIT;
constexpr u32 L_U = 8 * KCP;
constexpr u32 L_ST = L_U + 9216;
constexpr u32 L_CS = L_ST + 1024;
constexpr u32 S_FRE = 4128, S_HP = 4144, S_GP = KCP + 4128, S_H2 = KCP + 4144;
constexpr u32 CODE_BYTES = 24576;
constexpr u32 SPIN_CAP = 1u << 16;

struct Params {
  const u16 *x, *are, *aim, *bre, *bim, *cre, *cim, *logdt, *dskip, *wg, *wu, *wd;
  u16* out;
  char* ws;
};

#define STAMP_DECL
#define STAMP_AT(k)
#define STAMP_RT(v)
#define STAMP_NW(k)
#define STAMP_NWEND()
#define STAMP_OUT()

DI float bflo(u32 v) { return __builtin_bit_cast(float, v << 16); }
DI float bfhi(u32 v) { return __builtin_bit_cast(float, v & 0xffff0000u); }
DI u32 pk(float a, float b) { f32x2 f = {a, b}; return __builtin_bit_cast(u32, __builtin_convertvector(f, bf16x2)); }
DI float rbf(float a) { return bflo(pk(a, a)); }
DI void dma4(rsrc_t ra, rsrc_t rb, u32 lds, u32 v0, u32 v1, u32 v2, u32 v3, u32 soff) {
  asm volatile("s_mov_b32 m0, %0\n\ts_nop 0\n\tbuffer_load_dwordx4 %1, %5, %7 offen lds\n\tbuffer_load_dwordx4 %2, %5, %7 offen offset:1024 lds\n\t"
               "buffer_load_dwordx4 %3, %6, %7 offen offset:2048 lds\n\tbuffer_load_dwordx4 %4, %6, %7 offen offset:3072 lds"
               :: "s"(lds), "v"(v0), "v"(v1), "v"(v2), "v"(v3), "s"(ra), "s"(rb), "s"(soff));
}
DI void dma4s(rsrc_t r, u32 lds, u32 v0, u32 v1, u32 v2, u32 v3, u32 soff) {
  asm volatile("s_mov_b32 m0, %0\n\ts_nop 0\n\tbuffer_load_dwordx4 %1, %5, %6 offen sc1 lds\n\tbuffer_load_dwordx4 %2, %5, %6 offen offset:1024 sc1 lds\n\t"
               "buffer_load_dwordx4 %3, %5, %6 offen offset:2048 sc1 lds\n\tbuffer_load_dwordx4 %4, %5, %6 offen offset:3072 sc1 lds"
               :: "s"(lds), "v"(v0), "v"(v1), "v"(v2), "v"(v3), "s"(r), "s"(soff));
}
DI void dma1s(rsrc_t r, u32 lds, u32 v0, u32 soff) {
  asm volatile("s_mov_b32 m0, %0\n\ts_nop 0\n\tbuffer_load_dwordx4 %1, %2, %3 offen sc1 lds" :: "s"(lds), "v"(v0), "s"(r), "s"(soff));
}
#define WAIT_V(n) asm volatile("s_waitcnt vmcnt(" #n ")" ::: "memory")
#define WAIT_L() asm volatile("s_waitcnt lgkmcnt(0)" ::: "memory")
#define BARRIER() asm volatile("s_waitcnt lgkmcnt(0)\n\ts_barrier" ::: "memory")
#define FENCE() asm volatile("" ::: "memory")
#define SCHED() __builtin_amdgcn_sched_barrier(0)
DI bf16x8 ldfrag(LAS char* a) { return *(LAS bf16x8*)a; }
DI bf16x8 trfrag(LAS char* a0, LAS char* a1) {
  bf16x4 lo = __builtin_amdgcn_ds_read_tr16_b64_v4bf16((LAS bf16x4*)a0);
  bf16x4 hi = __builtin_amdgcn_ds_read_tr16_b64_v4bf16((LAS bf16x4*)a1);
  return __builtin_shufflevector(lo, hi, 0, 1, 2, 3, 4, 5, 6, 7);
}
#define MFMA32(a, b, c) __builtin_amdgcn_mfma_f32_32x32x16_bf16(a, b, c, 0, 0, 0)
#define MFMA16(a, b, c) __builtin_amdgcn_mfma_f32_16x16x32_bf16(a, b, c, 0, 0, 0)
DI u32x2 ld8_sc1(rsrc_t r, u32 voff, u32 soff) { return __builtin_amdgcn_raw_buffer_load_b64(r, voff, soff, 16); }
DI u32x4 ld16_sc1(rsrc_t r, u32 voff, u32 soff) { return __builtin_amdgcn_raw_buffer_load_b128(r, voff, soff, 16); }
DI void st8_sc1(rsrc_t r, u32x2 v, u32 voff, u32 soff) { __builtin_amdgcn_raw_buffer_store_b64(v, r, voff, soff, 16); }
DI void st16_sc1(rsrc_t r, u32x4 v, u32 voff) { __builtin_amdgcn_raw_buffer_store_b128(v, r, voff, 0, 16); }
template <int SLEEP> DI void wait_flags(rsrc_t r, u32 voff, u32 tlo, u32 thi) {
#pragma unroll 1
  for (u32 s = 0; s < SPIN_CAP; ++s) {
    FENCE();
    u32x2 f = ld8_sc1(r, voff, 0);
    if (__all(f.x == tlo && f.y == thi)) break;
    __builtin_amdgcn_s_sleep(SLEEP);
  }
  WAIT_V(0);
}
DI void post(LAS char* word, u32 v) {
  asm volatile("s_mov_b64 exec, 1\n\tds_write_b32 %0, %1\n\ts_mov_b64 exec, -1" :: "v"(word), "v"(v) : "memory");
}
DI u32 peek(LAS char* word) { return *(volatile LAS u32*)word; }
DI void await(LAS char* word, u32 seen, u32 need) {
  if (__builtin_expect(__any(seen < need), 0)) {
#pragma unroll 1
    for (u32 s = 0; s < SPIN_CAP * 16; ++s) { __builtin_amdgcn_s_sleep(1); if (!__any(peek(word) < need)) break; }
  }
  FENCE();
}
#define DPPADD(name, ctl) DI float name(float x) { float r; asm("s_nop 1\n\tv_add_f32_dpp %0, %1, %1 " ctl " row_mask:0xf bank_mask:0xf" : "=v"(r) : "v"(x)); return r; }
DPPADD(add_x1, "quad_perm:[1,0,3,2]") DPPADD(add_x2, "quad_perm:[2,3,0,1]") DPPADD(add_m8, "row_half_mirror") DPPADD(add_x8, "row_ror:8")
DI float fold32(float a, float b) {
  asm("s_nop 1\n\tv_permlane32_swap_b32 %0, %1\n\tv_add_f32 %0, %0, %1" : "+v"(a), "+v"(b)); return a;
}
DI float fold16(float a, float b) {
  asm("s_nop 1\n\tv_permlane16_swap_b32 %0, %1\n\tv_add_f32 %0, %0, %1" : "+v"(a), "+v"(b)); return a;
}
DI u32 vmov(u32 a) { u32 r; asm("v_mov_b32 %0, %1" : "=v"(r) : "v"(a)); return r; }
struct cpx { float r, i; };
struct cp2 { f32x2 r, i; };
DI cpx cmul(cpx a, cpx b) { return {a.r * b.r - a.i * b.i, a.r * b.i + a.i * b.r}; }
DI cpx csq(cpx a) { return {a.r * a.r - a.i * a.i, (a.r + a.r) * a.i}; }
DI cp2 cmul2(cp2 a, cpx b) { return {a.r * b.r - a.i * b.i, a.i * b.r + a.r * b.i}; }
DI cpx cexpl(float ar2, float air, float l) {
  float m = __builtin_amdgcn_exp2f(ar2 * l), f = __builtin_amdgcn_fractf(air * l);
  return {m * __builtin_amdgcn_cosf(f), m * __builtin_amdgcn_sinf(f)};
}

__global__ __launch_bounds__(512, 2) void s4ffn_kernel(Params p) {
  __shared__ __attribute__((aligned(16))) char smem[163840];
  LAS char* sm = (LAS char*)smem;
  const u32 smb = (u32)(size_t)sm;
  const int tid = threadIdx.x, lane = tid & 63;
  const int w = __builtin_amdgcn_readfirstlane(tid >> 6), v = w & 3;
  const int b = blockIdx.x, xcd = b & 7, o = b >> 3;
  u32 cpf;
  {
    u64 pc;
    asm volatile("s_getpc_b64 %0" : "=s"(pc));
    const u32 off = (8 * o + w) * 128;
    asm volatile("s_load_dword %0, %1, 0\n\ts_load_dword %0, %1, 64" : "=&s"(cpf) : "s"((pc & ~127ull) + (off < CODE_BYTES ? off : 0)));
    SCHED();
  }
  const bool aux = o >= 28;
  const int ma = xcd >> 1, fh = xcd & 1;
  const int il = o / 7, jl = o - il * 7;
  const int mi = 4 * ma + il, fj = 7 * fh + jl;
  const int ti2 = 4 * xcd + (o >> 3), n2 = o & 7;
  const int tb = 32 * xcd + o, ch0 = 16 * o + xcd;
  const int hh = lane >> 5, blk = (lane >> 4) & 1, q4 = (lane >> 2) & 3, p4 = lane & 3, g4 = lane >> 4;
  STAMP_DECL; STAMP_RT(rt0_); STAMP_AT(0);
#define HOLD(id, ticks)
#define BURN(id)
  BURN(4)
  const rsrc_t rx = __builtin_amdgcn_make_buffer_rsrc((void*)p.x, 0, L * D * 2, 0x00020000);
  const rsrc_t rwg = __builtin_amdgcn_make_buffer_rsrc((void*)p.wg, 0, D * F * 2, 0x00020000);
#define ROT (2 * ma)
#define ROT2 0
  const rsrc_t rws = __builtin_amdgcn_make_buffer_rsrc((void*)p.ws, 0, WS_END, 0x00020000);
  u64 ev;
#define LOAD_EPOCH() asm volatile("s_load_dwordx2 %0, %1, 0\n\ts_waitcnt lgkmcnt(0)" : "=&s"(ev) : "s"(p.ws + FL_E) : "memory")
#define MAKE_TAG() const u32 tlo = (u32)(ev + 0x9E3779B97F4A7C15ull), thi = (u32)((ev + 0x9E3779B97F4A7C15ull) >> 32), tg32 = tlo ^ thi; const u32x2 tag = {tlo, thi}
  asm volatile("s_waitcnt lgkmcnt(0)" :: "s"(cpf), "s"(mi), "s"(fj), "s"(ti2), "s"(ch0));
  LAS char* const sy = sm + 4 * p4;

  if (w < 4) {
  if (w < 2) {
    if (!aux) {
      const rsrc_t rwu2 = __builtin_amdgcn_make_buffer_rsrc((void*)((const char*)p.wu - 2048), 0, D * F * 2 + 2048, 0x00020000);
      const u32 vx0 = (lane >> 3) * 1024 + (((lane & 7) ^ g4) << 4), vx1 = (vx0 ^ 64) + 7168, vx2 = vx0 + 14336, vx3 = vx1 + 14336;
      const u32 vg0 = g4 * (F * 2) + (((((lane & 15) >> 1) ^ g4) << 5) | ((lane & 1) << 4)), vg1 = vg0 + 4 * F * 2 - 1024;
      const u32 vu0 = vg0 ^ 128, vu1 = vu0 + 4 * F * 2 - 1024;
      const u32 sx0 = (mi * 128 + 64 * v) * 1024, sw0 = 16 * v * (F * 2) + fj * 256;
      u32 dst = 8192 * v;
      auto ux = [&](u32 c) {
        const u32 s = sx0 + ((ROT + c) & 7) * 128;
        dma4(rx, rx, smb + RB + dst, vx0, vx1, vx2, vx3, s); dma4(rx, rx, smb + RB + dst + 4096, vx0, vx1, vx2, vx3, s + 32768);
        dst = (dst + UNIT) & (RING - 1);
      };
      auto uw = [&](u32 c, u32 h) {
        const u32 s = sw0 + (2 * ((ROT + c) & 7) + h) * (32 * F * 2);
        dma4(rwg, rwu2, smb + RB + dst, vg0, vg1, vu0, vu1, s); dma4(rwg, rwu2, smb + RB + dst + 4096, vg0, vg1, vu0, vu1, s + 8 * F * 2);
        dst = (dst + UNIT) & (RING - 1);
      };
#define LDELAY 2
      __builtin_amdgcn_s_sleep(LDELAY);
      ux(0); uw(0, 0);
      LOAD_EPOCH();
#pragma unroll 1
      for (int c = 0; c < 7; ++c) {
        uw(c, 1); ux(c + 1); uw(c + 1, 0);
        WAIT_V(16);
        if (c == 0) STAMP_AT(1);
        __builtin_amdgcn_s_barrier();
      }
      uw(7, 1);
      dma1s(rws, smb + L_ST, lane * 16, WS_ST + mi * 1024);
      dma1s(rws, smb + L_CS + 1024 * v, lane * 16, WS_CS + (v * F + fj * 128) * 8);
      STAMP_AT(2);
    } else {
      LOAD_EPOCH();
      const rsrc_t rp = __builtin_amdgcn_make_buffer_rsrc((void*)p.wd, 0, D * F * 2, 0x00020000);
#define PFN 0
      u32 t = 0;
#pragma unroll 1
      for (int q = 0; q < 14; ++q) {
        if (q == 0 || q == 4) __builtin_amdgcn_s_barrier();
        const u32 so = (28 * (o - 28) + 4 * (q % 7) + 2 * (q / 7) + v) * (16 * D * 2);
        if (q < PFN) asm volatile("s_waitcnt vmcnt(2)\n\tbuffer_load_dword %0, %1, %2, %3 offen\n\tbuffer_load_dword %0, %1, %2, %4 offen" : "+v"(t) : "v"(lane * 128), "s"(rp), "s"(so), "s"(so + 8192));
      }
      asm volatile("s_waitcnt vmcnt(0)" : "+v"(t));
      STAMP_AT(2);
    }
  }
    const rsrc_t rwd = __builtin_amdgcn_make_buffer_rsrc((void*)p.wd, 0, D * F * 2, 0x00020000);
    const int rot2 = ROT2;
    if (v >= 2) {
      const int k = v - 2;
      auto bar = [&] { SCHED(); if (!aux) __builtin_amdgcn_s_barrier(); FENCE(); SCHED(); };
      auto pull = [&](int i, int skip) {
        u32 d;
        asm volatile(".set pf_off, 0\n\t.rept 14\n\ts_load_dword %0, %1, pf_off\n\t.set pf_off, pf_off + 64\n\t.endr\n\t"
                     "s_cmp_ge_i32 %2, 28\n\ts_cbranch_scc1 1f\n\ts_barrier\n1:\n\ts_waitcnt lgkmcnt(0)"
                     : "=&s"(d) : "s"((const char*)p.wd + tb * 7168 + 896 * i), "s"(skip) : "scc", "memory");
      };
      auto kcopy = [&](int q) { dma4s(rws, smb + q * KCP + 32, lane * 16, lane * 16, lane * 16, lane * 16, WS_K + b * 16896 + 128 + 8448 * (q >> 2) + 4224 * (q & 1) - 2 * (q & 2)); };
      if (k && lane >= 2 && lane < 32) *(LAS u32x4*)(sm + KCP * (lane >> 2) - 32 + 16 * p4) = (u32x4)(0u);
      if (k == 0) {
        u32x4 xr[8];
#pragma unroll
        for (int i = 0; i < 8; ++i) xr[i] = __builtin_amdgcn_raw_buffer_load_b128(rx, lane * 16 + 1024 * (i & 3), (8 * tb + (i & 4)) * 1024, 0);
        LOAD_EPOCH();
        bar();
        float x[8][8], su[8], sq[8];
#pragma unroll
        for (int r = 0; r < 8; ++r) {
#pragma unroll
          for (int e = 0; e < 4; ++e) { x[r][2 * e] = bflo(xr[r][e]); x[r][2 * e + 1] = bfhi(xr[r][e]); }
          asm("v_add_f32 %0, %2, %3\n\tv_mul_f32 %1, %2, %2\n\tv_add_f32 %0, %0, %4\n\tv_fmac_f32 %1, %3, %3\n\tv_add_f32 %0, %0, %5\n\tv_fmac_f32 %1, %4, %4\n\t"
              "v_add_f32 %0, %0, %6\n\tv_fmac_f32 %1, %5, %5\n\tv_add_f32 %0, %0, %7\n\tv_fmac_f32 %1, %6, %6\n\tv_add_f32 %0, %0, %8\n\tv_fmac_f32 %1, %7, %7\n\t"
              "v_add_f32 %0, %0, %9\n\tv_fmac_f32 %1, %8, %8\n\tv_fmac_f32 %1, %9, %9"
              : "=&v"(su[r]), "=&v"(sq[r])
              : "v"(x[r][0]), "v"(x[r][1]), "v"(x[r][2]), "v"(x[r][3]), "v"(x[r][4]), "v"(x[r][5]), "v"(x[r][6]), "v"(x[r][7]));
        }
        STAMP_AT(1);
        bar();
        MAKE_TAG();
        auto total = [&](const float (&a)[8]) {
          const float lo = add_x8(fold16(fold32(a[0], a[2]), fold32(a[1], a[3]))), hi = add_x8(fold16(fold32(a[4], a[6]), fold32(a[5], a[7])));
          return add_m8(add_x2(add_x1(lane & 8 ? hi : lo)));
        };
        const float mean = total(su) * (1.f / D), nsq = -mean * mean;
        const float var = rbf(__builtin_fmaxf(__builtin_fmaf(total(sq), 1.f / D, nsq), 0.f));
        const u32 gran = pk(mean, __builtin_amdgcn_sqrtf(rbf(var + 1e-5f)));
        const float r = __builtin_amdgcn_rcpf(bfhi(gran)), mr = -bflo(gran) * r;
        if ((lane & 7) == 0) st8_sc1(rws, (u32x2){gran, tg32}, 8 * g4 + 4 * (lane & 8), WS_ST + 64 * tb);
        u32 n[8][4];
#pragma unroll
        for (int i = 0; i < 4; ++i) {
          if (i == 2) bar();
          const int l0 = 16 * (2 * i & 3) + 8 * (i >> 1);
          const u32 ra = __builtin_amdgcn_readlane(__builtin_bit_cast(u32, r), l0), rb = __builtin_amdgcn_readlane(__builtin_bit_cast(u32, r), l0 + 16);
          u32 ma_ = __builtin_amdgcn_readlane(__builtin_bit_cast(u32, mr), l0), mb_ = __builtin_amdgcn_readlane(__builtin_bit_cast(u32, mr), l0 + 16);
          asm volatile("s_nop 1" : "+v"(ma_), "+v"(mb_) : "s"(ra), "s"(rb));
#pragma unroll
          for (int e = 0; e < 8; ++e)
            asm("v_fma_f32 %1, %1, %3, %5\n\tv_fma_f32 %2, %2, %4, %6\n\tv_cvt_pk_bf16_f32 %0, %1, %2"
                : "=v"(n[e][i]), "+v"(x[2 * i][e]), "+v"(x[2 * i + 1][e]) : "s"(ra), "s"(rb), "v"(ma_), "v"(mb_));
        }
#pragma unroll
        for (int e = 0; e < 8; ++e) st16_sc1(rws, (u32x4){n[e][0], n[e][1], n[e][2], n[e][3]}, WS_U + tb * 8192 + lane * 16 + 1024 * e);
        SCHED(); __builtin_amdgcn_s_barrier();
        WAIT_V(0);
        if (lane == 0) st8_sc1(rws, tag, FL_1 + tb * 8, 0);
        STAMP_AT(2);
        pull(6, o - 16); pull(7, o);
      } else {
        const u32 dsk0 = p.dskip[ch0], dsk1 = p.dskip[ch0 + 8];
        LOAD_EPOCH();
        pull(0, o - 16); pull(1, o - 16);
        const u32 sk0 = WS_K + b * 16896 + 128;
#pragma unroll 1
        for (int j = 0; j < 2; ++j) {
        const u32 sk = sk0 + 8448 * j, dsk = j ? dsk1 : dsk0;
        float kv[16];
#pragma unroll
        for (int g = 0; g < 4; ++g) {
          LAS char* s = sm + 2 * KCP * j + 32 + 1024 * g + 16 * lane;
          const f32x4 a = *(LAS f32x4*)s, d = *(LAS f32x4*)(s + KCP);
#pragma unroll
          for (int e = 0; e < 4; ++e) asm("v_add_f32 %0, %1, %2" : "=v"(kv[4 * g + e]) : "v"(a[e]), "v"(d[e]));
        }
        const bool odd = lane & 1;
        if (lane == 0) kv[0] += bflo(dsk);
        u32x4 kc[4];
        kc[0] = (u32x4){pk(kv[0], kv[1]), pk(kv[2], kv[3]), pk(kv[4], kv[5]), pk(kv[6], kv[7])};
        kc[1] = (u32x4){pk(kv[8], kv[8]), pk(kv[8], kv[9]), pk(kv[9], kv[9]), pk(kv[10], kv[10])};
        kc[2] = (u32x4){pk(kv[11], kv[11]), pk(kv[11], kv[11]), pk(odd ? kv[12] : kv[11], kv[12]), pk(kv[12], kv[12])};
        kc[3] = (u32x4){pk(kv[13], kv[13]), pk(kv[13], kv[13]), pk(odd ? kv[14] : kv[13], kv[14]), pk(kv[14], kv[14])};
#pragma unroll
        for (int g = 0; g < 4; ++g) {
          const u32x4 cu = kc[g];
          int py = 0;
          if (g) py = __builtin_amdgcn_update_dpp(0, (int)kc[g ? g - 1 : 0].w, 0x13C, 0xf, 0xf, false);
          py = __builtin_amdgcn_update_dpp(py, (int)cu.w, 0x138, 0xf, 0xf, false);
          const u32x4 sh = {__builtin_amdgcn_alignbit(cu.x, py, 16), __builtin_amdgcn_alignbit(cu.y, cu.x, 16), __builtin_amdgcn_alignbit(cu.z, cu.y, 16), __builtin_amdgcn_alignbit(cu.w, cu.z, 16)};
          __builtin_amdgcn_raw_buffer_store_b128(cu, rws, sk + lane * 16 + 1024 * g, 0, 0);
          __builtin_amdgcn_raw_buffer_store_b128(sh, rws, sk + 4224 + lane * 16 + 1024 * g, 0, 0);
        }
        if (lane < 2) __builtin_amdgcn_raw_buffer_store_b32(0, rws, sk - 4 + 4224 * lane, 0, 0);
        pull(2 + j, o);
        }
        STAMP_AT(3);
        WAIT_V(0);
        kcopy(0); kcopy(1); kcopy(2);
        pull(4, o);
        kcopy(3); kcopy(4);
        pull(5, o);
        kcopy(5); kcopy(6);
      }
      MAKE_TAG();
      const u32 vf1 = FL_1 + (128 * k + 2 * lane) * 8;
      u32x4 f1 = ld16_sc1(rws, vf1, 0);
      const u32 vu0 = (lane >> 1) * 8192 + ((lane ^ g4) & 1) * 16, us = 32 * 8192 - 1024;
      bar();
#define QDELAY 3
      __builtin_amdgcn_s_sleep(QDELAY);
#pragma unroll 1
      for (u32 s = 0; s < SPIN_CAP && !__all(f1.x == tlo && f1.y == thi && f1.z == tlo && f1.w == thi); ++s) {
        FENCE();
        f1 = ld16_sc1(rws, vf1, 0);
        __builtin_amdgcn_s_sleep(16);
      }
      FENCE();
      STAMP_AT(4);
      dma4s(rws, smb + L_U + 1024 + 4096 * k, vu0, vu0 + us, vu0 + 2 * us, vu0 + 3 * us, WS_U + k * 128 * 8192 + tb * 32);
      if (k == 0) dma1s(rws, smb + L_U, lane * 16, 0x40000000u);
      else kcopy(7);
      STAMP_AT(5);
    }
    MAKE_TAG();
    const u32 dq = smb + RB + 4096 * v;
    const u32 vd0 = (lane >> 3) * (D * 2) + (((lane & 7) ^ ((lane >> 2) & 4)) << 4), vd1 = vd0 + 7168, vd2 = vd0 + 14336, vd3 = vd0 + 21504;
    auto ks_of = [&](int q) { int k = q % 7 + rot2; k = k >= 7 ? k - 7 : k; return 28 * v + 4 * k + 2 * (q / 7); };
    auto wdq = [&](int q, int slot) { dma4(rwd, rwd, dq + slot * UNIT, vd0, vd1, vd2, vd3, ks_of(q) * (16 * D * 2) + n2 * 128); };
    const u32 wo0 = RB + 4096 * v + (4 * hh + q4) * 128 + p4 * 8 + ((blk ^ (q4 & 2)) << 5), wo1 = wo0 ^ 64;
    auto wfrag = [&](int slot, int e, int ct) { LAS char* a = sm + slot * UNIT + 2048 * e + (ct ? wo1 : wo0); return trfrag(a, a + 1024); };
    bf16x8 wr_[14][2];
    auto take = [&](int q, int slot) {
#pragma unroll
      for (int e = 0; e < 4; ++e) wr_[2 * q + (e >> 1)][e & 1] = wfrag(slot, e >> 1, e & 1);
    };
    wdq(0, 1);
    if (v < 2) WAIT_V(4);
    STAMP_AT(6);
    if (!aux) __builtin_amdgcn_s_barrier();
    wdq(1, 2); wdq(2, 3); wdq(3, 4);
#define SWAP_UNIT(qa, slot, qb) do { WAIT_V(12); take(qa, slot); WAIT_L(); SCHED(); wdq(qb, slot); } while (0)
    SWAP_UNIT(0, 1, 4); SWAP_UNIT(1, 2, 5); SWAP_UNIT(2, 3, 6); SWAP_UNIT(3, 4, 10);
    SWAP_UNIT(4, 1, 7); SWAP_UNIT(5, 2, 8); SWAP_UNIT(6, 3, 9);
    STAMP_AT(7);
    if (!aux) await(sy + S_FRE, peek(sy + S_FRE), 8);
    if (v >= 2) post(sm + S_HP + 4 * (v - 2), 5);
    STAMP_AT(8);
    wdq(11, 5); wdq(12, 6); wdq(13, 7);
#define HDELAY 8
    if (!aux) { await(sy + S_GP, peek(sy + S_GP), 1); __builtin_amdgcn_s_sleep(HDELAY); }
    STAMP_AT(9);
    const u32 vf2 = FL_2 + (ti2 * 28 + 7 * v + (lane < 7 ? lane : 0)) * FS;
    wait_flags<8>(rws, vf2, tlo, thi);
    STAMP_AT(10);
    f32x16 acc[2][2];
#pragma unroll
    for (int a = 0; a < 4; ++a) acc[a >> 1][a & 1] = (f32x16)(0.f);
    constexpr int PD = 4;
    u32x4 hf[28][2];
    auto hload = [&](int i) {
      const u32 so = (ti2 * 112 + ks_of(i >> 1)) * 2048;
#define HAUX 16
      hf[i][0] = __builtin_amdgcn_raw_buffer_load_b128(rws, lane * 16 + 2048 * (i & 1), so, HAUX);
      hf[i][1] = __builtin_amdgcn_raw_buffer_load_b128(rws, lane * 16 + 2048 * (i & 1) + 1024, so, HAUX);
    };
#pragma unroll
    for (int i = 0; i < PD; ++i) hload(i);
    bf16x8 wl[2][2];
    u32x2 fb;
    SCHED();
#pragma unroll
    for (int i = 0; i < 28; ++i) {
      if (i + 1 >= 14 && i + 1 < 28) { wl[(i + 1) & 1][0] = wfrag((i - 11) >> 1, (i + 1) & 1, 0); wl[(i + 1) & 1][1] = wfrag((i - 11) >> 1, (i + 1) & 1, 1); }
      const bf16x8 a0 = i < 14 ? wr_[i < 14 ? i : 0][0] : wl[i & 1][0], a1 = i < 14 ? wr_[i < 14 ? i : 0][1] : wl[i & 1][1];
      const bf16x8 b0 = __builtin_bit_cast(bf16x8, hf[i][0]), b1 = __builtin_bit_cast(bf16x8, hf[i][1]);
      acc[0][0] = MFMA32(a0, b0, acc[0][0]);
      acc[1][0] = MFMA32(a1, b0, acc[1][0]);
      acc[0][1] = MFMA32(a0, b1, acc[0][1]);
      acc[1][1] = MFMA32(a1, b1, acc[1][1]);
      if (i == 6) STAMP_AT(15);
      if (i == 13) STAMP_AT(16);
      if (i == 20) STAMP_AT(17);
      SCHED();
#define ZAT 14
      if (i == ZAT) post(sm + S_H2 + 4 * v, 1);
      if (i == 2) fb = ld8_sc1(rws, vf2, FL_2B);
      if (i == 6) {
        if (__builtin_expect(!__all(fb.x == tlo && fb.y == thi), 0)) wait_flags<4>(rws, vf2 + FL_2B, tlo, thi);
        FENCE();
      }
      if (i < 4) { hload(PD + 2 * i); hload(PD + 2 * i + 1); }
      else if (i + PD + 4 < 28) hload(i + PD + 4);
      SCHED();
    }
    STAMP_AT(11);
#pragma unroll
    for (int g = 0; g < 4; ++g) {
      LAS char* t = sm + RB + UNIT + 4096 * v + (lane & 31) * 128 + (((2 * g + hh) ^ (lane & 7)) << 4);
#pragma unroll
      for (int a = 0; a < 4; ++a) {
        const f32x16& s = acc[a >> 1][a & 1];
        *(LAS f32x4*)(t + UNIT * a) = (f32x4){s[4 * g], s[4 * g + 1], s[4 * g + 2], s[4 * g + 3]};
      }
    }
    STAMP_AT(12);
    BARRIER();
  } else {
    __builtin_amdgcn_s_setprio(1);
    const int c = v >> 1, ah = v & 1, ch = ch0 + 8 * c;
    const int gi = ch * NM + 64 * ah + lane;
    const u32 r6 = p.logdt[ch], r0 = p.are[gi], r1 = p.aim[gi], r2 = p.bre[gi], r3 = p.bim[gi], r4 = p.cre[gi], r5 = p.cim[gi];
    SCHED();
    const int tg = c, fq = ah;
    u32 xa[4], wa[4], iw[4], ir[2];
    {
      const u32 row = 64 * tg + (lane & 31), f = (row >> 1) & 7, scr = RB + 5 * UNIT + 4096 * v, cki = 2 * blk + (p4 >> 1);
#pragma unroll
      for (int s = 0; s < 4; ++s) {
        xa[s] = RB + row * 128 + (((2 * s + hh) ^ f) << 4);
        wa[s] = RB + hh * 4096 + blk * 2048 + q4 * 256 + (((4 * fq + s) ^ (q4 + 4 * blk)) << 5) + p4 * 8;
        iw[s] = scr + lane * 64 + ((s ^ ((lane >> 1) & 3)) << 4);
        asm volatile("" : "+v"(xa[s]), "+v"(wa[s]), "+v"(iw[s]));
      }
      ir[0] = scr + (8 * hh + q4) * 64 + ((cki ^ (q4 >> 1)) << 4) + 8 * (p4 & 1);
      ir[1] = scr + (8 * hh + 4 + q4) * 64 + ((cki ^ (2 + (q4 >> 1))) << 4) + 8 * (p4 & 1);
      asm volatile("" : "+v"(ir[0]), "+v"(ir[1]));
    }
    LOAD_EPOCH();
    MAKE_TAG();
    SCHED();
    if (aux) {
      const int job = 4 * ma + (o - 28), mat = job >= 7, cg = 7 * fh + job - 7 * mat;
      float s[8];
#pragma unroll
      for (int e = 0; e < 8; ++e) s[e] = 0.f;
      if (job < 14) {
        const rsrc_t rw = __builtin_amdgcn_make_buffer_rsrc((void*)(mat ? p.wu : p.wg), 0, D * F * 2, 0x00020000);
        u32 k0 = 0x00003f80u, k1 = 0x3f800000u;
        asm("" : "+v"(k0), "+v"(k1));
        const bf16x2 s0 = __builtin_bit_cast(bf16x2, k0), s1 = __builtin_bit_cast(bf16x2, k1);
        u32x4 t[8];
        const u32 vw = g4 * (F * 2) + (lane & 15) * 16;
        u32 so = 128 * v * (F * 2) + cg * 256;
#pragma unroll
        for (int i = 0; i < 8; ++i) t[i] = __builtin_amdgcn_raw_buffer_load_b128(rw, vw, so + 4 * i * (F * 2), 0);
#pragma unroll 1
        for (int j = 0; j < 4; ++j) {
          so = j < 3 ? so + 32 * (F * 2) : 0x40000000u;
#pragma unroll
          for (int i = 0; i < 8; ++i) {
#pragma unroll
            for (int e = 0; e < 4; ++e) {
              const u32 d = t[i][e];
              s[2 * e] = __builtin_amdgcn_fdot2_f32_bf16(__builtin_bit_cast(bf16x2, d), s0, s[2 * e], false);
              s[2 * e + 1] = __builtin_amdgcn_fdot2_f32_bf16(__builtin_bit_cast(bf16x2, d), s1, s[2 * e + 1], false);
            }
            t[i] = __builtin_amdgcn_raw_buffer_load_b128(rw, vw, so + 4 * i * (F * 2), 0);
            SCHED();
          }
        }
      }
      const float a0 = fold16(fold32(s[0], s[4]), fold32(s[2], s[6])), a1 = fold16(fold32(s[1], s[5]), fold32(s[3], s[7]));
      *(LAS f32x2*)(sm + L_CS + 512 * v + 32 * (lane & 15) + 8 * g4) = (f32x2){a0, a1};
      BARRIER();
      LAS char* cx = sm + L_CS + 128 * v + 4 * (lane & 31);
      const float tot = (*(LAS float*)cx + *(LAS float*)(cx + 512)) + (*(LAS float*)(cx + 1024) + *(LAS float*)(cx + 1536)), th = rbf(tot);
      if (job < 14 && lane < 32) st8_sc1(rws, (u32x2){pk(th, tot - th), tg32}, lane * 8, WS_CS + (mat * F + cg * 128 + 32 * v) * 8);
      STAMP_AT(15);
    }
    bf16x8 yf[8], xf[8];
    {
      const float dt = rbf(__builtin_amdgcn_exp2f(bflo(r6) * 1.44269504088896f));
      const float ar2 = dt * bflo(r0) * 1.44269504088896f, air = dt * bflo(r1) * 0.159154943091895f;
      auto put = [&](int img, int g, u32x4 re, u32x4 im) {
        *(LAS u32x4*)(sm + iw[g] + 2 * img * UNIT) = re; *(LAS u32x4*)(sm + iw[g] + (1 - img) * UNIT) = im;
        SCHED();
      };
      auto get = [&](int img, int s) { const u32 d = (s < 4 ? 2 * img : 1 - img) * UNIT + 1024 * (s & 3); return trfrag(sm + ir[0] + d, sm + ir[1] + d); };
#define P2_(a) pk((a).x, (a).y)
#define P4_(a, b, c, d) (u32x4){P2_(a), P2_(b), P2_(c), P2_(d)}
      STAMP_AT(16);
      const cpx e8 = cexpl(ar2, air, 8.f);
      {
        const cpx s1 = {e8.r, -e8.i}, s2 = csq(s1), s4 = csq(s2), s8 = cexpl(ar2, -air, 64.f), s16 = csq(s8);
        const cp2 y0 = {{1.f, s1.r}, {0.f, s1.i}}, y1 = cmul2(y0, s2), y2 = cmul2(y0, s4), y3 = cmul2(y1, s4);
        put(0, 0, P4_(y0.r, y1.r, y2.r, y3.r), P4_(y0.i, y1.i, y2.i, y3.i));
        const cp2 y4 = cmul2(y0, s8), y5 = cmul2(y1, s8), y6 = cmul2(y2, s8), y7 = cmul2(y3, s8);
        put(0, 1, P4_(y4.r, y5.r, y6.r, y7.r), P4_(y4.i, y5.i, y6.i, y7.i));
        const cp2 y8 = cmul2(y0, s16), y9 = cmul2(y1, s16), ya = cmul2(y2, s16), yb = cmul2(y3, s16);
        put(0, 2, P4_(y8.r, y9.r, ya.r, yb.r), P4_(y8.i, y9.i, ya.i, yb.i));
        const cp2 yc = cmul2(y4, s16), yd = cmul2(y5, s16), ye = cmul2(y6, s16), yg = cmul2(y7, s16);
        put(0, 3, P4_(yc.r, yd.r, ye.r, yg.r), P4_(yc.i, yd.i, ye.i, yg.i));
      }
      {
        const cpx cf = cmul({bflo(r4), bflo(r5)}, {dt * bflo(r2), dt * bflo(r3)});
        const cpx e1 = cexpl(ar2, air, 1.f), e256 = cexpl(ar2, air, 256.f);
        const cpx e2 = csq(e1), e4 = csq(e2), e512 = csq(e256), e1024 = csq(e512);
        const cp2 cb = cmul2({{e1.r, e256.r}, {e1.i, e256.i}}, cf);
        const cp2 a0 = {{cf.r, cb.r.x}, {cf.i, cb.i.x}}, a1 = cmul2(a0, e2);
        const cp2 t0 = cmul2({{e2.r, e4.r}, {e2.i, e4.i}}, {cb.r.y, cb.i.y});
#define XA(m) (u32x4){P2_(a0.m), P2_(a1.m), pk(cb.m.y, cb.m.y), P2_(t0.m)}
#pragma unroll
        for (int s = 0; s < 4; ++s) yf[s] = get(0, s);
        put(1, 0, XA(r), XA(i));
        const cp2 a2 = cmul2(a0, e4), a3 = cmul2(a1, e4), t1 = cmul2(t0, e4);
#define XB(m) (u32x4){P2_(a2.m), P2_(a3.m), pk(t0.m.y, t0.m.y), P2_(t1.m)}
        put(1, 1, XB(r), XB(i));
#pragma unroll
        for (int s = 4; s < 8; ++s) yf[s] = get(0, s);
        const cp2 q0 = {{cf.r, cb.r.y}, {cf.i, cb.i.y}}, q1 = cmul2(q0, e512), q2 = cmul2(q0, e1024);
        const cp2 u4 = cmul2(q1, e4), u8 = cmul2(q1, e8);
#define XC(m) (u32x4){pk(q1.m.x, u4.m.x), pk(u8.m.x, q2.m.x), pk(q1.m.y, u4.m.y), pk(u8.m.y, q2.m.y)}
        put(1, 2, XC(r), XC(i));
        const cp2 q3 = cmul2(q1, e1024), v8 = cmul2(q2, e8), w8 = cmul2(q3, e8);
#define XD(m) (u32x4){pk(v8.m.x, q3.m.x), pk(w8.m.x, 0.f), pk(v8.m.y, q3.m.y), pk(w8.m.y, 0.f)}
        put(1, 3, XD(r), XD(i));
#pragma unroll
        for (int s = 0; s < 8; ++s) xf[s] = get(1, s);
      }
      STAMP_AT(17);
    }
    bf16x8 X0[2], W0[4];
    HOLD(2, 1500)
    if (!aux) BARRIER();
    X0[0] = ldfrag(sm + xa[0]); X0[1] = ldfrag(sm + xa[0] + 4096);
#pragma unroll
    for (int t = 0; t < 4; ++t) W0[t] = trfrag(sm + UNIT + wa[t], sm + UNIT + wa[t] + 1024);
    SCHED();
    {
      f32x16 kacc = (f32x16)(0.f);
#pragma unroll
      for (int s = 0; s < 8; ++s) kacc = MFMA32(xf[s], yf[s], kacc);
#pragma unroll
      for (int g = 0; g < 4; ++g) *(LAS f32x4*)(sm + KCP * v + 32 + 1024 * g + 16 * lane) = (f32x4){kacc[4 * g], kacc[4 * g + 1], kacc[4 * g + 2], kacc[4 * g + 3]};
    }
    SCHED();
    if (aux) BARRIER();
    else {
      f32x16 acc[2][4];
#pragma unroll
      for (int a = 0; a < 8; ++a) acc[a >> 2][a & 3] = (f32x16)(0.f);
      bf16x8 X1[2], W1[4];
      auto step = [&](const bf16x8 (&Xc)[2], const bf16x8 (&Wc)[4], bf16x8 (&Xn)[2], bf16x8 (&Wn)[4], u32 xb, u32 wb, int s, auto mid) {
        LAS char* xp = sm + (xb + xa[s]);
        auto wld = [&](int t) { LAS char* a = sm + (wb + wa[t]) + 8192 * (s & 1); Wn[t] = trfrag(a, a + 1024); };
        acc[0][0] = MFMA32(Wc[0], Xc[0], acc[0][0]); SCHED();
        mid(); wld(0); SCHED();
        acc[1][0] = MFMA32(Wc[0], Xc[1], acc[1][0]); SCHED();
        Xn[0] = ldfrag(xp); Xn[1] = ldfrag(xp + 4096); SCHED();
        acc[0][1] = MFMA32(Wc[1], Xc[0], acc[0][1]); SCHED();
        wld(1); SCHED();
        acc[1][1] = MFMA32(Wc[1], Xc[1], acc[1][1]); SCHED();
        wld(2); SCHED();
        acc[0][2] = MFMA32(Wc[2], Xc[0], acc[0][2]); SCHED();
        wld(3); SCHED();
        acc[1][2] = MFMA32(Wc[2], Xc[1], acc[1][2]); SCHED();
        acc[0][3] = MFMA32(Wc[3], Xc[0], acc[0][3]); SCHED();
        acc[1][3] = MFMA32(Wc[3], Xc[1], acc[1][3]); SCHED();
      };
      u32 xb = 0;
      step(X0, W0, X1, W1, xb, UNIT, 1, [&] {});
#pragma unroll 1
      for (int cc = 0;; ++cc) {
        const u32 w1 = (xb + 2 * UNIT) & (RING - 1);
        step(X1, W1, X0, W0, xb, w1, 2, [&] {});
        step(X0, W0, X1, W1, xb, w1, 3, [&] {});
        if (cc == 7) break;
        xb = (xb + 3 * UNIT) & (RING - 1);
        const u32 w0 = (xb + UNIT) & (RING - 1);
        step(X1, W1, X0, W0, xb, w0, 0, [&] { BARRIER(); });
        step(X0, W0, X1, W1, xb, w0, 1, [&] {});
      }
      STAMP_AT(2);
      HOLD(3, 3000)
      u32x2 st[2], cs[4];
      const u32 ost = 8 * (64 * tg + (lane & 31)), ocs = 8 * (64 * fq + (lane & 15));
#pragma unroll
      for (int t = 0; t < 2; ++t) st[t] = *(LAS u32x2*)(sm + L_ST + ost + 256 * t);
#pragma unroll
      for (int t = 0; t < 4; ++t) cs[t] = *(LAS u32x2*)(sm + L_CS + 1024 * blk + ocs + 128 * t);
      SCHED();
#pragma unroll
      for (int a = 0; a < 5; ++a) acc[a & 1][a >> 1] = MFMA32(W1[a >> 1], X1[a & 1], acc[a & 1][a >> 1]);
      SCHED();
#pragma unroll 1
      for (u32 s = 0; s < SPIN_CAP && __builtin_expect(!__all(st[0].y == tg32 && st[1].y == tg32 && cs[0].y == tg32 && cs[1].y == tg32 && cs[2].y == tg32 && cs[3].y == tg32), 0); ++s) {
        FENCE();
#pragma unroll
        for (int t = 0; t < 2; ++t) st[t] = ld8_sc1(rws, ost + 256 * t, WS_ST + mi * 1024);
#pragma unroll
        for (int t = 0; t < 4; ++t) cs[t] = ld8_sc1(rws, blk * F * 8 + ocs + 128 * t, WS_CS + fj * 1024);
        __builtin_amdgcn_s_sleep(8);
      }
      post(sm + S_FRE + 4 * v, 8);
      STAMP_NW(19);
      bf16x8 XM[2];
      float ka[2], kb[2], kr[2];
      acc[1][2] = MFMA32(W1[2], X1[1], acc[1][2]);
#pragma unroll
      for (int t = 0; t < 2; ++t) { const u32 m = hh ? 0u : (st[t].x & 0xffffu) ^ 0x8000u; XM[t] = __builtin_bit_cast(bf16x8, (u32x4){m | m << 16, 0, 0, 0}); }
      auto corr = [&](int a) { acc[a & 1][a >> 1] = MFMA32(__builtin_bit_cast(bf16x8, (u32x4){cs[a >> 1].x, 0, 0, 0}), XM[a & 1], acc[a & 1][a >> 1]); };
      SCHED();
      corr(0);
      const u32 vh = ((2 * mi + tg) * 112 + 8 * fj + 4 * fq) * 2048 + lane * 16;
#pragma unroll
      for (int t = 0; t < 2; ++t) {
        const float sd = bfhi(st[t].x), r = __builtin_amdgcn_rcpf(sd);
        kb[t] = -2.f * 0.7978845608028654f * 1.44269504088896f * r; ka[t] = kb[t] * 0.044715f * r * r; kr[t] = sd * sd;
      }
      SCHED();
#pragma unroll
      for (int a = 0; a < 8; ++a) {
        const int tt = a & 1, ft = a >> 1;
        f32x2 z[4], gu[4];
#pragma unroll
        for (int e = 0; e < 4; ++e) {
          const f32x2 g = {acc[tt][ft][2 * e], acc[tt][ft][2 * e + 1]}, u = {acc[tt][ft][8 + 2 * e], acc[tt][ft][9 + 2 * e]};
          z[e] = (g * g * ka[tt] + kb[tt]) * g; gu[e] = g * u;
        }
        SCHED();
        if (a < 7) corr(a + 1);
        SCHED();
#pragma unroll
        for (int e = 0; e < 4; ++e) z[e] = (f32x2){__builtin_amdgcn_exp2f(z[e].x), __builtin_amdgcn_exp2f(z[e].y)};
        SCHED();
#pragma unroll
        for (int e = 0; e < 4; ++e) z[e] = z[e] * kr[tt] + kr[tt];
        SCHED();
        if (a < 2) acc[a][3] = MFMA32(W1[3], X1[a], acc[a][3]);
        SCHED();
#pragma unroll
        for (int e = 0; e < 4; ++e) z[e] = (f32x2){__builtin_amdgcn_rcpf(z[e].x), __builtin_amdgcn_rcpf(z[e].y)};
        SCHED();
        u32x4 hv;
#pragma unroll
        for (int e = 0; e < 4; ++e) { const f32x2 hq = gu[e] * z[e]; hv[e] = pk(hq.x, hq.y); }
        st16_sc1(rws, hv, vh + 1024 * a);
#define FLAGA 5
        if (a == 1) STAMP_NW(20);
        if (a == 3) STAMP_NW(21);
        if (a == FLAGA) {
          STAMP_NW(22);
          asm volatile("s_waitcnt vmcnt(%0)" :: "i"(FLAGA - 3) : "memory");
          if (lane == 0) st8_sc1(rws, tag, FL_2 + ((2 * mi + tg) * 28 + 2 * fj + fq) * FS, 0);
          post(sm + S_GP + 4 * v, 1);
          STAMP_NW(18);
        }
        SCHED();
      }
      STAMP_NW(23); STAMP_NWEND();
      STAMP_AT(3);
    }
    LAS char* ka = sm + (4 * c + q4) * KCP + 2 * (28 + 128 * ah + 4 * p4 - 8 * g4);
    const u32 ub0 = 30 - 16 * ah + 2 * (lane & 15) + g4;
    f32x4 y[8];
#pragma unroll
    for (int i = 0; i < 8; ++i) y[i] = (f32x4)(0.f);
    await(sm + S_HP + 4 * (lane & 1), peek(sm + S_HP + 4 * (lane & 1)), 5);
    if (b == 255 && tid == 256) st8_sc1(rws, tag, FL_E, 0);
    const u32 vo = (64 * ti2 + 8 * v + (lane >> 3)) * (D * 2) + n2 * 128 + (lane & 7) * 16;
    u32x4 xres[2];
    STAMP_AT(4);
    bf16x8 A[2][8], B[2][8];
    auto cld = [&](int a, int i) {
      A[a & 1][i] = trfrag(ka + 64 * a + 512 * i + 8, ka + 64 * a + 512 * i);
      B[a & 1][i] = ldfrag(sm + L_U + 32 * (ub0 - 4 * a) + 16 * ((c ^ ((ub0 - 4 * a) >> 3)) & 1) + 1024 * i);
    };
#pragma unroll
    for (int i = 0; i < 8; ++i) cld(0, i);
#pragma unroll
    for (int a = 0; a < 4; ++a) {
#pragma unroll
      for (int s = 0; s < 8; ++s) {
#pragma unroll
        for (int i = 0; i <= s; ++i) {
          if (i + s < 8) y[i + s] = MFMA16(A[a & 1][i], B[a & 1][s], y[i + s]);
          if (i < s && i + s < 8) y[i + s] = MFMA16(A[a & 1][s], B[a & 1][i], y[i + s]);
        }
        SCHED();
        if (a < 3) cld(a + 1, s);
        SCHED();
      }
#define FLAG_AT 0
      if (a == FLAG_AT) {
        STAMP_AT(5);
        WAIT_V(0);
        if (!aux && lane == 0) st8_sc1(rws, tag, FL_2 + ((2 * mi + c) * 28 + 2 * fj + ah) * FS, FL_2B);
        xres[0] = __builtin_amdgcn_raw_buffer_load_b128(rx, vo, 0, 0);
        xres[1] = __builtin_amdgcn_raw_buffer_load_b128(rx, vo, 32 * D * 2, 0);
        STAMP_AT(6);
      }
    }
    STAMP_AT(7);
    LAS char* const pw = sm + S_GP + 4 * (v ^ 1);
    LAS char* ex = sm + 4 * c * KCP + 32;
    post(sm + S_GP + 4 * v, 2);
    await(pw, peek(pw), 2);
    if (ah) {
#pragma unroll
      for (int i = 0; i < 4; ++i) { *(LAS f32x4*)(ex + KCP + 1024 * i + 16 * lane) = y[i]; FENCE(); y[i] = y[4 + i]; }
    } else {
#pragma unroll
      for (int i = 0; i < 4; ++i) *(LAS f32x4*)(ex + 1024 * i + 16 * lane) = y[4 + i];
    }
    post(sm + S_GP + 4 * v, 3);
    await(pw, peek(pw), 3);
    STAMP_AT(8);
#pragma unroll
    for (int i = 0; i < 4; ++i) {
      const f32x4 t = *(LAS f32x4*)(ex + KCP * (ah ^ 1) + 1024 * i + 16 * lane) + y[i];
      st16_sc1(rws, (u32x4){pk(t[0], t[1]), tg32, pk(t[2], t[3]), tg32}, WS_ZT + ch * 8192 + 4096 * ah + 64 * (lane & 15) + 16 * g4 + 1024 * i);
    }
    STAMP_AT(9);
#define ZSLEEP 8
    const u32 vz = (64 * n2 + 16 * v + g4) * 8192 + 256 * ti2 + (lane & 15) * 16;
    u32x4 zz[4];
    await(sy + S_H2, peek(sy + S_H2), 1);
    STAMP_AT(10);
#pragma unroll 1
    for (u32 s = 0; s < SPIN_CAP; ++s) {
      if (s) __builtin_amdgcn_s_sleep(ZSLEEP);
      FENCE();
#pragma unroll
      for (int i = 0; i < 4; ++i) zz[i] = ld16_sc1(rws, vz, WS_ZT + 4 * 8192 * i);
      if (__all(zz[0].y == tg32 && zz[0].w == tg32 && zz[1].y == tg32 && zz[1].w == tg32 && zz[2].y == tg32 && zz[2].w == tg32 && zz[3].y == tg32 && zz[3].w == tg32)) break;
    }
    STAMP_AT(11);
    LAS char* stg = ex + 3 * KCP + 2048 * ah;
#pragma unroll
    for (int i = 0; i < 4; ++i) *(LAS u32x2*)(stg + 512 * i + 8 * lane) = (u32x2){zz[i].x, zz[i].z};
    LAS char* tr = stg + q4 * 128 + 32 * g4 + 8 * p4;
    const bf16x8 ra = trfrag(tr, tr + 512), rc = trfrag(tr + 1024, tr + 1536);
    LAS char* rb = ex + 2 * KCP + 2048 * ah + 32 * lane;
    *(LAS bf16x8*)rb = ra;
    *(LAS bf16x8*)(rb + 16) = rc;
    post(sm + S_GP + 4 * v, 4);
    await(sy + S_GP, peek(sy + S_GP), 4);
    const int tok = 8 * v + (lane >> 3), dc = lane & 7;
    f32x4 s0[2], s1[2];
#pragma unroll
    for (int i = 0; i < 2; ++i) {
      const u32x4 z = *(LAS u32x4*)(sm + (4 * (dc >> 2) + 2) * KCP + 32 + 2048 * ((dc >> 1) & 1) + 32 * tok + 16 * (dc & 1) + 1024 * i), x = xres[i];
      s0[i] = (f32x4){bflo(x[0]) + bflo(z[0]), bfhi(x[0]) + bfhi(z[0]), bflo(x[1]) + bflo(z[1]), bfhi(x[1]) + bfhi(z[1])};
      s1[i] = (f32x4){bflo(x[2]) + bflo(z[2]), bfhi(x[2]) + bfhi(z[2]), bflo(x[3]) + bflo(z[3]), bfhi(x[3]) + bfhi(z[3])};
    }
    u32 t0 = RB + UNIT + UNIT * 2 * (dc >> 2) + tok * 128 + (((2 * (dc & 3)) ^ (tok & 7)) << 4), t1 = t0 ^ 16u;
    char* op = (char*)p.out + vo;
    asm volatile("" : "+v"(s0[0]), "+v"(s0[1]), "+v"(s1[0]), "+v"(s1[1]), "+v"(t0), "+v"(t1), "+v"(op));
    STAMP_AT(12);
    BARRIER();
    STAMP_AT(13);
#pragma unroll
    for (int i = 0; i < 2; ++i) {
#pragma unroll
      for (int g = 0; g < 4; ++g) { s0[i] += *(LAS f32x4*)(sm + t0 + UNIT * i + 4096 * g); s1[i] += *(LAS f32x4*)(sm + t1 + UNIT * i + 4096 * g); }
      *(GAS u32x4*)(op + 32 * D * 2 * i) = (u32x4){pk(s0[i][0], s0[i][1]), pk(s0[i][2], s0[i][3]), pk(s1[i][0], s1[i][1]), pk(s1[i][2], s1[i][3])};
    }
  }
  HOLD(1, 4000) BURN(5)
  STAMP_AT(14); STAMP_OUT();
}

#define WSOFF 0
extern "C" void kernel_launch(void* const* d_in, const int* in_sizes, int n_in, void* d_out, int out_size, void* d_ws,
                              size_t ws_size, hipStream_t stream) {
  Params p;
  p.x = (const u16*)d_in[0]; p.are = (const u16*)d_in[1]; p.aim = (const u16*)d_in[2];
  p.bre = (const u16*)d_in[3]; p.bim = (const u16*)d_in[4]; p.cre = (const u16*)d_in[5];
  p.cim = (const u16*)d_in[6]; p.logdt = (const u16*)d_in[7]; p.dskip = (const u16*)d_in[8];
  p.wg = (const u16*)d_in[10]; p.wu = (const u16*)d_in[11]; p.wd = (const u16*)d_in[12];
  p.out = (u16*)d_out; p.ws = (char*)d_ws + ((size_t)WSOFF << 20);
  s4ffn_kernel<<<256, 512, 0, stream>>>(p);
}
```

```cpp
#include <hip/hip_runtime.h>
#include <stdint.h>

typedef unsigned short u16;
typedef unsigned u32;
typedef unsigned long long u64;
typedef __bf16 bf16x2 __attribute__((ext_vector_type(2)));
typedef __bf16 bf16x4 __attribute__((ext_vector_type(4)));
typedef __bf16 bf16x8 __attribute__((ext_vector_type(8)));
typedef float f32x2 __attribute__((ext_vector_type(2)));
typedef float f32x4 __attribute__((ext_vector_type(4)));
typedef float f32x16 __attribute__((ext_vector_type(16)));
typedef u32 u32x2 __attribute__((ext_vector_type(2)));
typedef u32 u32x4 __attribute__((ext_vector_type(4)));
#define LAS __attribute__((address_space(3)))
#define GAS __attribute__((address_space(1)))
#define DI __device__ __forceinline__
typedef __amdgpu_buffer_rsrc_t rsrc_t;

constexpr int L = 2048, D = 512, NM = 128, F = 1792;
constexpr u32 WS_H = 0, WS_U = 8u << 20, WS_ZT = 10u << 20, WS_ST = 14u << 20, WS_CS = WS_ST + 65536, WS_FL = 15u << 20, WS_END = 16u << 20;
constexpr u32 FL_E = WS_FL, FL_1 = WS_FL + 4096, FL_A = WS_FL + 8192, FL_2 = WS_FL + 131072, FL_2B = 131072, FS = 128;
constexpr u32 KCP = 4160, UNIT = 16384, RB = 32768, RING = 8 * UNIT;
constexpr u32 L_K = 0, L_U = 8 * KCP, US = 4608;
constexpr u32 S_GP = 4128, S_HP = 4144;
constexpr u32 L_P = 320, PS = 3840;
constexpr u32 L_NU0 = 15872, L_NUL = L_U + 2 * US;
constexpr u32 L_ST = 0;
constexpr u32 L_CS = KCP;
constexpr u32 CODE_BYTES = 27904;
constexpr u32 SPIN_CAP = 1u << 16;

struct Params {
  const u16 *x, *are, *aim, *bre, *bim, *cre, *cim, *logdt, *dskip, *wg, *wu, *wd;
  u16* out;
  char* ws;
};

#define STAMP_DECL
#define STAMP_AT(k)
#define STAMP_RT(v)
#define STAMP_NW(k)
#define STAMP_NWEND()
#define STAMP_OUT()

DI float bflo(u32 v) { return __builtin_bit_cast(float, v << 16); }
DI float bfhi(u32 v) { return __builtin_bit_cast(float, v & 0xffff0000u); }
DI u32 pk(float a, float b) { f32x2 f = {a, b}; return __builtin_bit_cast(u32, __builtin_convertvector(f, bf16x2)); }
DI float rbf(float a) { return bflo(pk(a, a)); }
DI void dma4(rsrc_t ra, rsrc_t rb, u32 lds, u32 v0, u32 v1, u32 v2, u32 v3, u32 soff) {
  asm volatile("s_mov_b32 m0, %0\n\ts_nop 0\n\tbuffer_load_dwordx4 %1, %5, %7 offen lds\n\tbuffer_load_dwordx4 %2, %5, %7 offen offset:1024 lds\n\t"
               "buffer_load_dwordx4 %3, %6, %7 offen offset:2048 lds\n\tbuffer_load_dwordx4 %4, %6, %7 offen offset:3072 lds"
               :: "s"(lds), "v"(v0), "v"(v1), "v"(v2), "v"(v3), "s"(ra), "s"(rb), "s"(soff));
}
DI void dma4s(rsrc_t r, u32 lds, u32 v0, u32 v1, u32 v2, u32 v3, u32 soff) {
  asm volatile("s_mov_b32 m0, %0\n\ts_nop 0\n\tbuffer_load_dwordx4 %1, %5, %6 offen sc1 lds\n\tbuffer_load_dwordx4 %2, %5, %6 offen offset:1024 sc1 lds\n\t"
               "buffer_load_dwordx4 %3, %5, %6 offen offset:2048 sc1 lds\n\tbuffer_load_dwordx4 %4, %5, %6 offen offset:3072 sc1 lds"
               :: "s"(lds), "v"(v0), "v"(v1), "v"(v2), "v"(v3), "s"(r), "s"(soff));
}
DI void dma1s(rsrc_t r, u32 lds, u32 v0, u32 soff) {
  asm volatile("s_mov_b32 m0, %0\n\ts_nop 0\n\tbuffer_load_dwordx4 %1, %2, %3 offen sc1 lds" :: "s"(lds), "v"(v0), "s"(r), "s"(soff));
}
#define WAIT_V(n) asm volatile("s_waitcnt vmcnt(" #n ")" ::: "memory")
#define WAIT_L() asm volatile("s_waitcnt lgkmcnt(0)" ::: "memory")
#define BARRIER() asm volatile("s_waitcnt lgkmcnt(0)\n\ts_barrier" ::: "memory")
#define FENCE() asm volatile("" ::: "memory")
#define SCHED() __builtin_amdgcn_sched_barrier(0)
DI bf16x8 ldfrag(LAS char* a) { return *(LAS bf16x8*)a; }
DI bf16x8 trfrag(LAS char* a0, LAS char* a1) {
  bf16x4 lo = __builtin_amdgcn_ds_read_tr16_b64_v4bf16((LAS bf16x4*)a0);
  bf16x4 hi = __builtin_amdgcn_ds_read_tr16_b64_v4bf16((LAS bf16x4*)a1);
  return __builtin_shufflevector(lo, hi, 0, 1, 2, 3, 4, 5, 6, 7);
}
#define MFMA32(a, b, c) __builtin_amdgcn_mfma_f32_32x32x16_bf16(a, b, c, 0, 0, 0)
#define MFMA16(a, b, c) __builtin_amdgcn_mfma_f32_16x16x32_bf16(a, b, c, 0, 0, 0)
DI u32x2 ld8_sc1(rsrc_t r, u32 voff, u32 soff) { return __builtin_amdgcn_raw_buffer_load_b64(r, voff, soff, 16); }
DI u32x4 ld16_sc1(rsrc_t r, u32 voff, u32 soff) { return __builtin_amdgcn_raw_buffer_load_b128(r, voff, soff, 16); }
DI void st8_sc1(rsrc_t r, u32x2 v, u32 voff, u32 soff) { __builtin_amdgcn_raw_buffer_store_b64(v, r, voff, soff, 16); }
DI void st16_sc1(rsrc_t r, u32x4 v, u32 voff) { __builtin_amdgcn_raw_buffer_store_b128(v, r, voff, 0, 16); }
template <int SLEEP> DI void wait_flags(rsrc_t r, u32 voff, u32 tlo, u32 thi) {
#pragma unroll 1
  for (u32 s = 0; s < SPIN_CAP; ++s) {
    FENCE();
    u32x2 f = ld8_sc1(r, voff, 0);
    if (__all(f.x == tlo && f.y == thi)) break;
    __builtin_amdgcn_s_sleep(SLEEP);
  }
  WAIT_V(0);
}
DI void post(LAS char* word, u32 v) {
  asm volatile("s_mov_b64 exec, 1\n\tds_write_b32 %0, %1\n\ts_mov_b64 exec, -1" :: "v"(word), "v"(v) : "memory");
}
DI u32 peek(LAS char* word) { return *(volatile LAS u32*)word; }
template <int SLEEP = 1> DI void await(LAS char* word, u32 seen, u32 need) {
  if (__builtin_expect(__any(seen < need), 0)) {
#pragma unroll 1
    for (u32 s = 0; s < SPIN_CAP * 16; ++s) { __builtin_amdgcn_s_sleep(SLEEP); if (!__any(peek(word) < need)) break; }
  }
  FENCE();
}
#define DPPADD(name, ctl) DI float name(float x) { float r; asm("s_nop 1\n\tv_add_f32_dpp %0, %1, %1 " ctl " row_mask:0xf bank_mask:0xf" : "=v"(r) : "v"(x)); return r; }
DPPADD(add_x1, "quad_perm:[1,0,3,2]") DPPADD(add_x2, "quad_perm:[2,3,0,1]") DPPADD(add_m8, "row_half_mirror") DPPADD(add_x8, "row_ror:8")
DI float fold32(float a, float b) {
  asm("s_nop 1\n\tv_permlane32_swap_b32 %0, %1\n\tv_add_f32 %0, %0, %1" : "+v"(a), "+v"(b)); return a;
}
DI float fold16(float a, float b) {
  asm("s_nop 1\n\tv_permlane16_swap_b32 %0, %1\n\tv_add_f32 %0, %0, %1" : "+v"(a), "+v"(b)); return a;
}
DI u32 vmov(u32 a) { u32 r; asm("v_mov_b32 %0, %1" : "=v"(r) : "v"(a)); return r; }
struct cpx { float r, i; };
struct cp2 { f32x2 r, i; };
DI cpx cmul(cpx a, cpx b) { return {a.r * b.r - a.i * b.i, a.r * b.i + a.i * b.r}; }
DI cpx csq(cpx a) { return {a.r * a.r - a.i * a.i, (a.r + a.r) * a.i}; }
DI cp2 cmul2(cp2 a, cpx b) { return {a.r * b.r - a.i * b.i, a.i * b.r + a.r * b.i}; }
DI cpx cexpl(float ar2, float air, float l) {
  float m = __builtin_amdgcn_exp2f(ar2 * l), f = __builtin_amdgcn_fractf(air * l);
  return {m * __builtin_amdgcn_cosf(f), m * __builtin_amdgcn_sinf(f)};
}

__global__ __launch_bounds__(512, 2) void s4ffn_kernel(Params p) {
  __shared__ __attribute__((aligned(16))) char smem[163840];
  LAS char* sm = (LAS char*)smem;
  const u32 smb = (u32)(size_t)sm;
  const int tid = threadIdx.x, lane = tid & 63;
  const int w = __builtin_amdgcn_readfirstlane(tid >> 6), v = w & 3;
  const int b = blockIdx.x, xcd = b & 7, o = b >> 3;
  u32 cpf;
  {
    u64 pc;
    asm volatile("s_getpc_b64 %0" : "=s"(pc));
    const u32 off = (8 * o + w) * 128;
    asm volatile("s_load_dword %0, %1, 0\n\ts_load_dword %0, %1, 64" : "=&s"(cpf) : "s"((pc & ~127ull) + (off < CODE_BYTES ? off : 0)));
    SCHED();
  }
  const bool aux = o >= 28;
  const int ma = xcd >> 1, fh = xcd & 1;
  const int il = o / 7, jl = o - il * 7;
  const int mi = 4 * ma + il, fj = 7 * fh + jl;
  const int ti2 = 4 * xcd + (o >> 3), n2 = o & 7;
  const int ch0 = 64 * xcd + 2 * o;
  const int hh = lane >> 5, blk = (lane >> 4) & 1, q4 = (lane >> 2) & 3, p4 = lane & 3, g4 = lane >> 4;
  STAMP_DECL; STAMP_RT(rt0_); STAMP_AT(0);
  const rsrc_t rx = __builtin_amdgcn_make_buffer_rsrc((void*)p.x, 0, L * D * 2, 0x00020000);
  const rsrc_t rws = __builtin_amdgcn_make_buffer_rsrc((void*)p.ws, 0, WS_END, 0x00020000);
  asm volatile("s_waitcnt lgkmcnt(0)" :: "s"(cpf), "s"(mi), "s"(fj), "s"(ti2), "s"(ch0));
  u64 ev;
#define LOAD_EPOCH() asm volatile("s_load_dwordx2 %0, %1, 0\n\ts_waitcnt lgkmcnt(0)" : "=&s"(ev) : "s"(p.ws + FL_E) : "memory")
#define MAKE_TAG() const u32 tlo = (u32)(ev + 0x9E3779B97F4A7C15ull), thi = (u32)((ev + 0x9E3779B97F4A7C15ull) >> 32), tg32 = tlo ^ thi; const u32x2 tag = {tlo, thi}
  LAS char* const sy = sm + 4 * p4;
  if (w == 3 && lane < 2) *(LAS u32x4*)(sm + S_GP + 16 * lane) = (u32x4)(0u);

  if (__builtin_expect(aux, 0)) {
    const int ab = 8 * ma + 4 * fh + o - 28, rb = 8 * ab + w;
    u32x4 xr[8];
#pragma unroll
    for (int i = 0; i < 8; ++i) xr[i] = __builtin_amdgcn_raw_buffer_load_b128(rx, lane * 16 + 1024 * (i & 3), (8 * rb + (i & 4)) * 1024, 0);
    LOAD_EPOCH();
    MAKE_TAG();
          u32 one = 0x3f803f80u;
          asm("" : "+v"(one));
          float su[8], sq[8];
#pragma unroll
          for (int r = 0; r < 8; ++r) {
            su[r] = sq[r] = 0.f;
#pragma unroll
            for (int e = 0; e < 4; ++e) {
              const u32 d = xr[r][e];
              su[r] = __builtin_amdgcn_fdot2_f32_bf16(__builtin_bit_cast(bf16x2, d), __builtin_bit_cast(bf16x2, one), su[r], false);
              sq[r] = __builtin_amdgcn_fdot2_f32_bf16(__builtin_bit_cast(bf16x2, d), __builtin_bit_cast(bf16x2, d), sq[r], false);
            }
          }
          asm volatile("s_nop 2" : "+v"(su[0]), "+v"(su[1]), "+v"(su[2]), "+v"(su[3]), "+v"(su[4]), "+v"(su[5]), "+v"(su[6]), "+v"(su[7]),
                                   "+v"(sq[0]), "+v"(sq[1]), "+v"(sq[2]), "+v"(sq[3]), "+v"(sq[4]), "+v"(sq[5]), "+v"(sq[6]), "+v"(sq[7]));
          auto total = [&](const float (&a)[8]) {
            const float lo = add_x8(fold16(fold32(a[0], a[2]), fold32(a[1], a[3]))), hi = add_x8(fold16(fold32(a[4], a[6]), fold32(a[5], a[7])));
            return add_m8(add_x2(add_x1(lane & 8 ? hi : lo)));
          };
          const float mean = total(su) * (1.f / D), nsq = -mean * mean;
          const float var = rbf(__builtin_fmaxf(__builtin_fmaf(total(sq), 1.f / D, nsq), 0.f));
          const u32 gran = pk(mean, __builtin_amdgcn_sqrtf(rbf(var + 1e-5f)));
          const float r = __builtin_amdgcn_rcpf(bfhi(gran)), mr = -bflo(gran) * r;
          u32 n[8][4];
#pragma unroll
          for (int i = 0; i < 4; ++i) {
            const int l0 = 16 * (2 * i & 3) + 8 * (i >> 1);
            auto at = [&](float t, int l) { return __builtin_bit_cast(float, __builtin_amdgcn_readlane(__builtin_bit_cast(int, t), l)); };
            const f32x2 r2 = {at(r, l0), at(r, l0 + 16)}, m2 = {at(mr, l0), at(mr, l0 + 16)};
#pragma unroll
            for (int e = 0; e < 4; ++e) {
              const u32 a = xr[2 * i][e], c = xr[2 * i + 1][e];
              const f32x2 lo = (f32x2){bflo(a), bflo(c)} * r2 + m2, hi = (f32x2){bfhi(a), bfhi(c)} * r2 + m2;
              n[2 * e][i] = pk(lo.x, lo.y); n[2 * e + 1][i] = pk(hi.x, hi.y);
            }
          }
    if ((lane & 7) == 0) st8_sc1(rws, (u32x2){gran, tg32}, 8 * g4 + 4 * (lane & 8), WS_ST + 64 * rb);
#pragma unroll
    for (int e = 0; e < 8; ++e) *(LAS u32x4*)(sm + RB + (8 * lane + e) * 128 + ((w ^ (lane & 7)) << 4)) = (u32x4){n[e][0], n[e][1], n[e][2], n[e][3]};
    STAMP_AT(18);
    BARRIER();
#pragma unroll
    for (int e = 0; e < 8; ++e) {
      const u32 row = 64 * w + 8 * e + (lane >> 3);
      st16_sc1(rws, *(LAS u32x4*)(sm + RB + row * 128 + (((lane & 7) ^ e) << 4)), WS_U + row * 4096 + ab * 128 + (lane & 7) * 16);
    }
    WAIT_V(0);
    if (lane == 0) st8_sc1(rws, tag, FL_1 + (w * 32 + ab) * 8, 0);
    STAMP_AT(19);
  }

  if (w < 4) {
    const int k = v & 1;
    const rsrc_t rwd = __builtin_amdgcn_make_buffer_rsrc((void*)p.wd, 0, D * F * 2, 0x00020000);
#define ROT2 0
    const int rot2 = ROT2;
    const u32 dq = smb + RB + 4096 * v;
    const u32 vd0 = (lane >> 3) * (D * 2) + (((lane & 7) ^ ((lane >> 2) & 4)) << 4), vd1 = vd0 + 7168, vd2 = vd0 + 14336, vd3 = vd0 + 21504;
    auto ks_of = [&](int q) { int j = q % 7 + rot2; j = j >= 7 ? j - 7 : j; return 28 * v + 4 * j + 2 * (q / 7); };
    const u32 sd0 = 28 * v * (16 * D * 2) + n2 * 128;
    auto wdq = [&](int q, int slot) {
      u32 so = sd0;
      asm volatile("" : "+s"(so));
      dma4(rwd, rwd, dq + slot * UNIT, vd0, vd1, vd2, vd3, so + (ks_of(q) - 28 * v) * (16 * D * 2));
    };
    const u32 wo0 = RB + 4096 * v + (4 * hh + q4) * 128 + p4 * 8 + ((blk ^ (q4 & 2)) << 5), wo1 = wo0 ^ 64;
    auto wfrag = [&](int slot, int e, int ct) { LAS char* a = sm + slot * (int)UNIT + 2048 * e + (ct ? wo1 : wo0); return trfrag(a, a + 1024); };
    bf16x8 wr_[14][2];
    auto take = [&](int q, int slot) {
#pragma unroll
      for (int e = 0; e < 4; ++e) wr_[2 * q + (e >> 1)][e & 1] = wfrag(slot, e >> 1, e & 1);
    };
    u32x4 kc[4][4];
    if (v < 2 && !aux) {
        const rsrc_t rwg = __builtin_amdgcn_make_buffer_rsrc((void*)p.wg, 0, D * F * 2, 0x00020000);
        const rsrc_t rwu2 = __builtin_amdgcn_make_buffer_rsrc((void*)((const char*)p.wu - 2048), 0, D * F * 2 + 2048, 0x00020000);
        const u32 vx0 = (lane >> 3) * 1024 + (((lane & 7) ^ g4) << 4), vx1 = (vx0 ^ 64) + 7168, vx2 = vx0 + 14336, vx3 = vx1 + 14336;
        const u32 vg0 = g4 * (F * 2) + (((((lane & 15) >> 1) ^ g4) << 5) | ((lane & 1) << 4)), vg1 = vg0 + 4 * F * 2 - 1024;
        const u32 vu0 = vg0 ^ 128, vu1 = vu0 + 4 * F * 2 - 1024;
        const u32 sx0 = (mi * 128 + 64 * v) * 1024, sw0 = 16 * v * (F * 2) + fj * 256;
        u32 dst = 8192 * v;
        auto ux = [&](u32 c) {
          const u32 s = sx0 + ((2 * ma + c) & 7) * 128;
          dma4(rx, rx, smb + RB + dst, vx0, vx1, vx2, vx3, s); dma4(rx, rx, smb + RB + dst + 4096, vx0, vx1, vx2, vx3, s + 32768);
          dst = (dst + UNIT) & (RING - 1);
        };
        auto uw = [&](u32 c, u32 h) {
          const u32 s = sw0 + (2 * ((2 * ma + c) & 7) + h) * (32 * F * 2);
          dma4(rwg, rwu2, smb + RB + dst, vg0, vg1, vu0, vu1, s); dma4(rwg, rwu2, smb + RB + dst + 4096, vg0, vg1, vu0, vu1, s + 8 * F * 2);
          dst = (dst + UNIT) & (RING - 1);
        };
#define LDELAY 2
#define KDELAY 5
      __builtin_amdgcn_s_sleep(LDELAY);
#define COOP 0
      if (COOP) {
        auto gx = [&](int u, int j) { const u32 g = (jl + j) & 3; dma4(rx, rx, smb + RB + u * UNIT + 4096 * g, vx0, vx1, vx2, vx3, (mi * 128 + 32 * g) * 1024 + (2 * ma + u / 3) * 128); };
        auto gw = [&](int u, int j) { const u32 g = (il + j) & 3; dma4(rwg, rwu2, smb + RB + u * UNIT + 4096 * g, vg0, vg1, vu0, vu1, (128 * ma + 32 * (u - 1 - u / 3) + 8 * g) * (F * 2) + fj * 256); };
        gw(1 + v, 0); gx(3 * v, 0);
        if (v) gx(0, 1); else gw(4, 0);
        gx(0, 2 + v); gw(1 + v, 1);
        LOAD_EPOCH();
        gw(1 + v, 2); gw(1 + v, 3);
        if (v) { gx(3, 1); gx(3, 2); gx(3, 3); } else { gw(4, 1); gw(4, 2); gw(4, 3); }
        dst += 5 * UNIT;
      } else {
        ux(0); uw(0, 0);
        LOAD_EPOCH();
        uw(0, 1); ux(1); uw(1, 0);
      }
#pragma unroll
      for (int c = 0; c < 7; ++c) {
        if (c) { uw(c, 1); ux(c + 1); WAIT_V(16); take(c - 1, -1); uw(c + 1, 0); WAIT_L(); SCHED(); }
        wdq(c, -1);
        if (c == 0 && COOP) WAIT_V(16); else WAIT_V(20);
        if (c == 0) STAMP_AT(1);
        __builtin_amdgcn_s_barrier();
        if (c == 0) __builtin_amdgcn_s_sleep(KDELAY);
      }
      uw(7, 1);
      WAIT_V(8); take(6, -1);
    } else {
      const bool wrk = v >= 2, csm = v == 3 && !aux;
      const int mat = il >> 1, r8 = lane >> 3;
      LAS char* const cp = sm + RB + UNIT + mat * 2048 + r8 * 256 + (((4 * (il & 1) + ((lane & 7) >> 1)) ^ ((r8 & 3) + 4 * mat)) << 5) + (lane & 1) * 16;
      const u32 vf1 = FL_1 + (32 * xcd + (lane & 31)) * 8;
      auto stats = [&] {
        dma4s(rws, smb + L_CS + 4096 * k, lane * 16, lane * 16, lane * 16, lane * 16, WS_CS + (fj * 2 + k) * 4096);
        if (k) dma1s(rws, smb + L_ST, lane * 16, WS_ST + mi * 1024);
      };
      float s[8]; u32x4 ct[16]; u32x2 f1; u32 dsk;
      auto cadd = [&](int i) {
#pragma unroll
        for (int e = 0; e < 4; ++e) {
          if (i == 0) { s[2 * e] = bflo(ct[0][e]); s[2 * e + 1] = bfhi(ct[0][e]); }
          else asm("v_add_f32 %0, %0, %2\n\tv_add_f32 %1, %1, %3" : "+v"(s[2 * e]), "+v"(s[2 * e + 1]) : "v"(bflo(ct[i][e])), "v"(bfhi(ct[i][e])));
        }
      };
#define TCH 3
#define TDELAY 12
      if (TDELAY) __builtin_amdgcn_s_sleep(TDELAY);
      if ((TCH & 1) && v == 2) asm volatile("s_mov_b32 m0, %0\n\ts_nop 0\n\tbuffer_load_dword %1, %2, %3 offen lds" :: "s"(smb + L_NU0), "v"(lane < 56 ? lane * 128 : 0), "s"(rwd), "s"(b * 7168));
      if ((TCH & 2) && csm) {
        const rsrc_t rw = __builtin_amdgcn_make_buffer_rsrc((void*)(mat ? p.wu : p.wg), 0, D * F * 2, 0x00020000);
        asm volatile("s_mov_b32 m0, %0\n\ts_nop 0\n\tbuffer_load_dword %1, %2, %3 offen lds" :: "s"(smb + L_NU0), "v"((lane & 31) * (F * 2)), "s"(rw), "s"((128 * ma + 96) * (F * 2) + fj * 256 + (il & 1) * 128));
      }
      if (!aux) LOAD_EPOCH();
      MAKE_TAG();
#define WDELAY 8
      __builtin_amdgcn_s_sleep(WDELAY);
      wdq(0, -1);
#pragma unroll
      for (int c = 0; c < 7; ++c) {
        WAIT_L(); SCHED();
        if (!aux || c == 2) __builtin_amdgcn_s_barrier();
        FENCE(); SCHED();
        if (c < 3 && csm) {
#pragma unroll
          for (int i = 0; i < 8; ++i) if (c < 2) ct[8 * c + i] = *(LAS u32x4*)(cp + 3 * UNIT * c + 4096 * i);
#pragma unroll
          for (int i = 6 * c; i < 6 * c + 6 && i < 16; ++i) cadd(i);
          if (c == 2) {
            const float lo = add_x8(fold16(fold32(s[0], s[4]), fold32(s[2], s[6]))), hi = add_x8(fold16(fold32(s[1], s[5]), fold32(s[3], s[7])));
            s[0] = lane & 8 ? hi : lo;
          }
          asm volatile("" : "+v"(s[0]), "+v"(s[1]), "+v"(s[2]), "+v"(s[3]), "+v"(s[4]), "+v"(s[5]), "+v"(s[6]), "+v"(s[7]));
        } else if (c == 0) __builtin_amdgcn_s_sleep(KDELAY);
        if (c == 3 && wrk) {
        float kv[16];
        LAS char* pa = sm + (k ? L_P + 2 * PS : 0), *pb = sm + L_P + (2 * k + 1) * PS;
#pragma unroll
        for (int g = 0; g < 3; ++g) {
          const f32x4 a = *(LAS f32x4*)(pa + 1024 * g + 16 * lane), d = *(LAS f32x4*)(pb + 1024 * g + 16 * lane);
#pragma unroll
          for (int e = 0; e < 4; ++e) asm("v_add_f32 %0, %1, %2" : "=v"(kv[4 * g + e]) : "v"(a[e]), "v"(d[e]));
          asm("v_add_f32 %0, %1, %2" : "=v"(kv[12 + g]) : "v"(*(LAS float*)(pa + 3072 + 256 * g + 4 * lane)), "v"(*(LAS float*)(pb + 3072 + 256 * g + 4 * lane)));
        }
        const bool odd = lane & 1;
        if (lane == 0) kv[0] += bflo(dsk);
        kc[0][0] = (u32x4){pk(kv[0], kv[1]), pk(kv[2], kv[3]), pk(kv[4], kv[5]), pk(kv[6], kv[7])};
        kc[1][0] = (u32x4){pk(kv[8], kv[8]), pk(kv[8], kv[9]), pk(kv[9], kv[9]), pk(kv[10], kv[10])};
        kc[2][0] = (u32x4){pk(kv[11], kv[11]), pk(kv[11], kv[11]), pk(odd ? kv[12] : kv[11], kv[12]), pk(kv[12], kv[12])};
        kc[3][0] = (u32x4){pk(kv[13], kv[13]), pk(kv[13], kv[13]), pk(odd ? kv[14] : kv[13], kv[14]), pk(kv[14], kv[14])};
          asm volatile("" : "+v"(kc[0][0]), "+v"(kc[1][0]), "+v"(kc[2][0]), "+v"(kc[3][0]));
        }
        if (c == 4 && wrk) {
#pragma unroll
        for (int g = 0; g < 4; ++g) {
          const u32x4 cu = kc[g][0];
          int px = 0, py = 0;
          if (g) {
            px = __builtin_amdgcn_update_dpp(0, (int)kc[g ? g - 1 : 0][0].z, 0x13C, 0xf, 0xf, false);
            py = __builtin_amdgcn_update_dpp(0, (int)kc[g ? g - 1 : 0][0].w, 0x13C, 0xf, 0xf, false);
          }
          px = __builtin_amdgcn_update_dpp(px, (int)cu.z, 0x138, 0xf, 0xf, false);
          py = __builtin_amdgcn_update_dpp(py, (int)cu.w, 0x138, 0xf, 0xf, false);
          const u32 s0 = __builtin_amdgcn_alignbit(py, px, 16), s1 = __builtin_amdgcn_alignbit(cu.x, py, 16), s2 = __builtin_amdgcn_alignbit(cu.y, cu.x, 16),
                    s3 = __builtin_amdgcn_alignbit(cu.z, cu.y, 16), s4 = __builtin_amdgcn_alignbit(cu.w, cu.z, 16);
          kc[g][1] = (u32x4){s1, s2, s3, s4}; kc[g][2] = (u32x4){(u32)py, vmov(cu.x), vmov(cu.y), vmov(cu.z)}; kc[g][3] = (u32x4){s0, vmov(s1), vmov(s2), vmov(s3)};
          asm volatile("" : "+v"(kc[g][0]), "+v"(kc[g][1]), "+v"(kc[g][2]), "+v"(kc[g][3]));
        }
          STAMP_AT(2);
        }
        WAIT_V(0);
        take(c, -1);
        if (c == 0 && wrk) { dsk = p.dskip[ch0 + k]; if (k && lane == 0) st8_sc1(rws, tag, FL_A + b * 8, 0); }
        if (c == 2 && csm) {
          const float th = rbf(s[0]);
          st8_sc1(rws, (u32x2){pk(th, s[0] - th), tg32}, 8 * (8 * (lane & 7) + 4 * hh + 2 * blk + (r8 & 1)), WS_CS + (((fj * 2 + mat) * 4 + ma) * 128 + 64 * (il & 1)) * 8);
        }
        if (c == 4 && wrk) f1 = ld8_sc1(rws, vf1, 0);
        if (c == 5 && wrk) { if (!__all(f1.x == tlo && f1.y == thi)) wait_flags<8>(rws, vf1, tlo, thi); FENCE(); if (!aux) stats(); }
        if (c == 6 && wrk) dma4s(rws, smb + L_U + US * k + 512, lane * 16, lane * 16, lane * 16, lane * 16, WS_U + (ch0 + k) * 4096);
        WAIT_L(); SCHED();
        if (c < 6) wdq(c + 1, -1);
        if (c == 6 && wrk && !aux) {
#pragma unroll 1
        for (u32 s = 0; s < SPIN_CAP; ++s) {
          u32 d = 0;
#pragma unroll
          for (int i = 0; i < 5; ++i) {
            const u32x4 t = *(LAS u32x4*)(sm + (i < 4 || !k ? L_CS + 4096 * k + 1024 * (i & 3) : L_ST) + 16 * lane);
            d |= (t.y ^ tg32) | (t.w ^ tg32);
          }
          if (!__any(d != 0)) break;
          stats();
          WAIT_V(0);
        }
        }
      }
    }
    STAMP_AT(4);
    MAKE_TAG();
    wdq(7, 1);
#define NPF 1
    if (NPF) {
      const u32 r = lane < 24 ? lane : 0, j = 4 + (r >> 3) + rot2, ks = 4 * (j >= 7 ? j - 7 : j);
      asm volatile("s_mov_b32 m0, %0\n\ts_nop 0\n\tbuffer_load_dword %1, %2, %3 offen lds" :: "s"(smb + L_NUL), "v"((16 * ks + (r & 7)) * (D * 2)), "s"(rwd), "s"((28 * v + 2) * (16 * D * 2) + (ti2 & 3) * 8192 + n2 * 128));
    }
    if (v < 2) { if (NPF) WAIT_V(5); else WAIT_V(4); }
    STAMP_AT(6);
    BARRIER();
    wdq(8, 2); wdq(9, 3); wdq(10, 4);
    LAS char* kd = sm + L_K + 4 * KCP * k + 16 * lane;
    LAS char* kz = sm + (lane < 32 ? L_U + US * k + 16 * lane : L_K + (4 * k + ((lane >> 1) & 3)) * KCP + 16 * (lane & 1));
    asm volatile("" : "+v"(kd), "+v"(kz));
    u32x4 zero = (u32x4)(0u);
    u32 two = 2;
    LAS char* hpw = sm + S_HP + 4 * v;
    asm volatile("" : "+v"(zero), "+v"(two), "+v"(hpw));
    STAMP_AT(5);
    SCHED();
    if (!aux) __builtin_amdgcn_s_barrier();
    FENCE();
    STAMP_AT(7);
    if (v >= 2) {
      WAIT_V(0);
      *(LAS u32x4*)kz = zero;
#pragma unroll
      for (int e = 0; e < 16; ++e) *(LAS u32x4*)(kd + 32 + KCP * (e & 3) + 1024 * (e >> 2)) = kc[e >> 2][e & 3];
      post(hpw, two);
      WAIT_L();
    } else __builtin_amdgcn_s_sleep(8);
    STAMP_AT(8);
#define LATEQ 1
#define WPRIO 2
    if (!LATEQ) { wdq(11, 5); wdq(12, 6); wdq(13, 7); }
    __builtin_amdgcn_s_setprio(WPRIO);
#define HDELAY 0
#define AUXSLEEP 32
    if (!aux) { await<4>(sy + S_GP, peek(sy + S_GP), 2); __builtin_amdgcn_s_sleep(HDELAY); }
    else __builtin_amdgcn_s_sleep(AUXSLEEP);
    if (LATEQ) { __builtin_amdgcn_s_setprio(0); wdq(11, 5); wdq(12, 6); wdq(13, 7); }
    STAMP_AT(9);
    const u32 vf2 = FL_2 + (ti2 * 28 + 7 * v + (lane < 7 ? lane : 0)) * FS;
#define APOLL 1
    if (APOLL) {
#pragma unroll 1
      for (u32 s = 0; s < SPIN_CAP; ++s) {
        u32x2 f = tag;
        if (lane < 7) { f = (u32x2)(0u); asm volatile("buffer_atomic_or_x2 %0, %1, %2, 0 offen sc0 sc1\n\ts_waitcnt vmcnt(0)" : "+v"(f) : "v"(vf2), "s"(rws) : "memory"); }
        if (__all(f.x == tlo && f.y == thi)) break;
        __builtin_amdgcn_s_sleep(2);
      }
      FENCE();
    } else wait_flags<8>(rws, vf2, tlo, thi);
    __builtin_amdgcn_s_setprio(0);
    STAMP_AT(10);
    f32x16 acc[2][2];
#pragma unroll
    for (int a = 0; a < 4; ++a) acc[a >> 1][a & 1] = (f32x16)(0.f);
#define PD 4
#define PX 1
    u32x4 hf[28][2];
    auto hload = [&](int i) {
      const u32 so = (ti2 * 112 + ks_of(i >> 1)) * 2048;
#define HAUX 16
      hf[i][0] = __builtin_amdgcn_raw_buffer_load_b128(rws, lane * 16 + 2048 * (i & 1), so, HAUX);
      hf[i][1] = __builtin_amdgcn_raw_buffer_load_b128(rws, lane * 16 + 2048 * (i & 1) + 1024, so, HAUX);
    };
#pragma unroll
    for (int i = 0; i < PD; ++i) hload(i);
    bf16x8 wl[2][2];
    u32x2 fb;
    SCHED();
#pragma unroll
    for (int i = 0; i < 28; ++i) {
      if (i + 1 >= 14 && i + 1 < 28) { wl[(i + 1) & 1][0] = wfrag((i - 11) >> 1, (i + 1) & 1, 0); wl[(i + 1) & 1][1] = wfrag((i - 11) >> 1, (i + 1) & 1, 1); }
      const bf16x8 a0 = i < 14 ? wr_[i < 14 ? i : 0][0] : wl[i & 1][0], a1 = i < 14 ? wr_[i < 14 ? i : 0][1] : wl[i & 1][1];
      const bf16x8 b0 = __builtin_bit_cast(bf16x8, hf[i][0]), b1 = __builtin_bit_cast(bf16x8, hf[i][1]);
      acc[0][0] = MFMA32(a0, b0, acc[0][0]);
      acc[1][0] = MFMA32(a1, b0, acc[1][0]);
      acc[0][1] = MFMA32(a0, b1, acc[0][1]);
      acc[1][1] = MFMA32(a1, b1, acc[1][1]);
      if (i == 6) STAMP_AT(15);
      if (i == 13) STAMP_AT(16);
      if (i == 20) STAMP_AT(17);
      SCHED();
#define ZAT 18
      if (i == ZAT) post(sm + S_HP + 4 * v, 3);
      if (i == 10 - PD - PX) fb = ld8_sc1(rws, vf2, FL_2B);
      if (i == 14 - PD - PX) {
        if (__builtin_expect(!__all(fb.x == tlo && fb.y == thi), 0)) wait_flags<4>(rws, vf2 + FL_2B, tlo, thi);
        FENCE();
      }
      if (i < PX) { hload(PD + 2 * i); hload(PD + 2 * i + 1); }
      else if (i + PD + PX < 28) hload(i + PD + PX);
      SCHED();
    }
    STAMP_AT(11);
#pragma unroll
    for (int tt = 0; tt < 2; ++tt) {
#pragma unroll
      for (int a = 0; a < 8; ++a) {
        const int ct = a >> 2, g = a & 3;
        const f32x16& s = acc[ct][tt];
        *(LAS f32x4*)(sm + RB + UNIT * (1 + 2 * ct + tt) + 4096 * v + (lane & 31) * 128 + (((2 * g + hh) ^ (lane & 7) ^ ct) << 4)) = (f32x4){s[4 * g], s[4 * g + 1], s[4 * g + 2], s[4 * g + 3]};
      }
      if (tt) STAMP_AT(12);
      BARRIER();
    }
  } else {
    __builtin_amdgcn_s_setprio(1);
    const int c = v >> 1, ah = v & 1, ch = ch0 + c;
    const int gi = ch * NM + 64 * ah + lane;
    const u32 r6 = p.logdt[ch], r0 = p.are[gi], r1 = p.aim[gi], r2 = p.bre[gi], r3 = p.bim[gi], r4 = p.cre[gi], r5 = p.cim[gi];
    SCHED();
    const int tg = c, fq = ah;
    u32 xa[4], wa[4], iw[4], ir[2];
    {
      const u32 row = 64 * tg + (lane & 31), f = (row >> 1) & 7, scr = RB + 5 * UNIT + 4096 * v, cki = 2 * blk + (p4 >> 1);
#pragma unroll
      for (int s = 0; s < 4; ++s) {
        xa[s] = RB + row * 128 + (((2 * s + hh) ^ f) << 4);
        wa[s] = RB + hh * 4096 + blk * 2048 + q4 * 256 + (((4 * fq + s) ^ (q4 + 4 * blk)) << 5) + p4 * 8;
        iw[s] = scr + lane * 64 + ((s ^ ((lane >> 1) & 3)) << 4);
        asm volatile("" : "+v"(xa[s]), "+v"(wa[s]), "+v"(iw[s]));
      }
      ir[0] = scr + (8 * hh + q4) * 64 + ((cki ^ (q4 >> 1)) << 4) + 8 * (p4 & 1);
      ir[1] = scr + (8 * hh + 4 + q4) * 64 + ((cki ^ (2 + (q4 >> 1))) << 4) + 8 * (p4 & 1);
      asm volatile("" : "+v"(ir[0]), "+v"(ir[1]));
    }
    SCHED();
    STAMP_AT(15);
    STAMP_AT(16);
    bf16x8 yf[8], xf[8];
    {
      const float dt = rbf(__builtin_amdgcn_exp2f(bflo(r6) * 1.44269504088896f));
      const float ar2 = dt * bflo(r0) * 1.44269504088896f, air = dt * bflo(r1) * 0.159154943091895f;
      auto put = [&](int img, int g, u32x4 re, u32x4 im) {
        *(LAS u32x4*)(sm + iw[g] + 2 * img * UNIT) = re; *(LAS u32x4*)(sm + iw[g] + (1 - img) * UNIT) = im;
        SCHED();
      };
      auto get = [&](int img, int s) { const u32 d = (s < 4 ? 2 * img : 1 - img) * UNIT + 1024 * (s & 3); return trfrag(sm + ir[0] + d, sm + ir[1] + d); };
#define P2_(a) pk((a).x, (a).y)
#define P4_(a, b, c, d) (u32x4){P2_(a), P2_(b), P2_(c), P2_(d)}
      const cpx e8 = cexpl(ar2, air, 8.f);
      {
        const cpx s1 = {e8.r, -e8.i}, s2 = csq(s1), s4 = csq(s2), s8 = cexpl(ar2, -air, 64.f), s16 = csq(s8);
        const cp2 y0 = {{1.f, s1.r}, {0.f, s1.i}}, y1 = cmul2(y0, s2), y2 = cmul2(y0, s4), y3 = cmul2(y1, s4);
        put(0, 0, P4_(y0.r, y1.r, y2.r, y3.r), P4_(y0.i, y1.i, y2.i, y3.i));
        const cp2 y4 = cmul2(y0, s8), y5 = cmul2(y1, s8), y6 = cmul2(y2, s8), y7 = cmul2(y3, s8);
        put(0, 1, P4_(y4.r, y5.r, y6.r, y7.r), P4_(y4.i, y5.i, y6.i, y7.i));
        const cp2 y8 = cmul2(y0, s16), y9 = cmul2(y1, s16), ya = cmul2(y2, s16), yb = cmul2(y3, s16);
        put(0, 2, P4_(y8.r, y9.r, ya.r, yb.r), P4_(y8.i, y9.i, ya.i, yb.i));
        const cp2 yc = cmul2(y4, s16), yd = cmul2(y5, s16), ye = cmul2(y6, s16), yg = cmul2(y7, s16);
        put(0, 3, P4_(yc.r, yd.r, ye.r, yg.r), P4_(yc.i, yd.i, ye.i, yg.i));
      }
      {
        const cpx cf = cmul({bflo(r4), bflo(r5)}, {dt * bflo(r2), dt * bflo(r3)});
        const cpx e1 = cexpl(ar2, air, 1.f), e256 = cexpl(ar2, air, 256.f);
        const cpx e2 = csq(e1), e4 = csq(e2), e512 = csq(e256), e1024 = csq(e512);
        const cp2 cb = cmul2({{e1.r, e256.r}, {e1.i, e256.i}}, cf);
        const cp2 a0 = {{cf.r, cb.r.x}, {cf.i, cb.i.x}}, a1 = cmul2(a0, e2);
        const cp2 t0 = cmul2({{e2.r, e4.r}, {e2.i, e4.i}}, {cb.r.y, cb.i.y});
#define XA(m) (u32x4){P2_(a0.m), P2_(a1.m), pk(cb.m.y, cb.m.y), P2_(t0.m)}
#pragma unroll
        for (int s = 0; s < 4; ++s) yf[s] = get(0, s);
        put(1, 0, XA(r), XA(i));
        const cp2 a2 = cmul2(a0, e4), a3 = cmul2(a1, e4), t1 = cmul2(t0, e4);
#define XB(m) (u32x4){P2_(a2.m), P2_(a3.m), pk(t0.m.y, t0.m.y), P2_(t1.m)}
        put(1, 1, XB(r), XB(i));
#pragma unroll
        for (int s = 4; s < 8; ++s) yf[s] = get(0, s);
        const cp2 q0 = {{cf.r, cb.r.y}, {cf.i, cb.i.y}}, q1 = cmul2(q0, e512), q2 = cmul2(q0, e1024);
        const cp2 u4 = cmul2(q1, e4), u8 = cmul2(q1, e8);
#define XC(m) (u32x4){pk(q1.m.x, u4.m.x), pk(u8.m.x, q2.m.x), pk(q1.m.y, u4.m.y), pk(u8.m.y, q2.m.y)}
        put(1, 2, XC(r), XC(i));
        const cp2 q3 = cmul2(q1, e1024), v8 = cmul2(q2, e8), w8 = cmul2(q3, e8);
#define XD(m) (u32x4){pk(v8.m.x, q3.m.x), pk(w8.m.x, 0.f), pk(v8.m.y, q3.m.y), pk(w8.m.y, 0.f)}
        put(1, 3, XD(r), XD(i));
#pragma unroll
        for (int s = 0; s < 8; ++s) xf[s] = get(1, s);
      }

      STAMP_AT(17);
    }
    bf16x8 X0[2], W0[4];
    if (!aux) { LOAD_EPOCH(); __builtin_amdgcn_s_barrier(); FENCE(); }
    MAKE_TAG();
    X0[0] = ldfrag(sm + xa[0]); X0[1] = ldfrag(sm + xa[0] + 4096);
#pragma unroll
    for (int t = 0; t < 4; ++t) W0[t] = trfrag(sm + UNIT + wa[t], sm + UNIT + wa[t] + 1024);
    SCHED();
    f32x16 kacc = (f32x16)(0.f);
#pragma unroll
    for (int s = 0; s < 8; ++s) kacc = MFMA32(xf[s], yf[s], kacc);
    auto kstore = [&](int g) {
      LAS char* d = sm + (v ? L_P + PS * v : 0);
      if (g < 3) *(LAS f32x4*)(d + 1024 * g + 16 * lane) = (f32x4){kacc[4 * g], kacc[4 * g + 1], kacc[4 * g + 2], kacc[4 * g + 3]};
      else { *(LAS float*)(d + 3072 + 4 * lane) = kacc[12]; *(LAS float*)(d + 3328 + 4 * lane) = kacc[13]; *(LAS float*)(d + 3584 + 4 * lane) = kacc[14]; }
    };
    SCHED();
    LAS char* ka = sm + L_K + (4 * c + q4) * KCP + 2 * (28 + 128 * ah + 4 * p4 - 8 * g4);
    LAS char* ua = sm + L_U + US * c + 2 * (240 - 96 - 128 * ah + 16 * (lane & 15) + 8 * g4);
    bf16x4 A[2][16]; bf16x8 B[2][8];
    auto cld1 = [&](int a, int n) {
      const int i = n / 3, r = n % 3;
      if (r < 2) A[a & 1][2 * i + r] = __builtin_amdgcn_ds_read_tr16_b64_v4bf16((LAS bf16x4*)(ka + 64 * a + 512 * i + 8 - 8 * r));
      else B[a & 1][i] = ldfrag(ua + 64 * (3 - a) + 512 * i);
    };
    auto cld = [&](int a, int i) { cld1(a, 3 * i); cld1(a, 3 * i + 1); cld1(a, 3 * i + 2); };
    LAS char* const hpw = sm + S_HP + 8 + 4 * (lane & 1);
    if (aux) { kstore(0); kstore(1); kstore(2); kstore(3); BARRIER(); BARRIER();
      await(hpw, 0, 2);
#pragma unroll
      for (int i = 0; i < 8; ++i) cld(0, i);
    }
    else {
      f32x16 acc[2][4];
#pragma unroll
      for (int a = 0; a < 8; ++a) acc[a >> 2][a & 3] = (f32x16)(0.f);
      bf16x8 X1[2], W1[4];
      auto step = [&](const bf16x8 (&Xc)[2], const bf16x8 (&Wc)[4], bf16x8 (&Xn)[2], bf16x8 (&Wn)[4], u32 xb, u32 wb, int s, auto mid) {
        LAS char* xp = sm + (xb + xa[s]);
        auto wld = [&](int t) { LAS char* a = sm + (wb + wa[t]) + 8192 * (s & 1); Wn[t] = trfrag(a, a + 1024); };
#pragma unroll
        for (int n = 0; n < 8; ++n) {
          acc[n & 1][n >> 1] = MFMA32(Wc[n >> 1], Xc[n & 1], acc[n & 1][n >> 1]); SCHED();
          mid(n);
          if (n == 0) wld(0);
          if (n == 1) { Xn[0] = ldfrag(xp); Xn[1] = ldfrag(xp + 4096); }
          if (n >= 2 && n < 5) wld(n - 1);
          SCHED();
        }
      };
      u32 xb = 0;
      step(X0, W0, X1, W1, xb, UNIT, 1, [&](int n) { if (n >= 2 && n < 6) kstore(n - 2); });
#pragma unroll 1
      for (int cc = 0; cc < 7; ++cc) {
        const u32 w1 = (xb + 2 * UNIT) & (RING - 1);
        step(X1, W1, X0, W0, xb, w1, 2, [&](int) {});
        step(X0, W0, X1, W1, xb, w1, 3, [&](int) {});
        xb = (xb + 3 * UNIT) & (RING - 1);
        const u32 w0 = (xb + UNIT) & (RING - 1);
        step(X1, W1, X0, W0, xb, w0, 0, [&](int n) { if (n == 0) BARRIER(); });
        step(X0, W0, X1, W1, xb, w0, 1, [&](int) {});
      }
      u32 st[2]; u32x4 cs[4]; bf16x8 XM[2];
      float ka[2], kb[2], kr[2];
      step(X1, W1, X0, W0, 5 * UNIT, 7 * UNIT, 2, [&](int n) {
        if (n == 5) { LAS char* a = sm + L_ST + 8 * (64 * tg + (lane & 31)); st[0] = *(LAS u32*)a; st[1] = *(LAS u32*)(a + 256); }
        if (n >= 6) {
          LAS char* a = sm + L_CS + 4096 * blk + 2048 * hh + 8 * (64 * fq + (lane & 15)) + 256 * (n - 6);
          cs[2 * n - 12] = (u32x4){*(LAS u32*)a, *(LAS u32*)(a + 1024), 0, 0}; cs[2 * n - 11] = (u32x4){*(LAS u32*)(a + 128), *(LAS u32*)(a + 1152), 0, 0};
        }
      });
      step(X0, W0, X1, W1, 5 * UNIT, 7 * UNIT, 3, [&](int n) {
        const int t = n & 1;
        if (n == 2 || n == 3) { const u32 m = ((st[t] & 0xffffu) ^ 0x8000u) * 0x10001u; XM[t] = __builtin_bit_cast(bf16x8, (u32x4){m, m, 0, 0}); asm volatile("" : "+v"(XM[t])); }
        if (n == 4) asm volatile("" : "+v"(cs[0]), "+v"(cs[1]));
        if (n == 7) asm volatile("" : "+v"(cs[2]), "+v"(cs[3]));
        if (n == 5 || n == 6) {
          const float sd = bfhi(st[t]), r = __builtin_amdgcn_rcpf(sd);
          kb[t] = -2.f * 0.7978845608028654f * 1.44269504088896f * r; ka[t] = kb[t] * (0.044715f * r * r); kr[t] = sd * sd;
          asm volatile("" : "+v"(ka[t]), "+v"(kb[t]), "+v"(kr[t]));
        }
      });
      STAMP_NW(19);
      auto last = [&](int a) { acc[a & 1][a >> 1] = MFMA32(W1[a >> 1], X1[a & 1], acc[a & 1][a >> 1]); };
      auto corr = [&](int a) { acc[a & 1][a >> 1] = MFMA32(__builtin_bit_cast(bf16x8, cs[a >> 1]), XM[a & 1], acc[a & 1][a >> 1]); };
      last(0); SCHED(); corr(0); SCHED();
      BARRIER();
      u32 hseen;
      const u32 vh = ((2 * mi + tg) * 112 + 8 * fj + 4 * fq) * 2048 + lane * 16;
      SCHED();
#pragma unroll
      for (int a = 0; a < 8; ++a) {
        const int tt = a & 1, ft = a >> 1;
        f32x2 z[4], gu[4];
#pragma unroll
        for (int e = 0; e < 4; ++e) {
          const f32x2 g = {acc[tt][ft][2 * e], acc[tt][ft][2 * e + 1]}, u = {acc[tt][ft][8 + 2 * e], acc[tt][ft][9 + 2 * e]};
          z[e] = (g * g * ka[tt] + kb[tt]) * g; gu[e] = g * u;
        }
        SCHED();
        if (a < 7) last(a + 1);
        if (a >= 6) { cld(0, 4 * a - 24); cld(0, 4 * a - 23); }
        SCHED();
#pragma unroll
        for (int e = 0; e < 4; ++e) z[e] = (f32x2){__builtin_amdgcn_exp2f(z[e].x), __builtin_amdgcn_exp2f(z[e].y)};
        SCHED();
#pragma unroll
        for (int e = 0; e < 4; ++e) z[e] = z[e] * kr[tt] + kr[tt];
        SCHED();
        if (a < 7) corr(a + 1);
        if (a >= 6) { cld(0, 4 * a - 22); cld(0, 4 * a - 21); }
        SCHED();
#pragma unroll
        for (int e = 0; e < 4; ++e) z[e] = (f32x2){__builtin_amdgcn_rcpf(z[e].x), __builtin_amdgcn_rcpf(z[e].y)};
        SCHED();
        u32x4 hv;
#pragma unroll
        for (int e = 0; e < 4; ++e) { const f32x2 hq = gu[e] * z[e]; hv[e] = pk(hq.x, hq.y); }
        st16_sc1(rws, hv, vh + 1024 * a);
#define FLAGA 6
        if (a == 4) hseen = peek(hpw);
        if (a == 5) await(hpw, hseen, 2);
        if (a == 1) STAMP_NW(20);
        if (a == 3) STAMP_NW(21);
        if (a == FLAGA) {
          STAMP_NW(22);
          asm volatile("s_waitcnt vmcnt(%0)" :: "i"(FLAGA - 3) : "memory");
          if (lane < 16) st8_sc1(rws, tag, lane * 8, FL_2 + ((2 * mi + tg) * 28 + 2 * fj + fq) * FS);
          post(sm + S_GP + 4 * v, 2);
          STAMP_NW(18);
        }
        SCHED();
      }
      STAMP_NW(23); STAMP_NWEND();
      STAMP_AT(3);
    }
    f32x4 y[8];
#pragma unroll
    for (int i = 0; i < 8; ++i) y[i] = (f32x4)(0.f);
    const u32 vo = (64 * ti2 + 8 * v + (lane >> 3)) * (D * 2) + n2 * 128 + (lane & 7) * 16;
    u32x4 xres[2];
    LAS char* const pw = sm + S_GP + 4 * (v ^ 1);
    u32 pseen;
    STAMP_AT(4);
#pragma unroll
    for (int a = 0; a < 4; ++a) {
      int n = 0;
      auto cm = [&](int i, int j) {
        y[i + j] = MFMA16(__builtin_shufflevector(A[a & 1][2 * i], A[a & 1][2 * i + 1], 0, 1, 2, 3, 4, 5, 6, 7), B[a & 1][j], y[i + j]);
        SCHED();
        if (a < 3 && n >= 4 && n < 28) cld1(a + 1, n - 4);
        if (a == 3 && n == 16) pseen = peek(pw);
        SCHED();
        ++n;
      };
#pragma unroll
      for (int s = 0; s < 8; ++s)
#pragma unroll
        for (int pass = 0; pass < 2; ++pass)
#pragma unroll
          for (int i = 0; i <= s; ++i) {
            if (pass == 0 && i + s < 8) cm(i, s);
            if (pass == 1 && i < s && i + s < 8) cm(s, i);
          }
      if (a == 2) post(sm + S_GP + 4 * v, 3);
#define FLAG_AT 1
      if (a == FLAG_AT) {
        STAMP_AT(5);
        WAIT_V(0);
        if (!aux && lane < 16) st8_sc1(rws, tag, lane * 8, FL_2 + FL_2B + ((2 * mi + c) * 28 + 2 * fj + ah) * FS);
        xres[0] = __builtin_amdgcn_raw_buffer_load_b128(rx, vo, 0, 0);
        xres[1] = __builtin_amdgcn_raw_buffer_load_b128(rx, vo, 32 * D * 2, 0);
        STAMP_AT(6);
      }
    }
    STAMP_AT(7);
    LAS char* ex = sm + L_K + 4 * c * KCP;
    await<0>(pw, pseen, 3);
    if (ah) {
#pragma unroll
      for (int i = 0; i < 4; ++i) { *(LAS f32x4*)(ex + KCP + 1024 * i + 16 * lane) = y[i]; FENCE(); y[i] = y[4 + i]; }
    } else {
#pragma unroll
      for (int i = 0; i < 4; ++i) *(LAS f32x4*)(ex + 1024 * i + 16 * lane) = y[4 + i];
    }
    post(sm + S_GP + 4 * v, 4);
    await<0>(pw, peek(pw), 4);
    STAMP_AT(8);
#pragma unroll
    for (int i = 0; i < 4; ++i) {
      const f32x4 t = *(LAS f32x4*)(ex + KCP * (ah ^ 1) + 1024 * i + 16 * lane) + y[i];
      st16_sc1(rws, (u32x4){pk(t[0], t[1]), tg32, pk(t[2], t[3]), tg32}, WS_ZT + ch * 8192 + 4096 * ah + 64 * (lane & 15) + 16 * g4 + 1024 * i);
    }
    STAMP_AT(9);
#define ZSLEEP 8
    const u32 vz = (64 * n2 + 16 * v + g4) * 8192 + 256 * ti2 + (lane & 15) * 16;
    u32x4 zz[4];
    if (b == 255 && v == 0) {
#pragma unroll 1
      for (int i = 0; i < 4; ++i) wait_flags<8>(rws, FL_A + (64 * i + lane) * 8, tlo, thi);
      if (lane == 0) st8_sc1(rws, tag, FL_E, 0);
    }
#define ZDELAY 12
    __builtin_amdgcn_s_sleep(ZDELAY);
    await(sy + S_HP, peek(sy + S_HP), 3);
    STAMP_AT(10);
#pragma unroll 1
    for (u32 s = 0; s < SPIN_CAP; ++s) {
      if (s) __builtin_amdgcn_s_sleep(ZSLEEP);
      FENCE();
#pragma unroll
      for (int i = 0; i < 4; ++i) zz[i] = ld16_sc1(rws, vz, WS_ZT + 4 * 8192 * i);
      if (__all(zz[0].y == tg32 && zz[0].w == tg32 && zz[1].y == tg32 && zz[1].w == tg32 && zz[2].y == tg32 && zz[2].w == tg32 && zz[3].y == tg32 && zz[3].w == tg32)) break;
    }
    STAMP_AT(11);
    LAS char* stg = ex + 3 * KCP + 2048 * ah;
#pragma unroll
    for (int i = 0; i < 4; ++i) *(LAS u32x2*)(stg + 512 * i + 8 * lane) = (u32x2){zz[i].x, zz[i].z};
    LAS char* tr = stg + q4 * 128 + 32 * g4 + 8 * p4;
    const bf16x8 ra = trfrag(tr, tr + 512), rc = trfrag(tr + 1024, tr + 1536);
    LAS char* rb = ex + 2 * KCP + 2048 * ah + 32 * lane;
    *(LAS bf16x8*)rb = ra;
    *(LAS bf16x8*)(rb + 16) = rc;
    post(sm + S_GP + 4 * v, 5);
    await<0>(sy + S_GP, peek(sy + S_GP), 5);
    const int tok = 8 * v + (lane >> 3), dc = lane & 7;
    f32x4 s0[2], s1[2];
#pragma unroll
    for (int i = 0; i < 2; ++i) {
      const u32x4 z = *(LAS u32x4*)(sm + L_K + (4 * (dc >> 2) + 2) * KCP + 2048 * ((dc >> 1) & 1) + 32 * tok + 16 * (dc & 1) + 1024 * i), x = xres[i];
      s0[i] = (f32x4){bflo(x[0]) + bflo(z[0]), bfhi(x[0]) + bfhi(z[0]), bflo(x[1]) + bflo(z[1]), bfhi(x[1]) + bfhi(z[1])};
      s1[i] = (f32x4){bflo(x[2]) + bflo(z[2]), bfhi(x[2]) + bfhi(z[2]), bflo(x[3]) + bflo(z[3]), bfhi(x[3]) + bfhi(z[3])};
    }
    u32 t0 = RB + UNIT + UNIT * 2 * (dc >> 2) + tok * 128 + (((2 * (dc & 3)) ^ (tok & 7) ^ (dc >> 2)) << 4), t1 = t0 ^ 16u;
    char* op = (char*)p.out + vo;
    asm volatile("" : "+v"(s0[0]), "+v"(s0[1]), "+v"(s1[0]), "+v"(s1[1]), "+v"(t0), "+v"(t1), "+v"(op));
    STAMP_AT(12);
#pragma unroll
    for (int i = 0; i < 2; ++i) {
      SCHED(); BARRIER(); SCHED();
      if (i == 0) STAMP_AT(13);
#pragma unroll
      for (int g = 0; g < 4; ++g) { s0[i] += *(LAS f32x4*)(sm + t0 + UNIT * i + 4096 * g); s1[i] += *(LAS f32x4*)(sm + t1 + UNIT * i + 4096 * g); }
      *(GAS u32x4*)(op + 32 * D * 2 * i) = (u32x4){pk(s0[i][0], s0[i][1]), pk(s0[i][2], s0[i][3]), pk(s1[i][0], s1[i][1]), pk(s1[i][2], s1[i][3])};
    }
  }
  STAMP_AT(14); STAMP_OUT();
}

#define WSOFF 0
extern "C" void kernel_launch(void* const* d_in, const int* in_sizes, int n_in, void* d_out, int out_size, void* d_ws,
                              size_t ws_size, hipStream_t stream) {
  Params p;
  p.x = (const u16*)d_in[0]; p.are = (const u16*)d_in[1]; p.aim = (const u16*)d_in[2];
  p.bre = (const u16*)d_in[3]; p.bim = (const u16*)d_in[4]; p.cre = (const u16*)d_in[5];
  p.cim = (const u16*)d_in[6]; p.logdt = (const u16*)d_in[7]; p.dskip = (const u16*)d_in[8];
  p.wg = (const u16*)d_in[10]; p.wu = (const u16*)d_in[11]; p.wd = (const u16*)d_in[12];
  p.out = (u16*)d_out; p.ws = (char*)d_ws + ((size_t)WSOFF << 20);
  s4ffn_kernel<<<256, 512, 0, stream>>>(p);
}
```

```cpp
#include <hip/hip_runtime.h>
#include <stdint.h>

typedef unsigned short u16;
typedef unsigned u32;
typedef unsigned long long u64;
typedef __bf16 bf16x2 __attribute__((ext_vector_type(2)));
typedef __bf16 bf16x4 __attribute__((ext_vector_type(4)));
typedef __bf16 bf16x8 __attribute__((ext_vector_type(8)));
typedef float f32x2 __attribute__((ext_vector_type(2)));
typedef float f32x4 __attribute__((ext_vector_type(4)));
typedef float f32x16 __attribute__((ext_vector_type(16)));
typedef u32 u32x2 __attribute__((ext_vector_type(2)));
typedef u32 u32x4 __attribute__((ext_vector_type(4)));
#define LAS __attribute__((address_space(3)))
#define GAS __attribute__((address_space(1)))
#define DI __device__ __forceinline__
typedef __amdgpu_buffer_rsrc_t rsrc_t;

constexpr int L = 2048, D = 512, NM = 128, F = 1792;
constexpr u32 WS_H = 0, WS_U = 8u << 20, WS_ZT = 10u << 20, WS_ST = 14u << 20, WS_CS = WS_ST + 65536, WS_FL = 15u << 20, WS_END = 16u << 20;
constexpr u32 FL_E = WS_FL, FL_1 = WS_FL + 4096, FL_A = WS_FL + 8192, FL_2 = WS_FL + 131072, FL_2B = 131072, FS = 128;
constexpr u32 KCP = 4160, UNIT = 16384, RB = 32768, RING = 8 * UNIT;
constexpr u32 L_K = 0, L_U = 8 * KCP, US = 4608;
constexpr u32 S_GP = 4128, S_HP = 4144;
constexpr u32 L_P = 320, PS = 3840;
constexpr u32 L_NU0 = 15872, L_NUL = L_U + 2 * US;
constexpr u32 L_ST = 0;
constexpr u32 L_CS = KCP;
constexpr u32 CODE_BYTES = 30720;
constexpr u32 SPIN_CAP = 1u << 16;

struct Params {
  const u16 *x, *are, *aim, *bre, *bim, *cre, *cim, *logdt, *dskip, *wg, *wu, *wd;
  u16* out;
  char* ws;
};

#define STAMP_DECL
#define STAMP_AT(k)
#define STAMP_RT(v)
#define STAMP_NW(k)
#define STAMP_NWEND()
#define STAMP_OUT()

DI float bflo(u32 v) { return __builtin_bit_cast(float, v << 16); }
DI float bfhi(u32 v) { return __builtin_bit_cast(float, v & 0xffff0000u); }
DI u32 pk(float a, float b) { f32x2 f = {a, b}; return __builtin_bit_cast(u32, __builtin_convertvector(f, bf16x2)); }
DI float rbf(float a) { return bflo(pk(a, a)); }
DI void dma4(rsrc_t ra, rsrc_t rb, u32 lds, u32 v0, u32 v1, u32 v2, u32 v3, u32 soff) {
  asm volatile("s_mov_b32 m0, %0\n\ts_nop 0\n\tbuffer_load_dwordx4 %1, %5, %7 offen lds\n\tbuffer_load_dwordx4 %2, %5, %7 offen offset:1024 lds\n\t"
               "buffer_load_dwordx4 %3, %6, %7 offen offset:2048 lds\n\tbuffer_load_dwordx4 %4, %6, %7 offen offset:3072 lds"
               :: "s"(lds), "v"(v0), "v"(v1), "v"(v2), "v"(v3), "s"(ra), "s"(rb), "s"(soff));
}
DI void dma4s(rsrc_t r, u32 lds, u32 v0, u32 v1, u32 v2, u32 v3, u32 soff) {
  asm volatile("s_mov_b32 m0, %0\n\ts_nop 0\n\tbuffer_load_dwordx4 %1, %5, %6 offen sc1 lds\n\tbuffer_load_dwordx4 %2, %5, %6 offen offset:1024 sc1 lds\n\t"
               "buffer_load_dwordx4 %3, %5, %6 offen offset:2048 sc1 lds\n\tbuffer_load_dwordx4 %4, %5, %6 offen offset:3072 sc1 lds"
               :: "s"(lds), "v"(v0), "v"(v1), "v"(v2), "v"(v3), "s"(r), "s"(soff));
}
DI void dma1s(rsrc_t r, u32 lds, u32 v0, u32 soff) {
  asm volatile("s_mov_b32 m0, %0\n\ts_nop 0\n\tbuffer_load_dwordx4 %1, %2, %3 offen sc1 lds" :: "s"(lds), "v"(v0), "s"(r), "s"(soff));
}
#define WAIT_V(n) asm volatile("s_waitcnt vmcnt(" #n ")" ::: "memory")
#define WAIT_L() asm volatile("s_waitcnt lgkmcnt(0)" ::: "memory")
#define BARRIER() asm volatile("s_waitcnt lgkmcnt(0)\n\ts_barrier" ::: "memory")
#define FENCE() asm volatile("" ::: "memory")
#define SCHED() __builtin_amdgcn_sched_barrier(0)
DI bf16x8 ldfrag(LAS char* a) { return *(LAS bf16x8*)a; }
DI bf16x4 tr4(LAS char* a) { return __builtin_amdgcn_ds_read_tr16_b64_v4bf16((LAS bf16x4*)a); }
DI bf16x8 cat(bf16x4 lo, bf16x4 hi) { return __builtin_shufflevector(lo, hi, 0, 1, 2, 3, 4, 5, 6, 7); }
DI bf16x8 trfrag(LAS char* a0, LAS char* a1) {
  bf16x4 lo = __builtin_amdgcn_ds_read_tr16_b64_v4bf16((LAS bf16x4*)a0);
  bf16x4 hi = __builtin_amdgcn_ds_read_tr16_b64_v4bf16((LAS bf16x4*)a1);
  return __builtin_shufflevector(lo, hi, 0, 1, 2, 3, 4, 5, 6, 7);
}
#define MFMA32(a, b, c) __builtin_amdgcn_mfma_f32_32x32x16_bf16(a, b, c, 0, 0, 0)
#define MFMA16_0(d, a, b) asm volatile("v_mfma_f32_16x16x32_bf16 %0, %1, %2, 0" : "=&v"(d) : "v"(a), "v"(b))
#define MFMA16_I(d, a, b) asm volatile("v_mfma_f32_16x16x32_bf16 %0, %1, %2, %0" : "+v"(d) : "v"(a), "v"(b))
#define MFMA16(a, b, c) __builtin_amdgcn_mfma_f32_16x16x32_bf16(a, b, c, 0, 0, 0)
DI u32x2 ld8_sc1(rsrc_t r, u32 voff, u32 soff) { return __builtin_amdgcn_raw_buffer_load_b64(r, voff, soff, 16); }
DI u32x4 ld16_sc1(rsrc_t r, u32 voff, u32 soff) { return __builtin_amdgcn_raw_buffer_load_b128(r, voff, soff, 16); }
DI void st8_sc1(rsrc_t r, u32x2 v, u32 voff, u32 soff) { __builtin_amdgcn_raw_buffer_store_b64(v, r, voff, soff, 16); }
DI void st16_sc1(rsrc_t r, u32x4 v, u32 voff) { __builtin_amdgcn_raw_buffer_store_b128(v, r, voff, 0, 16); }
template <int SLEEP> DI void wait_flags(rsrc_t r, u32 voff, u32 tlo, u32 thi) {
#pragma unroll 1
  for (u32 s = 0; s < SPIN_CAP; ++s) {
    FENCE();
    u32x2 f = ld8_sc1(r, voff, 0);
    if (__all(f.x == tlo && f.y == thi)) break;
    __builtin_amdgcn_s_sleep(SLEEP);
  }
  WAIT_V(0);
}
DI void post(LAS char* word, u32 v) {
  asm volatile("s_mov_b64 exec, 1\n\tds_write_b32 %0, %1\n\ts_mov_b64 exec, -1" :: "v"(word), "v"(v) : "memory");
}
DI u32 peek(LAS char* word) { return *(volatile LAS u32*)word; }
template <int SLEEP = 1> DI void await(LAS char* word, u32 seen, u32 need) {
  if (__builtin_expect(__any(seen < need), 0)) {
#pragma unroll 1
    for (u32 s = 0; s < SPIN_CAP * 16; ++s) { __builtin_amdgcn_s_sleep(SLEEP); if (!__any(peek(word) < need)) break; }
  }
  FENCE();
}
#define DPPADD(name, ctl) DI float name(float x) { float r; asm("s_nop 1\n\tv_add_f32_dpp %0, %1, %1 " ctl " row_mask:0xf bank_mask:0xf" : "=v"(r) : "v"(x)); return r; }
DPPADD(add_x1, "quad_perm:[1,0,3,2]") DPPADD(add_x2, "quad_perm:[2,3,0,1]") DPPADD(add_m8, "row_half_mirror") DPPADD(add_x8, "row_ror:8") DPPADD(add_r4, "row_ror:4")
DI float fold32(float a, float b) {
  asm("s_nop 1\n\tv_permlane32_swap_b32 %0, %1\n\tv_add_f32 %0, %0, %1" : "+v"(a), "+v"(b)); return a;
}
DI float fold16(float a, float b) {
  asm("s_nop 1\n\tv_permlane16_swap_b32 %0, %1\n\tv_add_f32 %0, %0, %1" : "+v"(a), "+v"(b)); return a;
}
DI u32 vmov(u32 a) { u32 r; asm("v_mov_b32 %0, %1" : "=v"(r) : "v"(a)); return r; }
DI float vadd(float a, float b) { float r; asm("v_add_f32 %0, %1, %2" : "=v"(r) : "v"(a), "v"(b)); return r; }
#define PIN(x) asm volatile("" : "+v"(x))
struct cpx { float r, i; };
struct cp2 { f32x2 r, i; };
DI cpx cmul(cpx a, cpx b) { return {a.r * b.r - a.i * b.i, a.r * b.i + a.i * b.r}; }
DI cpx csq(cpx a) { return {a.r * a.r - a.i * a.i, (a.r + a.r) * a.i}; }
DI cp2 cmul2(cp2 a, cpx b) { return {a.r * b.r - a.i * b.i, a.i * b.r + a.r * b.i}; }
DI cpx cexpl(float ar2, float air, float l) {
  float m = __builtin_amdgcn_exp2f(ar2 * l), f = __builtin_amdgcn_fractf(air * l);
  return {m * __builtin_amdgcn_cosf(f), m * __builtin_amdgcn_sinf(f)};
}

#define EXP 0
__global__ __launch_bounds__(512, 2) void s4ffn_kernel(Params p) {
  __shared__ __attribute__((aligned(16))) char smem[163840];
  LAS char* sm = (LAS char*)smem;
  const u32 smb = (u32)(size_t)sm;
  const int tid = threadIdx.x, lane = tid & 63;
  const int w = __builtin_amdgcn_readfirstlane(tid >> 6), v = w & 3;
  const int b = blockIdx.x, xcd = b & 7, o = b >> 3;
  if ((w >> 1) == 1) {
    u64 pc; u32 d;
    asm volatile("s_getpc_b64 %0" : "=s"(pc));
    const u32 off = (2 * o + (w & 1)) * 512;
    asm volatile(".set pf_off, 0\n\t.rept 8\n\ts_load_dword %0, %1, pf_off\n\t.set pf_off, pf_off + 64\n\t.endr\n\ts_waitcnt lgkmcnt(0)"
                 : "=&s"(d) : "s"((pc & ~255ull) + (off < CODE_BYTES ? off : 0)));
  }
  const bool aux = o >= 28;
  const int ma = xcd >> 1, fh = xcd & 1;
  const int il = o / 7, jl = o - il * 7;
  const int mi = 4 * ma + il, fj = 7 * fh + jl;
  const int ti2 = 4 * xcd + (o >> 3), n2 = o & 7;
  const int ch0 = 64 * xcd + 2 * o;
  const int hh = lane >> 5, blk = (lane >> 4) & 1, q4 = (lane >> 2) & 3, p4 = lane & 3, g4 = lane >> 4;
  STAMP_DECL; STAMP_RT(rt0_); STAMP_AT(0);
#define DESCRIPTORS() const rsrc_t rx = __builtin_amdgcn_make_buffer_rsrc((void*)p.x, 0, L * D * 2, 0x00020000), \
  rws = __builtin_amdgcn_make_buffer_rsrc((void*)p.ws, 0, WS_END, 0x00020000)
  u64 ev;
#define LOAD_EPOCH() asm volatile("s_load_dwordx2 %0, %1, 0\n\ts_waitcnt lgkmcnt(0)" : "=&s"(ev) : "s"(p.ws + FL_E) : "memory")
#define MAKE_TAG() const u32 tlo = (u32)(ev + 0x9E3779B97F4A7C15ull), thi = (u32)((ev + 0x9E3779B97F4A7C15ull) >> 32), tg32 = tlo ^ thi; const u32x2 tag = {tlo, thi}
  LAS char* const sy = sm + 4 * p4;
  if (w == 3 && lane < 2) *(LAS u32x4*)(sm + S_GP + 16 * lane) = (u32x4)(0u);

  if (__builtin_expect(aux, 0)) {
    DESCRIPTORS();
    const int ab = 8 * ma + 4 * fh + o - 28, rb = 8 * ab + w;
    u32x4 xr[8];
#pragma unroll
    for (int i = 0; i < 8; ++i) xr[i] = __builtin_amdgcn_raw_buffer_load_b128(rx, lane * 16 + 1024 * (i & 3), (8 * rb + (i & 4)) * 1024, 0);
    LOAD_EPOCH();
    MAKE_TAG();
          u32 one = 0x3f803f80u;
          asm("" : "+v"(one));
          float su[8], sq[8];
#pragma unroll
          for (int r = 0; r < 8; ++r) {
            su[r] = sq[r] = 0.f;
#pragma unroll
            for (int e = 0; e < 4; ++e) {
              const u32 d = xr[r][e];
              su[r] = __builtin_amdgcn_fdot2_f32_bf16(__builtin_bit_cast(bf16x2, d), __builtin_bit_cast(bf16x2, one), su[r], false);
              sq[r] = __builtin_amdgcn_fdot2_f32_bf16(__builtin_bit_cast(bf16x2, d), __builtin_bit_cast(bf16x2, d), sq[r], false);
            }
          }
          asm volatile("s_nop 2" : "+v"(su[0]), "+v"(su[1]), "+v"(su[2]), "+v"(su[3]), "+v"(su[4]), "+v"(su[5]), "+v"(su[6]), "+v"(su[7]),
                                   "+v"(sq[0]), "+v"(sq[1]), "+v"(sq[2]), "+v"(sq[3]), "+v"(sq[4]), "+v"(sq[5]), "+v"(sq[6]), "+v"(sq[7]));
          auto total = [&](const float (&a)[8]) {
            const float lo = add_x8(fold16(fold32(a[0], a[2]), fold32(a[1], a[3]))), hi = add_x8(fold16(fold32(a[4], a[6]), fold32(a[5], a[7])));
            return add_m8(add_x2(add_x1(lane & 8 ? hi : lo)));
          };
          const float mean = total(su) * (1.f / D), nsq = -mean * mean;
          const float var = rbf(__builtin_fmaxf(__builtin_fmaf(total(sq), 1.f / D, nsq), 0.f));
          const u32 gran = pk(mean, __builtin_amdgcn_sqrtf(rbf(var + 1e-5f)));
          const float r = __builtin_amdgcn_rcpf(bfhi(gran)), mr = -bflo(gran) * r;
          u32 n[8][4];
#pragma unroll
          for (int i = 0; i < 4; ++i) {
            const int l0 = 16 * (2 * i & 3) + 8 * (i >> 1);
            auto at = [&](float t, int l) { return __builtin_bit_cast(float, __builtin_amdgcn_readlane(__builtin_bit_cast(int, t), l)); };
            const f32x2 r2 = {at(r, l0), at(r, l0 + 16)}, m2 = {at(mr, l0), at(mr, l0 + 16)};
#pragma unroll
            for (int e = 0; e < 4; ++e) {
              const u32 a = xr[2 * i][e], c = xr[2 * i + 1][e];
              const f32x2 lo = (f32x2){bflo(a), bflo(c)} * r2 + m2, hi = (f32x2){bfhi(a), bfhi(c)} * r2 + m2;
              n[2 * e][i] = pk(lo.x, lo.y); n[2 * e + 1][i] = pk(hi.x, hi.y);
            }
          }
    if ((lane & 7) == 0) st8_sc1(rws, (u32x2){gran, tg32}, 8 * g4 + 4 * (lane & 8), WS_ST + 64 * rb);
#pragma unroll
    for (int e = 0; e < 8; ++e) *(LAS u32x4*)(sm + RB + (8 * lane + e) * 128 + ((w ^ (lane & 7)) << 4)) = (u32x4){n[e][0], n[e][1], n[e][2], n[e][3]};
    STAMP_AT(18);
    BARRIER();
#pragma unroll
    for (int e = 0; e < 8; ++e) {
      const u32 row = 64 * w + 8 * e + (lane >> 3);
      st16_sc1(rws, *(LAS u32x4*)(sm + RB + row * 128 + (((lane & 7) ^ e) << 4)), WS_U + row * 4096 + ab * 128 + (lane & 7) * 16);
    }
    WAIT_V(0);
    if (lane == 0) st8_sc1(rws, tag, FL_1 + (w * 32 + ab) * 8, 0);
    STAMP_AT(19);
  }

  if (w < 4) {
    const int k = v & 1;
    u32 vx0 = (lane >> 3) * 1024 + (((lane & 7) ^ g4) << 4), vx1 = (vx0 ^ 64) + 7168, vx2 = vx0 + 14336, vx3 = vx1 + 14336;
    u32 vg0 = g4 * (F * 2) + (((((lane & 15) >> 1) ^ g4) << 5) | ((lane & 1) << 4)), vg1 = vg0 + 4 * F * 2 - 1024;
    u32 vu0 = vg0 ^ 128, vu1 = vu0 + 4 * F * 2 - 1024;
    u32 sx0 = (mi * 128 + 64 * v) * 1024, sw0 = 16 * v * (F * 2) + fj * 256;
    asm volatile("" : "+v"(vx0), "+v"(vx1), "+v"(vx2), "+v"(vx3), "+v"(vg0), "+v"(vg1), "+v"(vu0), "+v"(vu1));
    asm volatile("" : "+s"(sx0), "+s"(sw0));
    SCHED();
    DESCRIPTORS();
    const rsrc_t rwd = __builtin_amdgcn_make_buffer_rsrc((void*)p.wd, 0, D * F * 2, 0x00020000);
#define ROT2 0
    const int rot2 = ROT2;
    const u32 dq = smb + RB + 4096 * v;
    const u32 vd0 = (lane >> 3) * (D * 2) + (((lane & 7) ^ (g4 << 1)) << 4), vd1 = vd0 + 7168, vd2 = vd0 + 14336, vd3 = vd0 + 21504;
    auto ks_of = [&](int q) { int j = q % 7 + rot2; j = j >= 7 ? j - 7 : j; return 28 * v + 4 * j + 2 * (q / 7); };
    const u32 sd0 = 28 * v * (16 * D * 2) + n2 * 128;
    auto wdq = [&](int q, int slot) {
      u32 so = sd0;
      asm volatile("" : "+s"(so));
      dma4(rwd, rwd, dq + slot * UNIT, vd0, vd1, vd2, vd3, so + (ks_of(q) - 28 * v) * (16 * D * 2));
    };
    const u32 wo0 = RB + 4096 * v + hh * 1024 + (4 * blk + q4) * 128 + ((2 * blk + (q4 >> 1)) << 5) + p4 * 8;
    auto whalf = [&](int slot, int ct, int t) { return tr4(sm + (wo0 ^ (ct << 5)) + slot * (int)UNIT + 2048 * t); };
    bf16x8 wr_[7][4];
    auto take = [&](int q, int slot) {
#pragma unroll
      for (int e = 0; e < 4; ++e) wr_[q][e] = cat(whalf(slot, e, 0), whalf(slot, e, 1));
    };
    auto last7 = [&] {
    wdq(7, 1);
    {
      const u32 r = lane < 24 ? lane : 0, j = 4 + (r >> 3) + rot2, ks = 4 * (j >= 7 ? j - 7 : j);
      asm volatile("s_mov_b32 m0, %0\n\ts_nop 0\n\tbuffer_load_dword %1, %2, %3 offen lds" :: "s"(smb + L_NUL), "v"((16 * ks + (r & 7)) * (D * 2)), "s"(rwd), "s"((28 * v + 2) * (16 * D * 2) + (ti2 & 3) * 8192 + n2 * 128));
    }
    };
    u32x4 kc[4][4];
    if (v < 2 && !aux) {
        const rsrc_t rwg = __builtin_amdgcn_make_buffer_rsrc((void*)p.wg, 0, D * F * 2, 0x00020000);
        const rsrc_t rwu2 = __builtin_amdgcn_make_buffer_rsrc((void*)((const char*)p.wu - 2048), 0, D * F * 2 + 2048, 0x00020000);
        u32 dst = 8192 * v;
        auto ux = [&](u32 c) {
          const u32 s = sx0 + ((2 * ma + c) & 7) * 128;
          dma4(rx, rx, smb + RB + dst, vx0, vx1, vx2, vx3, s); dma4(rx, rx, smb + RB + dst + 4096, vx0, vx1, vx2, vx3, s + 32768);
          dst = (dst + UNIT) & (RING - 1);
        };
        auto uw = [&](u32 c, u32 h) {
          const u32 s = sw0 + (2 * ((2 * ma + c) & 7) + h) * (32 * F * 2);
          dma4(rwg, rwu2, smb + RB + dst, vg0, vg1, vg0, vg1, s); dma4(rwg, rwu2, smb + RB + dst + 4096, vu0, vu1, vu0, vu1, s + 8 * F * 2);
          dst = (dst + UNIT) & (RING - 1);
        };
#define LDELAY 4
      __builtin_amdgcn_s_sleep(LDELAY);
      ux(0); uw(0, 0);
      LOAD_EPOCH();
      uw(0, 1); ux(1); uw(1, 0);
#pragma unroll
      for (int c = 0; c < 7; ++c) {
        if (c) { uw(c, 1); ux(c + 1); WAIT_V(16); take(c - 1, -1); uw(c + 1, 0); WAIT_L(); SCHED(); }
        wdq(c, -1);
        WAIT_V(20);
        if (c == 0) STAMP_AT(1);
        __builtin_amdgcn_s_barrier();
      }
      uw(7, 1);
      last7();
      WAIT_V(5);
    } else {
      const bool wrk = v >= 2, csm = wrk && !aux;
      const int mat = il >> 1, r8 = (lane >> 2) & 7;
      LAS char* const cp = sm + RB + UNIT + hh * 4096 + mat * 2048 + r8 * 256 + (((4 * (il & 1) + 2 * k + (p4 >> 1)) ^ ((r8 & 3) + 4 * hh)) << 5) + (lane & 1) * 16;
      const u32 vf1 = FL_1 + (32 * xcd + (lane & 31)) * 8;
      auto stats = [&] {
        dma4s(rws, smb + L_CS + 4096 * k, lane * 16, lane * 16, lane * 16, lane * 16, WS_CS + (fj * 2 + k) * 4096);
        if (k) dma1s(rws, smb + L_ST, lane * 16, WS_ST + mi * 1024);
      };
      float s[8]; u32x4 ct[8]; u32x2 f1; u32 dsk; bool f1bad;
      auto cadd = [&](int i) {
#pragma unroll
        for (int e = 0; e < 4; ++e) {
          if (i == 0) { s[2 * e] = bflo(ct[0][e]); s[2 * e + 1] = bfhi(ct[0][e]); }
          else asm("v_add_f32 %0, %0, %2\n\tv_add_f32 %1, %1, %3" : "+v"(s[2 * e]), "+v"(s[2 * e + 1]) : "v"(bflo(ct[i][e])), "v"(bfhi(ct[i][e])));
        }
      };
#define TCH 3
#define TDELAY 4
      dsk = p.dskip[ch0 + k];
      if (TDELAY) __builtin_amdgcn_s_sleep(TDELAY);
      if ((TCH & 1) && v == 2) asm volatile("s_mov_b32 m0, %0\n\ts_nop 0\n\tbuffer_load_dword %1, %2, %3 offen lds" :: "s"(smb + L_NU0), "v"(lane < 56 ? lane * 128 : 0), "s"(rwd), "s"(b * 7168));
      if ((TCH & 2) && v == 3 && !aux) {
        const rsrc_t rw = __builtin_amdgcn_make_buffer_rsrc((void*)(mat ? p.wu : p.wg), 0, D * F * 2, 0x00020000);
        asm volatile("s_mov_b32 m0, %0\n\ts_nop 0\n\tbuffer_load_dword %1, %2, %3 offen lds" :: "s"(smb + L_NU0), "v"((lane & 31) * (F * 2)), "s"(rw), "s"((128 * ma + 96) * (F * 2) + fj * 256 + (il & 1) * 128));
      }
      if (!aux) LOAD_EPOCH();
      MAKE_TAG();
#define WDELAY 8
      __builtin_amdgcn_s_sleep(WDELAY);
      wdq(0, -1);
#pragma unroll
      for (int c = 0; c < 7; ++c) {
        if (c) {
          if ((c == 2 && v == 3) || (c == 3 && csm)) WAIT_V(1); else if (c == 6 && csm) WAIT_V(5); else WAIT_V(0);
          if (c == 1) PIN(dsk);
          take(c - 1, -1);
          WAIT_L(); SCHED();
        }
        if (!aux || c == 2) __builtin_amdgcn_s_barrier();
        FENCE(); SCHED();
#define KPRIO 1
#define STC 5
        __builtin_amdgcn_s_setprio(KPRIO);
        if (c == 6 && wrk) f1bad = !__all(f1.x == tlo && f1.y == thi);
        if (c) wdq(c, -1);
        if (c == 6) last7();
        if (c == 1 && v == 3 && lane == 0) st8_sc1(rws, tag, FL_A + b * 8, 0);
        if (c == STC && csm) stats();
        if (c == 5 && wrk) f1 = ld8_sc1(rws, vf1, 0);
        if (c == 6 && wrk) {
          if (f1bad) wait_flags<8>(rws, vf1, tlo, thi);
          FENCE();
          dma4s(rws, smb + L_U + US * k + 512, lane * 16, lane * 16, lane * 16, lane * 16, WS_U + (ch0 + k) * 4096);
        }
        __builtin_amdgcn_s_setprio(0);
        SCHED();
        if (c < 2 && csm) {
#pragma unroll
          for (int i = 0; i < 4; ++i) ct[4 * c + i] = *(LAS u32x4*)(cp + 3 * UNIT * c + 8192 * i);
#pragma unroll
          for (int i = 0; i < 4; ++i) cadd(4 * c + i);
          asm volatile("" : "+v"(s[0]), "+v"(s[1]), "+v"(s[2]), "+v"(s[3]), "+v"(s[4]), "+v"(s[5]), "+v"(s[6]), "+v"(s[7]));
        }
        if (c == 3 && wrk) {
        float kv[16];
        LAS char* pa = sm + (k ? L_P + 2 * PS : 0), *pb = sm + L_P + (2 * k + 1) * PS;
#pragma unroll
        for (int g = 0; g < 3; ++g) {
          const f32x4 a = *(LAS f32x4*)(pa + 1024 * g + 16 * lane), d = *(LAS f32x4*)(pb + 1024 * g + 16 * lane);
#pragma unroll
          for (int e = 0; e < 4; ++e) asm("v_add_f32 %0, %1, %2" : "=v"(kv[4 * g + e]) : "v"(a[e]), "v"(d[e]));
          asm("v_add_f32 %0, %1, %2" : "=v"(kv[12 + g]) : "v"(*(LAS float*)(pa + 3072 + 256 * g + 4 * lane)), "v"(*(LAS float*)(pb + 3072 + 256 * g + 4 * lane)));
        }
        const bool odd = lane & 1;
        if (lane == 0) kv[0] += bflo(dsk);
        kc[0][0] = (u32x4){pk(kv[0], kv[1]), pk(kv[2], kv[3]), pk(kv[4], kv[5]), pk(kv[6], kv[7])};
        kc[1][0] = (u32x4){pk(kv[8], kv[8]), pk(kv[8], kv[9]), pk(kv[9], kv[9]), pk(kv[10], kv[10])};
        kc[2][0] = (u32x4){pk(kv[11], kv[11]), pk(kv[11], kv[11]), pk(odd ? kv[12] : kv[11], kv[12]), pk(kv[12], kv[12])};
        kc[3][0] = (u32x4){pk(kv[13], kv[13]), pk(kv[13], kv[13]), pk(odd ? kv[14] : kv[13], kv[14]), pk(kv[14], kv[14])};
          asm volatile("" : "+v"(kc[0][0]), "+v"(kc[1][0]), "+v"(kc[2][0]), "+v"(kc[3][0]));
        }
        if (c == 4 && wrk) {
#pragma unroll
        for (int g = 0; g < 4; ++g) {
          const u32x4 cu = kc[g][0];
          int px = 0, py = 0;
          if (g) {
            px = __builtin_amdgcn_update_dpp(0, (int)kc[g ? g - 1 : 0][0].z, 0x13C, 0xf, 0xf, false);
            py = __builtin_amdgcn_update_dpp(0, (int)kc[g ? g - 1 : 0][0].w, 0x13C, 0xf, 0xf, false);
          }
          px = __builtin_amdgcn_update_dpp(px, (int)cu.z, 0x138, 0xf, 0xf, false);
          py = __builtin_amdgcn_update_dpp(py, (int)cu.w, 0x138, 0xf, 0xf, false);
          const u32 s0 = __builtin_amdgcn_alignbit(py, px, 16), s1 = __builtin_amdgcn_alignbit(cu.x, py, 16), s2 = __builtin_amdgcn_alignbit(cu.y, cu.x, 16),
                    s3 = __builtin_amdgcn_alignbit(cu.z, cu.y, 16), s4 = __builtin_amdgcn_alignbit(cu.w, cu.z, 16);
          kc[g][1] = (u32x4){s1, s2, s3, s4}; kc[g][2] = (u32x4){(u32)py, vmov(cu.x), vmov(cu.y), vmov(cu.z)}; kc[g][3] = (u32x4){s0, vmov(s1), vmov(s2), vmov(s3)};
          asm volatile("" : "+v"(kc[g][0]), "+v"(kc[g][1]), "+v"(kc[g][2]), "+v"(kc[g][3]));
        }
          STAMP_AT(2);
        }
        if (c == 2 && csm) {
          const float lo = add_x8(add_r4(fold16(fold32(s[0], s[4]), fold32(s[2], s[6])))), hi = add_x8(add_r4(fold16(fold32(s[1], s[5]), fold32(s[3], s[7]))));
          s[0] = lane & 8 ? hi : lo;
          const float th = rbf(s[0]);
          if (!(lane & 4)) st8_sc1(rws, (u32x2){pk(th, s[0] - th), tg32}, 8 * (8 * p4 + 4 * hh + 2 * blk + ((lane >> 3) & 1)), WS_CS + (((fj * 2 + mat) * 4 + ma) * 128 + 64 * (il & 1) + 32 * k) * 8);
        }
        if (c == STC + 1 && wrk && !aux) {
#pragma unroll 1
        for (u32 s = 0; s < SPIN_CAP; ++s) {
          u32 d = 0;
#pragma unroll
          for (int i = 0; i < 5; ++i) {
            const u32x4 t = *(LAS u32x4*)(sm + (i < 4 || !k ? L_CS + 4096 * k + 1024 * (i & 3) : L_ST) + 16 * lane);
            d |= (t.y ^ tg32) | (t.w ^ tg32);
          }
          if (!__any(d != 0)) break;
          stats();
          WAIT_V(0);
        }
        }
      }
    }
    STAMP_AT(4);
    MAKE_TAG();
    STAMP_AT(6);
    BARRIER();
    wdq(8, 2); wdq(9, 3); wdq(10, 4);
    WAIT_V(17); take(6, -1);
    LAS char* kd = sm + L_K + 4 * KCP * k + 16 * lane;
    LAS char* kz = sm + (lane < 32 ? L_U + US * k + 16 * lane : L_K + (4 * k + ((lane >> 1) & 3)) * KCP + 16 * (lane & 1));
    asm volatile("" : "+v"(kd), "+v"(kz));
    u32x4 zero = (u32x4)(0u);
    u32 two = 2;
    LAS char* hpw = sm + S_HP + 4 * v;
    asm volatile("" : "+v"(zero), "+v"(two), "+v"(hpw));
    STAMP_AT(5);
    SCHED();
    BARRIER();
    STAMP_AT(7);
    if (v >= 2) {
      WAIT_V(0);
      *(LAS u32x4*)kz = zero;
#pragma unroll
      for (int e = 0; e < 16; ++e) *(LAS u32x4*)(kd + 32 + KCP * (e & 3) + 1024 * (e >> 2)) = kc[e >> 2][e & 3];
      post(hpw, two);
      WAIT_L();
    } else __builtin_amdgcn_s_sleep(8);
    STAMP_AT(8);
#define LATEQ 1
#define WPRIO 2
    if (!LATEQ) { wdq(11, 5); wdq(12, 6); wdq(13, 7); }
    __builtin_amdgcn_s_setprio(WPRIO);
#define HDELAY 0
#define AUXSLEEP 32
    if (!aux) { await<4>(sy + S_GP, peek(sy + S_GP), 2); __builtin_amdgcn_s_sleep(HDELAY); }
    else __builtin_amdgcn_s_sleep(AUXSLEEP);
    if (LATEQ == 1) { __builtin_amdgcn_s_setprio(0); wdq(11, 5); wdq(12, 6); wdq(13, 7); }
    STAMP_AT(9);
    const u32 vf2 = FL_2 + (ti2 * 28 + 7 * v + (lane < 7 ? lane : 0)) * FS;
#define APOLL 1
    if (APOLL) {
#pragma unroll 1
      for (u32 s = 0; s < SPIN_CAP; ++s) {
        u32x2 f = tag;
        if (lane < 7) { f = (u32x2)(0u); asm volatile("buffer_atomic_or_x2 %0, %1, %2, 0 offen sc0 sc1\n\ts_waitcnt vmcnt(0)" : "+v"(f) : "v"(vf2), "s"(rws) : "memory"); }
        if ((EXP & 4) || __all(f.x == tlo && f.y == thi)) break;
        __builtin_amdgcn_s_sleep(2);
      }
      FENCE();
    } else wait_flags<8>(rws, vf2, tlo, thi);
    __builtin_amdgcn_s_setprio(0);
    STAMP_AT(10);
    f32x4 acc[4][4];
#define PD 12
#define ZAT 9
    u32x4 hf[56];
    auto hload = [&](int n) { hf[n] = ld16_sc1(rws, lane * 16 + 1024 * (n & 3), (ti2 * 112 + ks_of(n >> 2)) * 2048); };
#pragma unroll
    for (int n = 0; n < PD; ++n) hload(n);
    bf16x4 wl[2][8];
    u32x2 fb; u32 pw_[4];
    SCHED();
#pragma unroll
    for (int i = 0; i < 14; ++i) {
#pragma unroll
      for (int n = 0; n < 16; ++n) {
        const int m = n >> 2, ct = m & 1 ? 3 - (n & 3) : n & 3, nx = 4 * i + m + PD;
        const bf16x8 a = i < 7 ? wr_[i < 7 ? i : 0][ct] : cat(wl[i & 1][2 * ct], wl[i & 1][2 * ct + 1]);
        const bf16x8 bb = __builtin_bit_cast(bf16x8, hf[4 * i + m]);
        if (i == 0) MFMA16_0(acc[ct][m], a, bb); else MFMA16_I(acc[ct][m], a, bb);
        SCHED();
        if ((n & 3) == 3 && nx < 56) hload(nx);
        if ((n & 3) < 2 && i >= 6 && i < 13) wl[(i + 1) & 1][2 * m + (n & 1)] = whalf(i - 5, m, n & 1);
        if ((n & 3) == 2) {
          if (nx == 20) fb = ld8_sc1(rws, vf2, FL_2B);
          if (nx == 28) {
            if (__builtin_expect(!__all(fb.x == tlo && fb.y == thi), 0)) wait_flags<4>(rws, vf2 + FL_2B, tlo, thi);
            FENCE();
          }
          if (i == ZAT && m == 0) post(sm + S_HP + 4 * v, 3);
          if (i == 13) { pw_[m] = (RB + UNIT + 4096 * v + (lane & 15) * 128 + ((g4 ^ (lane & 7)) << 4)) ^ ((m & 1) << 6) ^ ((m >> 1) << 4); PIN(pw_[m]); }
        }
        SCHED();
      }
      if (i == 3) STAMP_AT(15);
      if (i == 6) STAMP_AT(16);
      if (i == 10) STAMP_AT(17);
    }
    STAMP_AT(11);
#pragma unroll
    for (int tt = 0; tt < 2; ++tt) {
      if (tt) asm volatile("s_nop 7" : "+v"(acc[0][3]), "+v"(acc[1][3]), "+v"(acc[2][3]), "+v"(acc[3][3]));
#pragma unroll
      for (int a = 0; a < 8; ++a) {
        const int ct = a & 3, m = 2 * tt + (a >> 2);
        *(LAS f32x4*)(sm + pw_[ct] + UNIT * (2 * (ct >> 1) + tt) + 2048 * (m & 1)) = acc[ct][m];
      }
      if (tt) STAMP_AT(12);
      BARRIER();
    }
  } else {
    __builtin_amdgcn_s_setprio(1);
    const int c = v >> 1, ah = v & 1, ch = ch0 + c;
    u32 gi = (ch * NM + 64 * ah + lane) * 2, gd = ch * 2;
    asm volatile("" : "+v"(gi), "+v"(gd));
    SCHED();
    auto at = [](const u16* q, u32 off) { return (u32)*(const u16*)((const char*)q + off); };
    const u32 r6 = at(p.logdt, gd), r0 = at(p.are, gi), r1 = at(p.aim, gi), r2 = at(p.bre, gi), r3 = at(p.bim, gi), r4 = at(p.cre, gi), r5 = at(p.cim, gi);
    SCHED();
    DESCRIPTORS();
    const int tg = c, fq = ah;
    u32 xa[2], wa[4], iw[4], ir[2];
    {
      const u32 row = 64 * tg + (lane & 15), f = (row >> 1) & 7, scr = RB + 5 * UNIT + 4096 * v, cki = 2 * blk + (p4 >> 1);
#pragma unroll
      for (int s = 0; s < 4; ++s) {
        if (s < 2) xa[s] = RB + row * 128 + (((4 * s + g4) ^ f) << 4);
        wa[s] = RB + g4 * 4096 + q4 * 256 + (((4 * fq + s) ^ (q4 + 4 * blk)) << 5) + p4 * 8;
        iw[s] = scr + lane * 64 + ((s ^ ((lane >> 1) & 3)) << 4);
        asm volatile("" : "+v"(xa[s & 1]), "+v"(wa[s]), "+v"(iw[s]));
      }
      ir[0] = scr + (8 * hh + q4) * 64 + ((cki ^ (q4 >> 1)) << 4) + 8 * (p4 & 1);
      ir[1] = scr + (8 * hh + 4 + q4) * 64 + ((cki ^ (2 + (q4 >> 1))) << 4) + 8 * (p4 & 1);
      asm volatile("" : "+v"(ir[0]), "+v"(ir[1]));
    }
    LAS char* ka = sm + L_K + (4 * c + q4) * KCP + 2 * (28 + 128 * ah + 4 * p4 - 8 * g4);
    LAS char* ua = sm + L_U + US * c + 2 * (240 - 96 - 128 * ah + 16 * (lane & 15) + 8 * g4);
    LAS char *hpw = sm + S_HP + 8 + 4 * (lane & 1), *kp4 = sm + ((v ? L_P + PS * v : 0) + 16 * lane), *kp1 = sm + ((v ? L_P + PS * v : 0) + 3072 + 4 * lane);
    asm volatile("" : "+v"(ka), "+v"(ua), "+v"(hpw), "+v"(kp4), "+v"(kp1));
    u64 ev2;
    asm volatile("s_load_dwordx2 %0, %1, 0" : "=&s"(ev2) : "s"(p.ws + FL_E) : "memory");
    SCHED();
    STAMP_AT(15);
    STAMP_AT(16);
    bf16x8 yf[8], xf[8];
    {
      const float dt = rbf(__builtin_amdgcn_exp2f(bflo(r6) * 1.44269504088896f));
      const float ar2 = dt * bflo(r0) * 1.44269504088896f, air = dt * bflo(r1) * 0.159154943091895f;
      auto put = [&](int img, int g, u32x4 re, u32x4 im) {
        if (EXP & 8) return;
        *(LAS u32x4*)(sm + iw[g] + 2 * img * UNIT) = re; *(LAS u32x4*)(sm + iw[g] + (1 - img) * UNIT) = im;
        SCHED();
      };
      auto get = [&](int img, int s) { const u32 d = (s < 4 ? 2 * img : 1 - img) * UNIT + 1024 * (s & 3); return trfrag(sm + ir[0] + d, sm + ir[1] + d); };
#define P2_(a) pk((a).x, (a).y)
#define P4_(a, b, c, d) (u32x4){P2_(a), P2_(b), P2_(c), P2_(d)}
      const cpx e8 = cexpl(ar2, air, 8.f);
      {
        const cpx s1 = {e8.r, -e8.i}, s2 = csq(s1), s4 = csq(s2), s8 = cexpl(ar2, -air, 64.f), s16 = csq(s8);
        const cp2 y0 = {{1.f, s1.r}, {0.f, s1.i}}, y1 = cmul2(y0, s2), y2 = cmul2(y0, s4), y3 = cmul2(y1, s4);
        SCHED();
        asm volatile("s_waitcnt lgkmcnt(0)" : "+s"(ev2) :: "memory");
        if (!aux) ev = ev2;
        put(0, 0, P4_(y0.r, y1.r, y2.r, y3.r), P4_(y0.i, y1.i, y2.i, y3.i));
        const cp2 y4 = cmul2(y0, s8), y5 = cmul2(y1, s8), y6 = cmul2(y2, s8), y7 = cmul2(y3, s8);
        put(0, 1, P4_(y4.r, y5.r, y6.r, y7.r), P4_(y4.i, y5.i, y6.i, y7.i));
        const cp2 y8 = cmul2(y0, s16), y9 = cmul2(y1, s16), ya = cmul2(y2, s16), yb = cmul2(y3, s16);
        put(0, 2, P4_(y8.r, y9.r, ya.r, yb.r), P4_(y8.i, y9.i, ya.i, yb.i));
        const cp2 yc = cmul2(y4, s16), yd = cmul2(y5, s16), ye = cmul2(y6, s16), yg = cmul2(y7, s16);
        put(0, 3, P4_(yc.r, yd.r, ye.r, yg.r), P4_(yc.i, yd.i, ye.i, yg.i));
      }
      {
        const cpx cf = cmul({bflo(r4), bflo(r5)}, {dt * bflo(r2), dt * bflo(r3)});
        const cpx e1 = cexpl(ar2, air, 1.f), e256 = cexpl(ar2, air, 256.f);
        const cpx e2 = csq(e1), e4 = csq(e2), e512 = csq(e256), e1024 = csq(e512);
        const cp2 cb = cmul2({{e1.r, e256.r}, {e1.i, e256.i}}, cf);
        const cp2 a0 = {{cf.r, cb.r.x}, {cf.i, cb.i.x}}, a1 = cmul2(a0, e2);
        const cp2 t0 = cmul2({{e2.r, e4.r}, {e2.i, e4.i}}, {cb.r.y, cb.i.y});
#define XA(m) (u32x4){P2_(a0.m), P2_(a1.m), pk(cb.m.y, cb.m.y), P2_(t0.m)}
#pragma unroll
        for (int s = 0; s < 4; ++s) yf[s] = get(0, s);
        put(1, 0, XA(r), XA(i));
        const cp2 a2 = cmul2(a0, e4), a3 = cmul2(a1, e4), t1 = cmul2(t0, e4);
#define XB(m) (u32x4){P2_(a2.m), P2_(a3.m), pk(t0.m.y, t0.m.y), P2_(t1.m)}
        put(1, 1, XB(r), XB(i));
#pragma unroll
        for (int s = 4; s < 8; ++s) yf[s] = get(0, s);
        const cp2 q0 = {{cf.r, cb.r.y}, {cf.i, cb.i.y}}, q1 = cmul2(q0, e512), q2 = cmul2(q0, e1024);
        const cp2 u4 = cmul2(q1, e4), u8 = cmul2(q1, e8);
#define XC(m) (u32x4){pk(q1.m.x, u4.m.x), pk(u8.m.x, q2.m.x), pk(q1.m.y, u4.m.y), pk(u8.m.y, q2.m.y)}
        put(1, 2, XC(r), XC(i));
        const cp2 q3 = cmul2(q1, e1024), v8 = cmul2(q2, e8), w8 = cmul2(q3, e8);
#define XD(m) (u32x4){pk(v8.m.x, q3.m.x), pk(w8.m.x, 0.f), pk(v8.m.y, q3.m.y), pk(w8.m.y, 0.f)}
        put(1, 3, XD(r), XD(i));
#pragma unroll
        for (int s = 0; s < 8; ++s) xf[s] = get(1, s);
      }

      STAMP_AT(17);
    }
    f32x16 kacc = (f32x16)(0.f);
#pragma unroll
    for (int s = 0; s < 8; ++s) kacc = MFMA32(xf[s], yf[s], kacc);
    auto kstore = [&](int g) {
      if (g < 3) *(LAS f32x4*)(kp4 + 1024 * g) = (f32x4){kacc[4 * g], kacc[4 * g + 1], kacc[4 * g + 2], kacc[4 * g + 3]};
      else { *(LAS float*)kp1 = kacc[12]; *(LAS float*)(kp1 + 256) = kacc[13]; *(LAS float*)(kp1 + 512) = kacc[14]; }
    };
    kstore(0); kstore(1); kstore(2); kstore(3);
    STAMP_AT(1);
    bf16x8 X0[4]; bf16x4 W0[16];
    BARRIER();
    MAKE_TAG();
    W0[0] = tr4(sm + UNIT + wa[0]); W0[1] = tr4(sm + UNIT + wa[0] + 1024);
#pragma unroll
    for (int t = 0; t < 4; ++t) X0[t] = ldfrag(sm + xa[0] + 2048 * t);
#pragma unroll
    for (int t = 2; t < 16; ++t) W0[t] = tr4(sm + UNIT + wa[t >> 2] + 1024 * (t & 3));
    SCHED();
    bf16x4 A[2][16]; bf16x8 B[2][8];
    auto cld1 = [&](int a, int n) {
      const int i = n / 3, r = n % 3;
      if (r < 2) A[a & 1][2 * i + r] = __builtin_amdgcn_ds_read_tr16_b64_v4bf16((LAS bf16x4*)(ka + 64 * a + 512 * i + 8 - 8 * r));
      else B[a & 1][i] = ldfrag(ua + 64 * (3 - a) + 512 * i);
    };
    auto cld = [&](int a, int i) { cld1(a, 3 * i); cld1(a, 3 * i + 1); cld1(a, 3 * i + 2); };
    if (aux) { BARRIER(); BARRIER();
      await(hpw, 0, 2);
#pragma unroll
      for (int i = 0; i < 8; ++i) cld(0, i);
    }
    else {
      f32x4 acc[4][8];
      bf16x8 X1[4]; bf16x4 W1[16];
      auto step = [&](const bf16x8 (&Xc)[4], const bf16x4 (&Wc)[16], bf16x8 (&Xn)[4], bf16x4 (&Wn)[16], u32 xb, u32 wb, int s, bool first, auto mid) {
        u32 xp, wp;
#pragma unroll
        for (int n = 0; n < 32; ++n) {
          const int t = n >> 2, r = n & 3, m = t & 1 ? 3 - r : r;
          const bf16x8 wf = cat(Wc[2 * t], Wc[2 * t + 1]);
          if (first) MFMA16_0(acc[m][t], wf, Xc[m]); else MFMA16_I(acc[m][t], wf, Xc[m]);
          SCHED();
          mid(n);
          if (n == 1) { xp = xb + xa[s]; PIN(xp); }
          if (n == 2 || n == 8 || n == 15 || n == 22) { wp = wb + wa[n / 7]; PIN(wp); }
          if (n >= 4 && n < 8) Wn[n - 4] = tr4(sm + wp + 1024 * (n - 4));
          if (n >= 9 && n < 13) Wn[n - 5] = tr4(sm + wp + 1024 * (n - 9));
          if (n == 13 || n == 14) Xn[n - 13] = ldfrag(sm + xp + 2048 * (n - 13));
          if (n >= 16 && n < 20) Wn[n - 8] = tr4(sm + wp + 1024 * (n - 16));
          if (n == 20 || n == 21) Xn[n - 18] = ldfrag(sm + xp + 2048 * (n - 18));
          if (n >= 23 && n < 27) Wn[n - 11] = tr4(sm + wp + 1024 * (n - 23));
          SCHED();
        }
      };
      u32 xb = 0;
      step(X0, W0, X1, W1, xb, 2 * UNIT, 1, true, [&](int) {});
#pragma unroll 1
      for (int cc = 0; cc < 6; ++cc) {
        xb = (xb + 3 * UNIT) & (RING - 1);
        step(X1, W1, X0, W0, xb, (xb + UNIT) & (RING - 1), 0, false, [&](int n) { if (n == 3) BARRIER(); });
        step(X0, W0, X1, W1, xb, (xb + 2 * UNIT) & (RING - 1), 1, false, [&](int) {});
      }
      step(X1, W1, X0, W0, 5 * UNIT, 6 * UNIT, 0, false, [&](int n) { if (n == 3) BARRIER(); });
      u32 st[4]; u32x2 cs[4]; u32x4 XM[4];
      float ka[4], kb[4], kr[4];
      step(X0, W0, X1, W1, 5 * UNIT, 7 * UNIT, 1, false, [&](int n) {
        if (n == 0) { LAS char* a = sm + L_ST + 8 * (64 * tg + (lane & 15)); st[0] = *(LAS u32*)a; st[1] = *(LAS u32*)(a + 128); st[2] = *(LAS u32*)(a + 256); st[3] = *(LAS u32*)(a + 384); }
        if (n >= 27 && n < 31) { LAS char* a = sm + L_CS + 1024 * g4 + 8 * (64 * fq + (lane & 15)) + 128 * (n - 27); cs[n - 27] = (u32x2){*(LAS u32*)a, *(LAS u32*)(a + 4096)}; }
        if (n >= 8) {
          const int t = (n - 8) / 6, r = (n - 8) % 6;
          if (r == 0) { kr[t] = bfhi(st[t]); ka[t] = __builtin_amdgcn_rcpf(kr[t]); PIN(kr[t]); PIN(ka[t]); }
          if (r == 1) { const u32 mm = ((st[t] & 0xffffu) ^ 0x8000u) * 0x10001u; XM[t].x = mm; PIN(XM[t]); }
          if (r == 2) { kb[t] = -2.f * 0.7978845608028654f * 1.44269504088896f * ka[t]; ka[t] *= ka[t]; PIN(kb[t]); PIN(ka[t]); }
          if (r == 3) { ka[t] *= 0.044715f; kr[t] *= kr[t]; PIN(ka[t]); PIN(kr[t]); }
          if (r == 4) { ka[t] *= kb[t]; XM[t].w = XM[t].x; PIN(ka[t]); PIN(XM[t]); }
          if (r == 5) { XM[t].y = 0; XM[t].z = 0; PIN(XM[t]); }
        }
      });
      STAMP_NW(19);
      auto last = [&](int a, int e) { const int t = (a & 4) + e; const bf16x8 wf = cat(W1[2 * t], W1[2 * t + 1]); MFMA16_I(acc[a & 3][t], wf, X1[a & 3]); SCHED(); };
      auto corr = [&](int a, int e) {
        const int t = (a & 4) + e;
        const u32x4 xm = XM[a & 3];
        const u32x2 xh = e & 1 ? (u32x2){xm.z, xm.w} : (u32x2){xm.x, xm.y};
        asm volatile("v_mfma_f32_16x16x16_bf16 %0, %1, %2, %0" : "+v"(acc[a & 3][t]) : "v"(cs[t >> 1]), "v"(xh));
        SCHED();
      };
#pragma unroll
      for (int e = 0; e < 4; ++e) last(0, e);
#pragma unroll
      for (int e = 0; e < 4; ++e) corr(0, e);
      BARRIER();
      asm volatile("s_nop 5" : "+v"(acc[0][0]), "+v"(acc[0][1]), "+v"(acc[0][2]), "+v"(acc[0][3]));
      u32 hseen;
      const u32 vh = ((2 * mi + tg) * 112 + 8 * fj + 4 * fq) * 2048 + lane * 16;
      SCHED();
#pragma unroll
      for (int a = 0; a < 8; ++a) {
        const int m = a & 3;
        f32x2 z[4], gu[4];
#pragma unroll
        for (int e = 0; e < 4; ++e) {
          const f32x4 gq = acc[m][(a & 4) + (e & 2)], uq = acc[m][(a & 4) + (e & 2) + 1];
          const f32x2 g = {gq[2 * (e & 1)], gq[2 * (e & 1) + 1]}, u = {uq[2 * (e & 1)], uq[2 * (e & 1) + 1]};
          z[e] = (g * g * ka[m] + kb[m]) * g; gu[e] = g * u;
        }
        SCHED();
        if (a == 7) { cld(0, 0); cld(0, 1); cld(0, 2); cld(0, 3); SCHED(); }
#pragma unroll
        for (int e = 0; e < 8; ++e) {
          if (a < 7 && e < 4) last(a + 1, e);
          z[e >> 1][e & 1] = __builtin_amdgcn_exp2f(z[e >> 1][e & 1]);
          SCHED();
        }
#pragma unroll
        for (int e = 0; e < 4; ++e) z[e] = z[e] * kr[m] + kr[m];
        SCHED();
        if (a == 7) { cld(0, 4); cld(0, 5); cld(0, 6); cld(0, 7); SCHED(); }
#pragma unroll
        for (int e = 0; e < 8; ++e) {
          if (a < 7 && e < 4) corr(a + 1, e);
          z[e >> 1][e & 1] = __builtin_amdgcn_rcpf(z[e >> 1][e & 1]);
          SCHED();
        }
        u32x4 hv;
#pragma unroll
        for (int e = 0; e < 4; ++e) { const f32x2 hq = gu[e] * z[e]; hv[e] = pk(hq.x, hq.y); }
        if (EXP & 32) asm volatile("" :: "v"(hv)); else st16_sc1(rws, hv, vh + 1024 * a);
#define FLAGA 6
        if (a == 1) STAMP_NW(20);
        if (a == 3) STAMP_NW(21);
        if (a == FLAGA) {
          STAMP_NW(22);
          asm volatile("s_waitcnt vmcnt(%0)" :: "i"(FLAGA - 3) : "memory");
          if (lane < 16) st8_sc1(rws, tag, lane * 8, FL_2 + ((2 * mi + tg) * 28 + 2 * fj + fq) * FS);
          STAMP_NW(18);
        }
#define GATEA 6
        if (a == GATEA) post(sm + S_GP + 4 * v, 2);
        if (a == 5) hseen = peek(hpw);
        if (a == 6) await(hpw, hseen, 2);
        SCHED();
      }
      STAMP_NW(23); STAMP_NWEND();
      STAMP_AT(3);
    }
    f32x4 y[8];
    const u32 vo = (64 * ti2 + 8 * v + (lane >> 3)) * (D * 2) + n2 * 128 + (lane & 7) * 16;
    u32x4 xres[2];
    LAS char* const pw = sm + S_GP + 4 * (v ^ 1);
    u32 pseen, vzs, zsrc; LAS char *exw, *exr; u32x4 zo[4];
    STAMP_AT(4);
#pragma unroll
    for (int a = 0; a < 4; ++a) {
      auto cm = [&](int n, int i, int j) {
        const bf16x8 af = __builtin_shufflevector(A[a & 1][2 * i], A[a & 1][2 * i + 1], 0, 1, 2, 3, 4, 5, 6, 7);
        if (a == 0 && j - i <= 1 && j >= i) asm volatile("v_mfma_f32_16x16x32_bf16 %0, %1, %2, 0" : "=&v"(y[i + j]) : "v"(af), "v"(B[a & 1][j]));
        else if ((EXP & 16) && a) asm volatile("" : "+v"(y[i + j]) : "v"(af), "v"(B[a & 1][j]));
        else asm volatile("v_mfma_f32_16x16x32_bf16 %0, %1, %2, %0" : "+v"(y[i + j]) : "v"(af), "v"(B[a & 1][j]));
        SCHED();
        if (a < 3 && n >= 4 && n < 28) cld1(a + 1, n - 4);
        if (a == 3) {
          if (n == 4) { exw = sm + (L_K + (4 * c + ah) * KCP + 16 * lane); PIN(exw); }
          if (n == 8) { exr = sm + (L_K + (4 * c + (ah ^ 1)) * KCP + 16 * lane); PIN(exr); }
          if (n == 12) { zsrc = 64 * p4 + (lane & 60); PIN(zsrc); }
          if (n == 16) { vzs = WS_ZT + ch * 8192 + 4096 * ah + 16 * lane; PIN(vzs); }
          if (n == 20) pseen = peek(pw);
          if (n >= 22 && n < 30 && !(n & 1)) { zo[(n - 22) / 2].y = tg32; zo[(n - 22) / 2].w = tg32; PIN(zo[(n - 22) / 2]); }
        }
        SCHED();
      };
      cm(0, 0, 0); cm(1, 0, 1); cm(2, 1, 1); cm(3, 1, 0); cm(4, 0, 2); cm(5, 1, 2); cm(6, 2, 2); cm(7, 2, 0); cm(8, 2, 1);
      cm(9, 0, 3); cm(10, 1, 3); cm(11, 2, 3); cm(12, 3, 3); cm(13, 3, 0); cm(14, 3, 1); cm(15, 3, 2); cm(16, 0, 4); cm(17, 1, 4);
      cm(18, 2, 4); cm(19, 3, 4); cm(20, 4, 0); cm(21, 4, 1); cm(22, 4, 2); cm(23, 4, 3); cm(24, 0, 5); cm(25, 1, 5); cm(26, 2, 5);
      cm(27, 5, 0); cm(28, 5, 1); cm(29, 5, 2); cm(30, 0, 6); cm(31, 1, 6); cm(32, 6, 0); cm(33, 6, 1); cm(34, 0, 7); cm(35, 7, 0);
      if (a == 2) post(sm + S_GP + 4 * v, 3);
#define FLAG_AT 1
      if (a == FLAG_AT) {
        STAMP_AT(5);
        WAIT_V(0);
        if (!aux && lane < 16) st8_sc1(rws, tag, lane * 8, FL_2 + FL_2B + ((2 * mi + c) * 28 + 2 * fj + ah) * FS);
        xres[0] = __builtin_amdgcn_raw_buffer_load_b128(rx, vo, 0, 0);
        xres[1] = __builtin_amdgcn_raw_buffer_load_b128(rx, vo, 32 * D * 2, 0);
        STAMP_AT(6);
      }
    }
    STAMP_AT(7);
    asm volatile("s_nop 7" : "+v"(y[0]), "+v"(y[1]), "+v"(y[2]), "+v"(y[3]), "+v"(y[4]), "+v"(y[5]), "+v"(y[6]), "+v"(y[7]));
    await<0>(pw, pseen, 3);
    auto finish = [&](int send, int keep) {
#pragma unroll
      for (int i = 0; i < 4; ++i) *(LAS f32x4*)(exw + 1024 * i) = y[send + i];
      post(sm + S_GP + 4 * v, 4);
      await<0>(pw, peek(pw), 4);
      f32x4 t[4];
#pragma unroll
      for (int i = 0; i < 4; ++i) t[i] = *(LAS f32x4*)(exr + 1024 * i);
      SCHED();
#pragma unroll
      for (int i = 0; i < 4; ++i) {
        zo[i].x = __builtin_amdgcn_ds_bpermute(zsrc, pk(vadd(t[i][0], y[keep + i][0]), vadd(t[i][1], y[keep + i][1])));
        zo[i].z = __builtin_amdgcn_ds_bpermute(zsrc, pk(vadd(t[i][2], y[keep + i][2]), vadd(t[i][3], y[keep + i][3])));
      }
    };
    if (ah) finish(0, 4); else finish(4, 0);
    STAMP_AT(8);
#pragma unroll
    for (int i = 0; i < 4; ++i) {
      if (EXP & 1) asm volatile("" :: "v"(zo[i])); else st16_sc1(rws, zo[i], vzs + 1024 * i);
      SCHED();
    }
    STAMP_AT(9);
#define ZSLEEP 8
    u32 vz = WS_ZT + (64 * n2 + 16 * v + g4) * 8192 + 256 * ti2 + (lane & 15) * 16;
    u32x4 zz[4];
    const int tok = 8 * v + (lane >> 3), dc = lane & 7;
    const u32 stg = L_K + (4 * c + 3) * KCP + 2048 * ah;
    LAS char *sw = sm + (stg + 8 * lane), *tr = sm + (stg + q4 * 128 + 32 * g4 + 8 * p4), *rb = sm + (stg - KCP + 32 * lane);
    LAS char* rz = sm + (L_K + (4 * (dc >> 2) + 2) * KCP + 2048 * ((dc >> 1) & 1) + 32 * tok + 16 * (dc & 1));
    u32 t0 = RB + UNIT + UNIT * 2 * (dc >> 2) + tok * 128 + (((2 * (dc & 3)) ^ (tok & 7) ^ (dc >> 2)) << 4), t1 = t0 ^ 16u;
    char *op0 = (char*)p.out + vo, *op1 = op0 + 32 * D * 2;
    f32x4 s0[2], s1[2];
#pragma unroll
    for (int i = 0; i < 2; ++i) {
      s0[i] = (f32x4){bflo(xres[i][0]), bfhi(xres[i][0]), bflo(xres[i][1]), bfhi(xres[i][1])};
      s1[i] = (f32x4){bflo(xres[i][2]), bfhi(xres[i][2]), bflo(xres[i][3]), bfhi(xres[i][3])};
    }
    asm volatile("" : "+v"(vz), "+v"(sw), "+v"(tr), "+v"(rb), "+v"(rz), "+v"(t0), "+v"(t1), "+v"(op0), "+v"(op1), "+v"(s0[0]), "+v"(s0[1]), "+v"(s1[0]), "+v"(s1[1]));
    if (b == 255 && v == 0) {
#pragma unroll 1
      for (int i = 0; i < 4; ++i) wait_flags<8>(rws, FL_A + (64 * i + lane) * 8, tlo, thi);
      if (lane == 0) st8_sc1(rws, tag, FL_E, 0);
    }
#define ZDELAY 12
    __builtin_amdgcn_s_sleep(ZDELAY);
    await(sy + S_HP, peek(sy + S_HP), 3);
    STAMP_AT(10);
#pragma unroll 1
    for (u32 s = 0; s < SPIN_CAP; ++s) {
      FENCE();
#pragma unroll
      for (int i = 0; i < 4; ++i) zz[i] = (EXP & 1) ? (u32x4){vz, tg32, vz, tg32} : ld16_sc1(rws, vz, 4 * 8192 * i);
      u32 d = 0;
#pragma unroll
      for (int i = 0; i < 4; ++i) {
        *(LAS u32*)(sw + 512 * i) = zz[i].x; *(LAS u32*)(sw + 512 * i + 4) = zz[i].z;
        d |= zz[i].y ^ tg32; PIN(d); d |= zz[i].w ^ tg32; PIN(d);
      }
      if (!__any(d != 0)) break;
      __builtin_amdgcn_s_sleep(ZSLEEP);
    }
    STAMP_AT(11);
    const bf16x8 ra = trfrag(tr, tr + 512), rc = trfrag(tr + 1024, tr + 1536);
    *(LAS bf16x8*)rb = ra;
    *(LAS bf16x8*)(rb + 16) = rc;
    post(sm + S_GP + 4 * v, 5);
    await<0>(sy + S_GP, peek(sy + S_GP), 5);
#pragma unroll
    for (int i = 0; i < 2; ++i) {
      const u32x4 z = *(LAS u32x4*)(rz + 1024 * i);
      s0[i] = (f32x4){vadd(s0[i][0], bflo(z[0])), vadd(s0[i][1], bfhi(z[0])), vadd(s0[i][2], bflo(z[1])), vadd(s0[i][3], bfhi(z[1]))};
      s1[i] = (f32x4){vadd(s1[i][0], bflo(z[2])), vadd(s1[i][1], bfhi(z[2])), vadd(s1[i][2], bflo(z[3])), vadd(s1[i][3], bfhi(z[3]))};
    }
    asm volatile("" : "+v"(s0[0]), "+v"(s0[1]), "+v"(s1[0]), "+v"(s1[1]));
    STAMP_AT(12);
#pragma unroll
    for (int i = 0; i < 2; ++i) {
      SCHED(); BARRIER(); SCHED();
      if (i == 0) STAMP_AT(13);
      f32x4 pa[4], pb[4];
#pragma unroll
      for (int g = 0; g < 4; ++g) { pa[g] = *(LAS f32x4*)(sm + t0 + UNIT * i + 4096 * g); pb[g] = *(LAS f32x4*)(sm + t1 + UNIT * i + 4096 * g); }
      SCHED();
#pragma unroll
      for (int g = 0; g < 4; ++g) { s0[i] += pa[g]; s1[i] += pb[g]; }
      *(GAS u32x4*)(i ? op1 : op0) = (u32x4){pk(s0[i][0], s0[i][1]), pk(s0[i][2], s0[i][3]), pk(s1[i][0], s1[i][1]), pk(s1[i][2], s1[i][3])};
    }
  }
  STAMP_AT(14); STAMP_OUT();
}

#define WSOFF 0
extern "C" void kernel_launch(void* const* d_in, const int* in_sizes, int n_in, void* d_out, int out_size, void* d_ws,
                              size_t ws_size, hipStream_t stream) {
  Params p;
  p.x = (const u16*)d_in[0]; p.are = (const u16*)d_in[1]; p.aim = (const u16*)d_in[2];
  p.bre = (const u16*)d_in[3]; p.bim = (const u16*)d_in[4]; p.cre = (const u16*)d_in[5];
  p.cim = (const u16*)d_in[6]; p.logdt = (const u16*)d_in[7]; p.dskip = (const u16*)d_in[8];
  p.wg = (const u16*)d_in[10]; p.wu = (const u16*)d_in[11]; p.wd = (const u16*)d_in[12];
  p.out = (u16*)d_out; p.ws = (char*)d_ws + ((size_t)WSOFF << 20);
  s4ffn_kernel<<<256, 512, 0, stream>>>(p);
}
```

```cpp
#include <hip/hip_runtime.h>
#include <stdint.h>

typedef unsigned short u16;
typedef unsigned u32;
typedef unsigned long long u64;
typedef __bf16 bf16x2 __attribute__((ext_vector_type(2)));
typedef __bf16 bf16x4 __attribute__((ext_vector_type(4)));
typedef __bf16 bf16x8 __attribute__((ext_vector_type(8)));
typedef float f32x2 __attribute__((ext_vector_type(2)));
typedef float f32x4 __attribute__((ext_vector_type(4)));
typedef float f32x16 __attribute__((ext_vector_type(16)));
typedef u32 u32x2 __attribute__((ext_vector_type(2)));
typedef u32 u32x4 __attribute__((ext_vector_type(4)));
#define LAS __attribute__((address_space(3)))
#define GAS __attribute__((address_space(1)))
#define DI __device__ __forceinline__
typedef __amdgpu_buffer_rsrc_t rsrc_t;

constexpr int L = 2048, D = 512, NM = 128, F = 1792;
constexpr u32 WS_H = 0, WS_U = 8u << 20, WS_ZT = 10u << 20, WS_ST = 14u << 20, WS_CS = WS_ST + 65536, WS_FL = 15u << 20, WS_END = 16u << 20;
constexpr u32 FL_E = WS_FL, FL_1 = WS_FL + 4096, FL_A = WS_FL + 8192, FL_2 = WS_FL + 131072, FL_2B = 131072, FS = 128;
constexpr u32 KCP = 4160, UNIT = 16384, RB = 32768, RING = 8 * UNIT;
constexpr u32 L_K = 0, L_U = 8 * KCP, US = 4608;
constexpr u32 S_GP = 4128, S_HP = 4144;
constexpr u32 L_P = 320, PS = 3840;
constexpr u32 L_NU0 = 15872, L_NUL = L_U + 2 * US;
constexpr u32 L_ST = 0;
constexpr u32 L_CS = KCP;
constexpr u32 CODE_BYTES = 30720;
constexpr u32 SPIN_CAP = 1u << 16;

struct Params {
  const u16 *x, *are, *aim, *bre, *bim, *cre, *cim, *logdt, *dskip, *wg, *wu, *wd;
  u16* out;
  char* ws;
};

#define STAMP_DECL
#define STAMP_AT(k)
#define STAMP_RT(v)
#define STAMP_NW(k)
#define STAMP_NWEND()
#define STAMP_OUT()

DI float bflo(u32 v) { return __builtin_bit_cast(float, v << 16); }
DI float bfhi(u32 v) { return __builtin_bit_cast(float, v & 0xffff0000u); }
DI u32 pk(float a, float b) { f32x2 f = {a, b}; return __builtin_bit_cast(u32, __builtin_convertvector(f, bf16x2)); }
DI float rbf(float a) { return bflo(pk(a, a)); }
DI void dma4(rsrc_t ra, rsrc_t rb, u32 lds, u32 v0, u32 v1, u32 v2, u32 v3, u32 soff) {
  asm volatile("s_mov_b32 m0, %0\n\ts_nop 0\n\tbuffer_load_dwordx4 %1, %5, %7 offen lds\n\tbuffer_load_dwordx4 %2, %5, %7 offen offset:1024 lds\n\t"
               "buffer_load_dwordx4 %3, %6, %7 offen offset:2048 lds\n\tbuffer_load_dwordx4 %4, %6, %7 offen offset:3072 lds"
               :: "s"(lds), "v"(v0), "v"(v1), "v"(v2), "v"(v3), "s"(ra), "s"(rb), "s"(soff));
}
DI void dma4s(rsrc_t r, u32 lds, u32 v0, u32 v1, u32 v2, u32 v3, u32 soff) {
  asm volatile("s_mov_b32 m0, %0\n\ts_nop 0\n\tbuffer_load_dwordx4 %1, %5, %6 offen sc1 lds\n\tbuffer_load_dwordx4 %2, %5, %6 offen offset:1024 sc1 lds\n\t"
               "buffer_load_dwordx4 %3, %5, %6 offen offset:2048 sc1 lds\n\tbuffer_load_dwordx4 %4, %5, %6 offen offset:3072 sc1 lds"
               :: "s"(lds), "v"(v0), "v"(v1), "v"(v2), "v"(v3), "s"(r), "s"(soff));
}
DI void dma1s(rsrc_t r, u32 lds, u32 v0, u32 soff) {
  asm volatile("s_mov_b32 m0, %0\n\ts_nop 0\n\tbuffer_load_dwordx4 %1, %2, %3 offen sc1 lds" :: "s"(lds), "v"(v0), "s"(r), "s"(soff));
}
#define WAIT_V(n) asm volatile("s_waitcnt vmcnt(" #n ")" ::: "memory")
#define WAIT_L() asm volatile("s_waitcnt lgkmcnt(0)" ::: "memory")
#define BARRIER() asm volatile("s_waitcnt lgkmcnt(0)\n\ts_barrier" ::: "memory")
#define FENCE() asm volatile("" ::: "memory")
#define SCHED() __builtin_amdgcn_sched_barrier(0)
DI bf16x8 ldfrag(LAS char* a) { return *(LAS bf16x8*)a; }
DI bf16x4 tr4(LAS char* a) { return __builtin_amdgcn_ds_read_tr16_b64_v4bf16((LAS bf16x4*)a); }
DI bf16x8 cat(bf16x4 lo, bf16x4 hi) { return __builtin_shufflevector(lo, hi, 0, 1, 2, 3, 4, 5, 6, 7); }
DI bf16x8 trfrag(LAS char* a0, LAS char* a1) {
  bf16x4 lo = __builtin_amdgcn_ds_read_tr16_b64_v4bf16((LAS bf16x4*)a0);
  bf16x4 hi = __builtin_amdgcn_ds_read_tr16_b64_v4bf16((LAS bf16x4*)a1);
  return __builtin_shufflevector(lo, hi, 0, 1, 2, 3, 4, 5, 6, 7);
}
#define MFMA32(a, b, c) __builtin_amdgcn_mfma_f32_32x32x16_bf16(a, b, c, 0, 0, 0)
#define MFMA16_0(d, a, b) asm volatile("v_mfma_f32_16x16x32_bf16 %0, %1, %2, 0" : "=&v"(d) : "v"(a), "v"(b))
#define MFMA16_I(d, a, b) asm volatile("v_mfma_f32_16x16x32_bf16 %0, %1, %2, %0" : "+v"(d) : "v"(a), "v"(b))
#define MFMA16(a, b, c) __builtin_amdgcn_mfma_f32_16x16x32_bf16(a, b, c, 0, 0, 0)
DI u32x2 ld8_sc1(rsrc_t r, u32 voff, u32 soff) { return __builtin_amdgcn_raw_buffer_load_b64(r, voff, soff, 16); }
DI u32x4 ld16_sc1(rsrc_t r, u32 voff, u32 soff) { return __builtin_amdgcn_raw_buffer_load_b128(r, voff, soff, 16); }
DI void st8_sc1(rsrc_t r, u32x2 v, u32 voff, u32 soff) { __builtin_amdgcn_raw_buffer_store_b64(v, r, voff, soff, 16); }
DI void st16_sc1(rsrc_t r, u32x4 v, u32 voff) { __builtin_amdgcn_raw_buffer_store_b128(v, r, voff, 0, 16); }
template <int SLEEP> DI void wait_flags(rsrc_t r, u32 voff, u32 tlo, u32 thi) {
#pragma unroll 1
  for (u32 s = 0; s < SPIN_CAP; ++s) {
    FENCE();
    u32x2 f = ld8_sc1(r, voff, 0);
    if (__all(f.x == tlo && f.y == thi)) break;
    __builtin_amdgcn_s_sleep(SLEEP);
  }
  WAIT_V(0);
}
DI void post(LAS char* word, u32 v) {
  asm volatile("s_mov_b64 exec, 1\n\tds_write_b32 %0, %1\n\ts_mov_b64 exec, -1" :: "v"(word), "v"(v) : "memory");
}
DI u32 peek(LAS char* word) { return *(volatile LAS u32*)word; }
template <int SLEEP = 1> DI void await(LAS char* word, u32 seen, u32 need) {
  if (__builtin_expect(__any(seen < need), 0)) {
#pragma unroll 1
    for (u32 s = 0; s < SPIN_CAP * 16; ++s) { __builtin_amdgcn_s_sleep(SLEEP); if (!__any(peek(word) < need)) break; }
  }
  FENCE();
}
#define DPPADD(name, ctl) DI float name(float x) { float r; asm("s_nop 1\n\tv_add_f32_dpp %0, %1, %1 " ctl " row_mask:0xf bank_mask:0xf" : "=v"(r) : "v"(x)); return r; }
DPPADD(add_x1, "quad_perm:[1,0,3,2]") DPPADD(add_x2, "quad_perm:[2,3,0,1]") DPPADD(add_m8, "row_half_mirror") DPPADD(add_x8, "row_ror:8") DPPADD(add_r4, "row_ror:4")
DI float fold32(float a, float b) {
  asm("s_nop 1\n\tv_permlane32_swap_b32 %0, %1\n\tv_add_f32 %0, %0, %1" : "+v"(a), "+v"(b)); return a;
}
DI float fold16(float a, float b) {
  asm("s_nop 1\n\tv_permlane16_swap_b32 %0, %1\n\tv_add_f32 %0, %0, %1" : "+v"(a), "+v"(b)); return a;
}
DI u32 vmov(u32 a) { u32 r; asm("v_mov_b32 %0, %1" : "=v"(r) : "v"(a)); return r; }
DI float vadd(float a, float b) { float r; asm("v_add_f32 %0, %1, %2" : "=v"(r) : "v"(a), "v"(b)); return r; }
#define PIN(x) asm volatile("" : "+v"(x))
struct cpx { float r, i; };
struct cp2 { f32x2 r, i; };
DI cpx cmul(cpx a, cpx b) { return {a.r * b.r - a.i * b.i, a.r * b.i + a.i * b.r}; }
DI cpx csq(cpx a) { return {a.r * a.r - a.i * a.i, (a.r + a.r) * a.i}; }
DI cp2 cmul2(cp2 a, cpx b) { return {a.r * b.r - a.i * b.i, a.i * b.r + a.r * b.i}; }
DI cpx cexpl(float ar2, float air, float l) {
  float m = __builtin_amdgcn_exp2f(ar2 * l), f = __builtin_amdgcn_fractf(air * l);
  return {m * __builtin_amdgcn_cosf(f), m * __builtin_amdgcn_sinf(f)};
}

#define EXP 0
__global__ __launch_bounds__(512, 2) void s4ffn_kernel(Params p) {
  __shared__ __attribute__((aligned(16))) char smem[163840];
  LAS char* sm = (LAS char*)smem;
  const u32 smb = (u32)(size_t)sm;
  const int tid = threadIdx.x, lane = tid & 63;
  const int w = __builtin_amdgcn_readfirstlane(tid >> 6), v = w & 3;
  const int b = blockIdx.x, xcd = b & 7, o = b >> 3;
  if ((w >> 1) == 1) {
    u64 pc; u32 d;
    asm volatile("s_getpc_b64 %0" : "=s"(pc));
    const u32 off = (2 * o + (w & 1)) * 512;
    asm volatile(".set pf_off, 0\n\t.rept 8\n\ts_load_dword %0, %1, pf_off\n\t.set pf_off, pf_off + 64\n\t.endr\n\ts_waitcnt lgkmcnt(0)"
                 : "=&s"(d) : "s"((pc & ~255ull) + (off < CODE_BYTES ? off : 0)));
  }
  const bool aux = o >= 28;
  const int ma = xcd >> 1, fh = xcd & 1;
  const int il = o / 7, jl = o - il * 7;
  const int mi = 4 * ma + il, fj = 7 * fh + jl;
  const int ti2 = 4 * xcd + (o >> 3), n2 = o & 7;
  const int ch0 = 64 * xcd + 2 * o;
  const int hh = lane >> 5, blk = (lane >> 4) & 1, q4 = (lane >> 2) & 3, p4 = lane & 3, g4 = lane >> 4;
  STAMP_DECL; STAMP_RT(rt0_); STAMP_AT(0);
#define DESCRIPTORS() const rsrc_t rx = __builtin_amdgcn_make_buffer_rsrc((void*)p.x, 0, L * D * 2, 0x00020000), \
  rws = __builtin_amdgcn_make_buffer_rsrc((void*)p.ws, 0, WS_END, 0x00020000)
  u64 ev;
#define LOAD_EPOCH() asm volatile("s_load_dwordx2 %0, %1, 0\n\ts_waitcnt lgkmcnt(0)" : "=&s"(ev) : "s"(p.ws + FL_E) : "memory")
#define MAKE_TAG() const u32 tlo = (u32)(ev + 0x9E3779B97F4A7C15ull), thi = (u32)((ev + 0x9E3779B97F4A7C15ull) >> 32), tg32 = tlo ^ thi; const u32x2 tag = {tlo, thi}
  LAS char* const sy = sm + 4 * p4;
  if (w == 3 && lane < 2) *(LAS u32x4*)(sm + S_GP + 16 * lane) = (u32x4)(0u);

  if (__builtin_expect(aux, 0)) {
    DESCRIPTORS();
    const int ab = 8 * ma + 4 * fh + o - 28, rb = 8 * ab + w;
    u32x4 xr[8];
#pragma unroll
    for (int i = 0; i < 8; ++i) xr[i] = __builtin_amdgcn_raw_buffer_load_b128(rx, lane * 16 + 1024 * (i & 3), (8 * rb + (i & 4)) * 1024, 0);
    LOAD_EPOCH();
    MAKE_TAG();
          u32 one = 0x3f803f80u;
          asm("" : "+v"(one));
          float su[8], sq[8];
#pragma unroll
          for (int r = 0; r < 8; ++r) {
            su[r] = sq[r] = 0.f;
#pragma unroll
            for (int e = 0; e < 4; ++e) {
              const u32 d = xr[r][e];
              su[r] = __builtin_amdgcn_fdot2_f32_bf16(__builtin_bit_cast(bf16x2, d), __builtin_bit_cast(bf16x2, one), su[r], false);
              sq[r] = __builtin_amdgcn_fdot2_f32_bf16(__builtin_bit_cast(bf16x2, d), __builtin_bit_cast(bf16x2, d), sq[r], false);
            }
          }
          asm volatile("s_nop 2" : "+v"(su[0]), "+v"(su[1]), "+v"(su[2]), "+v"(su[3]), "+v"(su[4]), "+v"(su[5]), "+v"(su[6]), "+v"(su[7]),
                                   "+v"(sq[0]), "+v"(sq[1]), "+v"(sq[2]), "+v"(sq[3]), "+v"(sq[4]), "+v"(sq[5]), "+v"(sq[6]), "+v"(sq[7]));
          auto total = [&](const float (&a)[8]) {
            const float lo = add_x8(fold16(fold32(a[0], a[2]), fold32(a[1], a[3]))), hi = add_x8(fold16(fold32(a[4], a[6]), fold32(a[5], a[7])));
            return add_m8(add_x2(add_x1(lane & 8 ? hi : lo)));
          };
          const float mean = total(su) * (1.f / D), nsq = -mean * mean;
          const float var = rbf(__builtin_fmaxf(__builtin_fmaf(total(sq), 1.f / D, nsq), 0.f));
          const u32 gran = pk(mean, __builtin_amdgcn_sqrtf(rbf(var + 1e-5f)));
          const float r = __builtin_amdgcn_rcpf(bfhi(gran)), mr = -bflo(gran) * r;
          u32 n[8][4];
#pragma unroll
          for (int i = 0; i < 4; ++i) {
            const int l0 = 16 * (2 * i & 3) + 8 * (i >> 1);
            auto at = [&](float t, int l) { return __builtin_bit_cast(float, __builtin_amdgcn_readlane(__builtin_bit_cast(int, t), l)); };
            const f32x2 r2 = {at(r, l0), at(r, l0 + 16)}, m2 = {at(mr, l0), at(mr, l0 + 16)};
#pragma unroll
            for (int e = 0; e < 4; ++e) {
              const u32 a = xr[2 * i][e], c = xr[2 * i + 1][e];
              const f32x2 lo = (f32x2){bflo(a), bflo(c)} * r2 + m2, hi = (f32x2){bfhi(a), bfhi(c)} * r2 + m2;
              n[2 * e][i] = pk(lo.x, lo.y); n[2 * e + 1][i] = pk(hi.x, hi.y);
            }
          }
    if ((lane & 7) == 0) st8_sc1(rws, (u32x2){gran, tg32}, 8 * g4 + 4 * (lane & 8), WS_ST + 64 * rb);
#pragma unroll
    for (int e = 0; e < 8; ++e) *(LAS u32x4*)(sm + RB + (8 * lane + e) * 128 + ((w ^ (lane & 7)) << 4)) = (u32x4){n[e][0], n[e][1], n[e][2], n[e][3]};
    STAMP_AT(18);
    BARRIER();
#pragma unroll
    for (int e = 0; e < 8; ++e) {
      const u32 row = 64 * w + 8 * e + (lane >> 3);
      st16_sc1(rws, *(LAS u32x4*)(sm + RB + row * 128 + (((lane & 7) ^ e) << 4)), WS_U + row * 4096 + ab * 128 + (lane & 7) * 16);
    }
    WAIT_V(0);
    if (lane == 0) st8_sc1(rws, tag, FL_1 + (w * 32 + ab) * 8, 0);
    STAMP_AT(19);
  }

  if (w < 4) {
    const int k = v & 1;
    u32 vx0 = (lane >> 3) * 1024 + (((lane & 7) ^ g4) << 4), vx1 = (vx0 ^ 64) + 7168, vx2 = vx0 + 14336, vx3 = vx1 + 14336;
    u32 vg0 = g4 * (F * 2) + (((((lane & 15) >> 1) ^ g4) << 5) | ((lane & 1) << 4)), vg1 = vg0 + 4 * F * 2 - 1024;
    u32 vu0 = vg0 ^ 128, vu1 = vu0 + 4 * F * 2 - 1024;
    u32 sx0 = (mi * 128 + 64 * v) * 1024, sw0 = 16 * v * (F * 2) + fj * 256;
    asm volatile("" : "+v"(vx0), "+v"(vx1), "+v"(vx2), "+v"(vx3), "+v"(vg0), "+v"(vg1), "+v"(vu0), "+v"(vu1));
    asm volatile("" : "+s"(sx0), "+s"(sw0));
    SCHED();
    DESCRIPTORS();
    const rsrc_t rwd = __builtin_amdgcn_make_buffer_rsrc((void*)p.wd, 0, D * F * 2, 0x00020000);
#define ROT2 0
    const int rot2 = ROT2;
    const u32 dq = smb + RB + 4096 * v;
    const u32 vd0 = (lane >> 3) * (D * 2) + (((lane & 7) ^ (g4 << 1)) << 4), vd1 = vd0 + 7168, vd2 = vd0 + 14336, vd3 = vd0 + 21504;
    auto ks_of = [&](int q) { int j = q % 7 + rot2; j = j >= 7 ? j - 7 : j; return 28 * v + 4 * j + 2 * (q / 7); };
    const u32 sd0 = 28 * v * (16 * D * 2) + n2 * 128;
    auto wdq = [&](int q, int slot) {
      u32 so = sd0;
      asm volatile("" : "+s"(so));
      dma4(rwd, rwd, dq + slot * UNIT, vd0, vd1, vd2, vd3, so + (ks_of(q) - 28 * v) * (16 * D * 2));
    };
    const u32 wo0 = RB + 4096 * v + hh * 1024 + (4 * blk + q4) * 128 + ((2 * blk + (q4 >> 1)) << 5) + p4 * 8;
    auto whalf = [&](int slot, int ct, int t) { return tr4(sm + (wo0 ^ (ct << 5)) + slot * (int)UNIT + 2048 * t); };
    bf16x8 wr_[7][4];
    auto take = [&](int q, int slot) {
#pragma unroll
      for (int e = 0; e < 4; ++e) wr_[q][e] = cat(whalf(slot, e, 0), whalf(slot, e, 1));
    };
    auto last7 = [&] {
    wdq(7, 1);
    {
      const u32 r = lane < 24 ? lane : 0, j = 4 + (r >> 3) + rot2, ks = 4 * (j >= 7 ? j - 7 : j);
      asm volatile("s_mov_b32 m0, %0\n\ts_nop 0\n\tbuffer_load_dword %1, %2, %3 offen lds" :: "s"(smb + L_NUL), "v"((16 * ks + (r & 7)) * (D * 2)), "s"(rwd), "s"((28 * v + 2) * (16 * D * 2) + (ti2 & 3) * 8192 + n2 * 128));
    }
    };
    u32x4 kc[4][4];
    if (v < 2 && !aux) {
        const rsrc_t rwg = __builtin_amdgcn_make_buffer_rsrc((void*)p.wg, 0, D * F * 2, 0x00020000);
        const rsrc_t rwu2 = __builtin_amdgcn_make_buffer_rsrc((void*)((const char*)p.wu - 2048), 0, D * F * 2 + 2048, 0x00020000);
        u32 dst = 8192 * v;
        auto ux = [&](u32 c) {
          const u32 s = sx0 + ((2 * ma + c) & 7) * 128;
          dma4(rx, rx, smb + RB + dst, vx0, vx1, vx2, vx3, s); dma4(rx, rx, smb + RB + dst + 4096, vx0, vx1, vx2, vx3, s + 32768);
          dst = (dst + UNIT) & (RING - 1);
        };
        auto uw = [&](u32 c, u32 h) {
          const u32 s = sw0 + (2 * ((2 * ma + c) & 7) + h) * (32 * F * 2);
          dma4(rwg, rwu2, smb + RB + dst, vg0, vg1, vg0, vg1, s); dma4(rwg, rwu2, smb + RB + dst + 4096, vu0, vu1, vu0, vu1, s + 8 * F * 2);
          dst = (dst + UNIT) & (RING - 1);
        };
#define LDELAY 4
      __builtin_amdgcn_s_sleep(LDELAY);
      ux(0); uw(0, 0);
      LOAD_EPOCH();
      uw(0, 1); ux(1); uw(1, 0);
#pragma unroll
      for (int c = 0; c < 7; ++c) {
        if (c) { uw(c, 1); ux(c + 1); WAIT_V(16); take(c - 1, -1); uw(c + 1, 0); WAIT_L(); SCHED(); }
        wdq(c, -1);
        WAIT_V(20);
        if (c == 0) STAMP_AT(1);
        __builtin_amdgcn_s_barrier();
      }
      uw(7, 1);
      last7();
      WAIT_V(5);
    } else {
      const bool wrk = v >= 2, csm = wrk && !aux;
      const int mat = il >> 1, r8 = (lane >> 2) & 7;
      LAS char* const cp = sm + RB + UNIT + hh * 4096 + mat * 2048 + r8 * 256 + (((4 * (il & 1) + 2 * k + (p4 >> 1)) ^ ((r8 & 3) + 4 * hh)) << 5) + (lane & 1) * 16;
      const u32 vf1 = FL_1 + (32 * xcd + (lane & 31)) * 8;
      auto stats = [&] {
        dma4s(rws, smb + L_CS + 4096 * k, lane * 16, lane * 16, lane * 16, lane * 16, WS_CS + (fj * 2 + k) * 4096);
        if (k) dma1s(rws, smb + L_ST, lane * 16, WS_ST + mi * 1024);
      };
      float s[8]; u32x4 ct[8]; u32x2 f1; u32 dsk; bool f1bad;
      auto cadd = [&](int i) {
#pragma unroll
        for (int e = 0; e < 4; ++e) {
          if (i == 0) { s[2 * e] = bflo(ct[0][e]); s[2 * e + 1] = bfhi(ct[0][e]); }
          else asm("v_add_f32 %0, %0, %2\n\tv_add_f32 %1, %1, %3" : "+v"(s[2 * e]), "+v"(s[2 * e + 1]) : "v"(bflo(ct[i][e])), "v"(bfhi(ct[i][e])));
        }
      };
#define TCH 3
#define TDELAY 4
      dsk = p.dskip[ch0 + k];
      if (TDELAY) __builtin_amdgcn_s_sleep(TDELAY);
      if ((TCH & 1) && v == 2) asm volatile("s_mov_b32 m0, %0\n\ts_nop 0\n\tbuffer_load_dword %1, %2, %3 offen lds" :: "s"(smb + L_NU0), "v"(lane < 56 ? lane * 128 : 0), "s"(rwd), "s"(b * 7168));
      if ((TCH & 2) && v == 3 && !aux) {
        const rsrc_t rw = __builtin_amdgcn_make_buffer_rsrc((void*)(mat ? p.wu : p.wg), 0, D * F * 2, 0x00020000);
        asm volatile("s_mov_b32 m0, %0\n\ts_nop 0\n\tbuffer_load_dword %1, %2, %3 offen lds" :: "s"(smb + L_NU0), "v"((lane & 31) * (F * 2)), "s"(rw), "s"((128 * ma + 96) * (F * 2) + fj * 256 + (il & 1) * 128));
      }
      if (!aux) LOAD_EPOCH();
      MAKE_TAG();
#define WDELAY 8
      __builtin_amdgcn_s_sleep(WDELAY);
      wdq(0, -1);
#pragma unroll
      for (int c = 0; c < 7; ++c) {
        if (c) {
          if ((c == 2 && v == 3) || (c == 3 && csm)) WAIT_V(1); else if (c == 6 && csm) WAIT_V(5); else WAIT_V(0);
          if (c == 1) PIN(dsk);
          take(c - 1, -1);
          WAIT_L(); SCHED();
        }
        if (!aux || c == 2) __builtin_amdgcn_s_barrier();
        FENCE(); SCHED();
#define KPRIO 1
#define STC 5
        __builtin_amdgcn_s_setprio(KPRIO);
        if (c == 6 && wrk) f1bad = !__all(f1.x == tlo && f1.y == thi);
        if (c) wdq(c, -1);
        if (c == 6) last7();
        if (c == 1 && v == 3 && lane == 0) st8_sc1(rws, tag, FL_A + b * 8, 0);
        if (c == STC && csm) stats();
        if (c == 5 && wrk) f1 = ld8_sc1(rws, vf1, 0);
        if (c == 6 && wrk) {
          if (f1bad) wait_flags<8>(rws, vf1, tlo, thi);
          FENCE();
          dma4s(rws, smb + L_U + US * k + 512, lane * 16, lane * 16, lane * 16, lane * 16, WS_U + (ch0 + k) * 4096);
        }
        __builtin_amdgcn_s_setprio(0);
        SCHED();
        if (c < 2 && csm) {
#pragma unroll
          for (int i = 0; i < 4; ++i) ct[4 * c + i] = *(LAS u32x4*)(cp + 3 * UNIT * c + 8192 * i);
#pragma unroll
          for (int i = 0; i < 4; ++i) cadd(4 * c + i);
          asm volatile("" : "+v"(s[0]), "+v"(s[1]), "+v"(s[2]), "+v"(s[3]), "+v"(s[4]), "+v"(s[5]), "+v"(s[6]), "+v"(s[7]));
        }
        if (c == 3 && wrk) {
        float kv[16];
        LAS char* pa = sm + (k ? L_P + 2 * PS : 0), *pb = sm + L_P + (2 * k + 1) * PS;
#pragma unroll
        for (int g = 0; g < 3; ++g) {
          const f32x4 a = *(LAS f32x4*)(pa + 1024 * g + 16 * lane), d = *(LAS f32x4*)(pb + 1024 * g + 16 * lane);
#pragma unroll
          for (int e = 0; e < 4; ++e) asm("v_add_f32 %0, %1, %2" : "=v"(kv[4 * g + e]) : "v"(a[e]), "v"(d[e]));
          asm("v_add_f32 %0, %1, %2" : "=v"(kv[12 + g]) : "v"(*(LAS float*)(pa + 3072 + 256 * g + 4 * lane)), "v"(*(LAS float*)(pb + 3072 + 256 * g + 4 * lane)));
        }
        const bool odd = lane & 1;
        if (lane == 0) kv[0] += bflo(dsk);
        kc[0][0] = (u32x4){pk(kv[0], kv[1]), pk(kv[2], kv[3]), pk(kv[4], kv[5]), pk(kv[6], kv[7])};
        kc[1][0] = (u32x4){pk(kv[8], kv[8]), pk(kv[8], kv[9]), pk(kv[9], kv[9]), pk(kv[10], kv[10])};
        kc[2][0] = (u32x4){pk(kv[11], kv[11]), pk(kv[11], kv[11]), pk(odd ? kv[12] : kv[11], kv[12]), pk(kv[12], kv[12])};
        kc[3][0] = (u32x4){pk(kv[13], kv[13]), pk(kv[13], kv[13]), pk(odd ? kv[14] : kv[13], kv[14]), pk(kv[14], kv[14])};
          asm volatile("" : "+v"(kc[0][0]), "+v"(kc[1][0]), "+v"(kc[2][0]), "+v"(kc[3][0]));
        }
        if (c == 4 && wrk) {
#pragma unroll
        for (int g = 0; g < 4; ++g) {
          const u32x4 cu = kc[g][0];
          int px = 0, py = 0;
          if (g) {
            px = __builtin_amdgcn_update_dpp(0, (int)kc[g ? g - 1 : 0][0].z, 0x13C, 0xf, 0xf, false);
            py = __builtin_amdgcn_update_dpp(0, (int)kc[g ? g - 1 : 0][0].w, 0x13C, 0xf, 0xf, false);
          }
          px = __builtin_amdgcn_update_dpp(px, (int)cu.z, 0x138, 0xf, 0xf, false);
          py = __builtin_amdgcn_update_dpp(py, (int)cu.w, 0x138, 0xf, 0xf, false);
          const u32 s0 = __builtin_amdgcn_alignbit(py, px, 16), s1 = __builtin_amdgcn_alignbit(cu.x, py, 16), s2 = __builtin_amdgcn_alignbit(cu.y, cu.x, 16),
                    s3 = __builtin_amdgcn_alignbit(cu.z, cu.y, 16), s4 = __builtin_amdgcn_alignbit(cu.w, cu.z, 16);
          kc[g][1] = (u32x4){s1, s2, s3, s4}; kc[g][2] = (u32x4){(u32)py, vmov(cu.x), vmov(cu.y), vmov(cu.z)}; kc[g][3] = (u32x4){s0, vmov(s1), vmov(s2), vmov(s3)};
          asm volatile("" : "+v"(kc[g][0]), "+v"(kc[g][1]), "+v"(kc[g][2]), "+v"(kc[g][3]));
        }
          STAMP_AT(2);
        }
        if (c == 2 && csm) {
          const float lo = add_x8(add_r4(fold16(fold32(s[0], s[4]), fold32(s[2], s[6])))), hi = add_x8(add_r4(fold16(fold32(s[1], s[5]), fold32(s[3], s[7]))));
          s[0] = lane & 8 ? hi : lo;
          const float th = rbf(s[0]);
          if (!(lane & 4)) st8_sc1(rws, (u32x2){pk(th, s[0] - th), tg32}, 8 * (8 * p4 + 4 * hh + 2 * blk + ((lane >> 3) & 1)), WS_CS + (((fj * 2 + mat) * 4 + ma) * 128 + 64 * (il & 1) + 32 * k) * 8);
        }
        if (c == STC + 1 && wrk && !aux) {
#pragma unroll 1
        for (u32 s = 0; s < SPIN_CAP; ++s) {
          u32 d = 0;
#pragma unroll
          for (int i = 0; i < 5; ++i) {
            const u32x4 t = *(LAS u32x4*)(sm + (i < 4 || !k ? L_CS + 4096 * k + 1024 * (i & 3) : L_ST) + 16 * lane);
            d |= (t.y ^ tg32) | (t.w ^ tg32);
          }
          if (!__any(d != 0)) break;
          stats();
          WAIT_V(0);
        }
        }
      }
    }
    STAMP_AT(4);
    MAKE_TAG();
    STAMP_AT(6);
    BARRIER();
    wdq(8, 2); wdq(9, 3); wdq(10, 4);
    WAIT_V(17); take(6, -1);
    LAS char* kd = sm + L_K + 4 * KCP * k + 16 * lane;
    LAS char* kz = sm + (lane < 32 ? L_U + US * k + 16 * lane : L_K + (4 * k + ((lane >> 1) & 3)) * KCP + 16 * (lane & 1));
    asm volatile("" : "+v"(kd), "+v"(kz));
    u32x4 zero = (u32x4)(0u);
    u32 two = 2;
    LAS char* hpw = sm + S_HP + 4 * v;
    asm volatile("" : "+v"(zero), "+v"(two), "+v"(hpw));
    STAMP_AT(5);
    SCHED();
    BARRIER();
    STAMP_AT(7);
    if (v >= 2) {
      WAIT_V(0);
      *(LAS u32x4*)kz = zero;
#pragma unroll
      for (int e = 0; e < 16; ++e) *(LAS u32x4*)(kd + 32 + KCP * (e & 3) + 1024 * (e >> 2)) = kc[e >> 2][e & 3];
      post(hpw, two);
      WAIT_L();
    } else __builtin_amdgcn_s_sleep(8);
    STAMP_AT(8);
#define LATEQ 1
#define WPRIO 2
    if (!LATEQ) { wdq(11, 5); wdq(12, 6); wdq(13, 7); }
    __builtin_amdgcn_s_setprio(WPRIO);
#define HDELAY 0
#define AUXSLEEP 32
    if (!aux) { await<4>(sy + S_GP, peek(sy + S_GP), 2); __builtin_amdgcn_s_sleep(HDELAY); }
    else __builtin_amdgcn_s_sleep(AUXSLEEP);
    if (LATEQ == 1) { __builtin_amdgcn_s_setprio(0); wdq(11, 5); wdq(12, 6); wdq(13, 7); }
    STAMP_AT(9);
    const u32 vf2 = FL_2 + (ti2 * 28 + 7 * v + (lane < 7 ? lane : 0)) * FS;
#define APOLL 1
    if (APOLL) {
#pragma unroll 1
      for (u32 s = 0; s < SPIN_CAP; ++s) {
        u32x2 f = tag;
        if (lane < 7) { f = (u32x2)(0u); asm volatile("buffer_atomic_or_x2 %0, %1, %2, 0 offen sc0 sc1\n\ts_waitcnt vmcnt(0)" : "+v"(f) : "v"(vf2), "s"(rws) : "memory"); }
        if ((EXP & 4) || __all(f.x == tlo && f.y == thi)) break;
        __builtin_amdgcn_s_sleep(2);
      }
      FENCE();
    } else wait_flags<8>(rws, vf2, tlo, thi);
    __builtin_amdgcn_s_setprio(0);
    STAMP_AT(10);
    f32x4 acc[4][4];
#define PD 12
#define ZAT 9
    u32x4 hf[56];
    auto hload = [&](int n) { hf[n] = ld16_sc1(rws, lane * 16 + 1024 * (n & 3), (ti2 * 112 + ks_of(n >> 2)) * 2048); };
#pragma unroll
    for (int n = 0; n < PD; ++n) hload(n);
    bf16x4 wl[2][8];
    u32x2 fb; u32 pw_[4];
    SCHED();
#pragma unroll
    for (int i = 0; i < 14; ++i) {
#pragma unroll
      for (int n = 0; n < 16; ++n) {
        const int m = n >> 2, ct = m & 1 ? 3 - (n & 3) : n & 3, nx = 4 * i + m + PD;
        const bf16x8 a = i < 7 ? wr_[i < 7 ? i : 0][ct] : cat(wl[i & 1][2 * ct], wl[i & 1][2 * ct + 1]);
        const bf16x8 bb = __builtin_bit_cast(bf16x8, hf[4 * i + m]);
        if (i == 0) MFMA16_0(acc[ct][m], a, bb); else MFMA16_I(acc[ct][m], a, bb);
        SCHED();
        if ((n & 3) == 3 && nx < 56) hload(nx);
        if ((n & 3) < 2 && i >= 6 && i < 13) wl[(i + 1) & 1][2 * m + (n & 1)] = whalf(i - 5, m, n & 1);
        if ((n & 3) == 2) {
          if (nx == 20) fb = ld8_sc1(rws, vf2, FL_2B);
          if (nx == 28) {
            if (__builtin_expect(!__all(fb.x == tlo && fb.y == thi), 0)) wait_flags<4>(rws, vf2 + FL_2B, tlo, thi);
            FENCE();
          }
          if (i == ZAT && m == 0) post(sm + S_HP + 4 * v, 3);
          if (i == 13) { pw_[m] = (RB + UNIT + 4096 * v + (lane & 15) * 128 + ((g4 ^ (lane & 7)) << 4)) ^ ((m & 1) << 6) ^ ((m >> 1) << 4); PIN(pw_[m]); }
        }
        SCHED();
      }
      if (i == 3) STAMP_AT(15);
      if (i == 6) STAMP_AT(16);
      if (i == 10) STAMP_AT(17);
    }
    STAMP_AT(11);
#pragma unroll
    for (int tt = 0; tt < 2; ++tt) {
      if (tt) asm volatile("s_nop 7" : "+v"(acc[0][3]), "+v"(acc[1][3]), "+v"(acc[2][3]), "+v"(acc[3][3]));
#pragma unroll
      for (int a = 0; a < 8; ++a) {
        const int ct = a & 3, m = 2 * tt + (a >> 2);
        *(LAS f32x4*)(sm + pw_[ct] + UNIT * (2 * (ct >> 1) + tt) + 2048 * (m & 1)) = acc[ct][m];
      }
      if (tt) STAMP_AT(12);
      BARRIER();
    }
  } else {
    __builtin_amdgcn_s_setprio(1);
    const int c = v >> 1, ah = v & 1, ch = ch0 + c;
    u32 gi = (ch * NM + 64 * ah + lane) * 2, gd = ch * 2;
    asm volatile("" : "+v"(gi), "+v"(gd));
    SCHED();
    auto at = [](const u16* q, u32 off) { return (u32)*(const u16*)((const char*)q + off); };
    const u32 r6 = at(p.logdt, gd), r0 = at(p.are, gi), r1 = at(p.aim, gi), r2 = at(p.bre, gi), r3 = at(p.bim, gi), r4 = at(p.cre, gi), r5 = at(p.cim, gi);
    SCHED();
    DESCRIPTORS();
    const int tg = c, fq = ah;
    u32 xa[2], wa[4], iw[4], ir[2];
    {
      const u32 row = 64 * tg + (lane & 15), f = (row >> 1) & 7, scr = RB + 5 * UNIT + 4096 * v, cki = 2 * blk + (p4 >> 1);
#pragma unroll
      for (int s = 0; s < 4; ++s) {
        if (s < 2) xa[s] = RB + row * 128 + (((4 * s + g4) ^ f) << 4);
        wa[s] = RB + g4 * 4096 + q4 * 256 + (((4 * fq + s) ^ (q4 + 4 * blk)) << 5) + p4 * 8;
        iw[s] = scr + lane * 64 + ((s ^ ((lane >> 1) & 3)) << 4);
        asm volatile("" : "+v"(xa[s & 1]), "+v"(wa[s]), "+v"(iw[s]));
      }
      ir[0] = scr + (8 * hh + q4) * 64 + ((cki ^ (q4 >> 1)) << 4) + 8 * (p4 & 1);
      ir[1] = scr + (8 * hh + 4 + q4) * 64 + ((cki ^ (2 + (q4 >> 1))) << 4) + 8 * (p4 & 1);
      asm volatile("" : "+v"(ir[0]), "+v"(ir[1]));
    }
    LAS char* ka = sm + L_K + (4 * c + q4) * KCP + 2 * (28 + 128 * ah + 4 * p4 - 8 * g4);
    LAS char* ua = sm + L_U + US * c + 2 * (240 - 96 - 128 * ah + 16 * (lane & 15) + 8 * g4);
    LAS char *hpw = sm + S_HP + 8 + 4 * (lane & 1), *kp4 = sm + ((v ? L_P + PS * v : 0) + 16 * lane), *kp1 = sm + ((v ? L_P + PS * v : 0) + 3072 + 4 * lane);
    asm volatile("" : "+v"(ka), "+v"(ua), "+v"(hpw), "+v"(kp4), "+v"(kp1));
    u64 ev2;
    asm volatile("s_load_dwordx2 %0, %1, 0" : "=&s"(ev2) : "s"(p.ws + FL_E) : "memory");
    SCHED();
    STAMP_AT(15);
    STAMP_AT(16);
    bf16x8 yf[8], xf[8];
    {
      const float dt = rbf(__builtin_amdgcn_exp2f(bflo(r6) * 1.44269504088896f));
      const float ar2 = dt * bflo(r0) * 1.44269504088896f, air = dt * bflo(r1) * 0.159154943091895f;
      auto put = [&](int img, int g, u32x4 re, u32x4 im) {
        if (EXP & 8) return;
        *(LAS u32x4*)(sm + iw[g] + 2 * img * UNIT) = re; *(LAS u32x4*)(sm + iw[g] + (1 - img) * UNIT) = im;
        SCHED();
      };
      auto get = [&](int img, int s) { const u32 d = (s < 4 ? 2 * img : 1 - img) * UNIT + 1024 * (s & 3); return trfrag(sm + ir[0] + d, sm + ir[1] + d); };
#define P2_(a) pk((a).x, (a).y)
#define P4_(a, b, c, d) (u32x4){P2_(a), P2_(b), P2_(c), P2_(d)}
      const cpx e8 = cexpl(ar2, air, 8.f);
      {
        const cpx s1 = {e8.r, -e8.i}, s2 = csq(s1), s4 = csq(s2), s8 = cexpl(ar2, -air, 64.f), s16 = csq(s8);
        const cp2 y0 = {{1.f, s1.r}, {0.f, s1.i}}, y1 = cmul2(y0, s2), y2 = cmul2(y0, s4), y3 = cmul2(y1, s4);
        SCHED();
        asm volatile("s_waitcnt lgkmcnt(0)" : "+s"(ev2) :: "memory");
        if (!aux) ev = ev2;
        put(0, 0, P4_(y0.r, y1.r, y2.r, y3.r), P4_(y0.i, y1.i, y2.i, y3.i));
        const cp2 y4 = cmul2(y0, s8), y5 = cmul2(y1, s8), y6 = cmul2(y2, s8), y7 = cmul2(y3, s8);
        put(0, 1, P4_(y4.r, y5.r, y6.r, y7.r), P4_(y4.i, y5.i, y6.i, y7.i));
        const cp2 y8 = cmul2(y0, s16), y9 = cmul2(y1, s16), ya = cmul2(y2, s16), yb = cmul2(y3, s16);
        put(0, 2, P4_(y8.r, y9.r, ya.r, yb.r), P4_(y8.i, y9.i, ya.i, yb.i));
        const cp2 yc = cmul2(y4, s16), yd = cmul2(y5, s16), ye = cmul2(y6, s16), yg = cmul2(y7, s16);
        put(0, 3, P4_(yc.r, yd.r, ye.r, yg.r), P4_(yc.i, yd.i, ye.i, yg.i));
      }
      {
        const cpx cf = cmul({bflo(r4), bflo(r5)}, {dt * bflo(r2), dt * bflo(r3)});
        const cpx e1 = cexpl(ar2, air, 1.f), e256 = cexpl(ar2, air, 256.f);
        const cpx e2 = csq(e1), e4 = csq(e2), e512 = csq(e256), e1024 = csq(e512);
        const cp2 cb = cmul2({{e1.r, e256.r}, {e1.i, e256.i}}, cf);
        const cp2 a0 = {{cf.r, cb.r.x}, {cf.i, cb.i.x}}, a1 = cmul2(a0, e2);
        const cp2 t0 = cmul2({{e2.r, e4.r}, {e2.i, e4.i}}, {cb.r.y, cb.i.y});
#define XA(m) (u32x4){P2_(a0.m), P2_(a1.m), pk(cb.m.y, cb.m.y), P2_(t0.m)}
#pragma unroll
        for (int s = 0; s < 4; ++s) yf[s] = get(0, s);
        put(1, 0, XA(r), XA(i));
        const cp2 a2 = cmul2(a0, e4), a3 = cmul2(a1, e4), t1 = cmul2(t0, e4);
#define XB(m) (u32x4){P2_(a2.m), P2_(a3.m), pk(t0.m.y, t0.m.y), P2_(t1.m)}
        put(1, 1, XB(r), XB(i));
#pragma unroll
        for (int s = 4; s < 8; ++s) yf[s] = get(0, s);
        const cp2 q0 = {{cf.r, cb.r.y}, {cf.i, cb.i.y}}, q1 = cmul2(q0, e512), q2 = cmul2(q0, e1024);
        const cp2 u4 = cmul2(q1, e4), u8 = cmul2(q1, e8);
#define XC(m) (u32x4){pk(q1.m.x, u4.m.x), pk(u8.m.x, q2.m.x), pk(q1.m.y, u4.m.y), pk(u8.m.y, q2.m.y)}
        put(1, 2, XC(r), XC(i));
        const cp2 q3 = cmul2(q1, e1024), v8 = cmul2(q2, e8), w8 = cmul2(q3, e8);
#define XD(m) (u32x4){pk(v8.m.x, q3.m.x), pk(w8.m.x, 0.f), pk(v8.m.y, q3.m.y), pk(w8.m.y, 0.f)}
        put(1, 3, XD(r), XD(i));
#pragma unroll
        for (int s = 0; s < 8; ++s) xf[s] = get(1, s);
      }

      STAMP_AT(17);
    }
    f32x16 kacc = (f32x16)(0.f);
#pragma unroll
    for (int s = 0; s < 8; ++s) kacc = MFMA32(xf[s], yf[s], kacc);
    auto kstore = [&](int g) {
      if (g < 3) *(LAS f32x4*)(kp4 + 1024 * g) = (f32x4){kacc[4 * g], kacc[4 * g + 1], kacc[4 * g + 2], kacc[4 * g + 3]};
      else { *(LAS float*)kp1 = kacc[12]; *(LAS float*)(kp1 + 256) = kacc[13]; *(LAS float*)(kp1 + 512) = kacc[14]; }
    };
    kstore(0); kstore(1); kstore(2); kstore(3);
    STAMP_AT(1);
    bf16x8 X0[4]; bf16x4 W0[16];
    BARRIER();
    MAKE_TAG();
    W0[0] = tr4(sm + UNIT + wa[0]); W0[1] = tr4(sm + UNIT + wa[0] + 1024);
#pragma unroll
    for (int t = 0; t < 4; ++t) X0[t] = ldfrag(sm + xa[0] + 2048 * t);
#pragma unroll
    for (int t = 2; t < 16; ++t) W0[t] = tr4(sm + UNIT + wa[t >> 2] + 1024 * (t & 3));
    SCHED();
    bf16x4 A[2][16]; bf16x8 B[2][8];
    auto cld1 = [&](int a, int n) {
      const int i = n / 3, r = n % 3;
      if (r < 2) A[a & 1][2 * i + r] = __builtin_amdgcn_ds_read_tr16_b64_v4bf16((LAS bf16x4*)(ka + 64 * a + 512 * i + 8 - 8 * r));
      else B[a & 1][i] = ldfrag(ua + 64 * (3 - a) + 512 * i);
    };
    auto cld = [&](int a, int i) { cld1(a, 3 * i); cld1(a, 3 * i + 1); cld1(a, 3 * i + 2); };
    if (aux) { BARRIER(); BARRIER();
      await(hpw, 0, 2);
#pragma unroll
      for (int i = 0; i < 8; ++i) cld(0, i);
    }
    else {
      f32x4 acc[4][8];
      bf16x8 X1[4]; bf16x4 W1[16];
      auto step = [&](const bf16x8 (&Xc)[4], const bf16x4 (&Wc)[16], bf16x8 (&Xn)[4], bf16x4 (&Wn)[16], u32 xb, u32 wb, int s, bool first, auto mid) {
        u32 xp, wp;
#pragma unroll
        for (int n = 0; n < 32; ++n) {
          const int t = n >> 2, r = n & 3, m = t & 1 ? 3 - r : r;
          const bf16x8 wf = cat(Wc[2 * t], Wc[2 * t + 1]);
          if (first) MFMA16_0(acc[m][t], wf, Xc[m]); else MFMA16_I(acc[m][t], wf, Xc[m]);
          SCHED();
          mid(n);
          if (n == 1) { xp = xb + xa[s]; PIN(xp); }
          if (n == 2 || n == 8 || n == 15 || n == 22) { wp = wb + wa[n / 7]; PIN(wp); }
          if (n >= 4 && n < 8) Wn[n - 4] = tr4(sm + wp + 1024 * (n - 4));
          if (n >= 9 && n < 13) Wn[n - 5] = tr4(sm + wp + 1024 * (n - 9));
          if (n == 13 || n == 14) Xn[n - 13] = ldfrag(sm + xp + 2048 * (n - 13));
          if (n >= 16 && n < 20) Wn[n - 8] = tr4(sm + wp + 1024 * (n - 16));
          if (n == 20 || n == 21) Xn[n - 18] = ldfrag(sm + xp + 2048 * (n - 18));
          if (n >= 23 && n < 27) Wn[n - 11] = tr4(sm + wp + 1024 * (n - 23));
          SCHED();
        }
      };
      u32 xb = 0;
      step(X0, W0, X1, W1, xb, 2 * UNIT, 1, true, [&](int) {});
#pragma unroll 1
      for (int cc = 0; cc < 6; ++cc) {
        xb = (xb + 3 * UNIT) & (RING - 1);
        step(X1, W1, X0, W0, xb, (xb + UNIT) & (RING - 1), 0, false, [&](int n) { if (n == 3) BARRIER(); });
        step(X0, W0, X1, W1, xb, (xb + 2 * UNIT) & (RING - 1), 1, false, [&](int) {});
      }
      step(X1, W1, X0, W0, 5 * UNIT, 6 * UNIT, 0, false, [&](int n) { if (n == 3) BARRIER(); });
      u32 st[4]; u32x2 cs[4]; u32x4 XM[4];
      float ka[4], kb[4], kr[4];
      step(X0, W0, X1, W1, 5 * UNIT, 7 * UNIT, 1, false, [&](int n) {
        if (n == 0) { LAS char* a = sm + L_ST + 8 * (64 * tg + (lane & 15)); st[0] = *(LAS u32*)a; st[1] = *(LAS u32*)(a + 128); st[2] = *(LAS u32*)(a + 256); st[3] = *(LAS u32*)(a + 384); }
        if (n >= 27 && n < 31) { LAS char* a = sm + L_CS + 1024 * g4 + 8 * (64 * fq + (lane & 15)) + 128 * (n - 27); cs[n - 27] = (u32x2){*(LAS u32*)a, *(LAS u32*)(a + 4096)}; }
        if (n >= 8) {
          const int t = (n - 8) / 6, r = (n - 8) % 6;
          if (r == 0) { kr[t] = bfhi(st[t]); ka[t] = __builtin_amdgcn_rcpf(kr[t]); PIN(kr[t]); PIN(ka[t]); }
          if (r == 1) { const u32 mm = ((st[t] & 0xffffu) ^ 0x8000u) * 0x10001u; XM[t].x = mm; PIN(XM[t]); }
          if (r == 2) { kb[t] = -2.f * 0.7978845608028654f * 1.44269504088896f * ka[t]; ka[t] *= ka[t]; PIN(kb[t]); PIN(ka[t]); }
          if (r == 3) { ka[t] *= 0.044715f; kr[t] *= kr[t]; PIN(ka[t]); PIN(kr[t]); }
          if (r == 4) { ka[t] *= kb[t]; XM[t].w = XM[t].x; PIN(ka[t]); PIN(XM[t]); }
          if (r == 5) { XM[t].y = 0; XM[t].z = 0; PIN(XM[t]); }
        }
      });
      STAMP_NW(19);
      auto last = [&](int a, int e) { const int t = (a & 4) + e; const bf16x8 wf = cat(W1[2 * t], W1[2 * t + 1]); MFMA16_I(acc[a & 3][t], wf, X1[a & 3]); SCHED(); };
      auto corr = [&](int a, int e) {
        const int t = (a & 4) + e;
        const u32x4 xm = XM[a & 3];
        const u32x2 xh = e & 1 ? (u32x2){xm.z, xm.w} : (u32x2){xm.x, xm.y};
        asm volatile("v_mfma_f32_16x16x16_bf16 %0, %1, %2, %0" : "+v"(acc[a & 3][t]) : "v"(cs[t >> 1]), "v"(xh));
        SCHED();
      };
#pragma unroll
      for (int e = 0; e < 4; ++e) last(0, e);
#pragma unroll
      for (int e = 0; e < 4; ++e) corr(0, e);
      BARRIER();
      asm volatile("s_nop 5" : "+v"(acc[0][0]), "+v"(acc[0][1]), "+v"(acc[0][2]), "+v"(acc[0][3]));
      u32 hseen;
      const u32 vh = ((2 * mi + tg) * 112 + 8 * fj + 4 * fq) * 2048 + lane * 16;
      SCHED();
#pragma unroll
      for (int a = 0; a < 8; ++a) {
        const int m = a & 3;
        f32x2 z[4], gu[4];
#pragma unroll
        for (int e = 0; e < 4; ++e) {
          const f32x4 gq = acc[m][(a & 4) + (e & 2)], uq = acc[m][(a & 4) + (e & 2) + 1];
          const f32x2 g = {gq[2 * (e & 1)], gq[2 * (e & 1) + 1]}, u = {uq[2 * (e & 1)], uq[2 * (e & 1) + 1]};
          z[e] = (g * g * ka[m] + kb[m]) * g; gu[e] = g * u;
        }
        SCHED();
        if (a == 7) { cld(0, 0); cld(0, 1); cld(0, 2); cld(0, 3); SCHED(); }
#pragma unroll
        for (int e = 0; e < 8; ++e) {
          if (a < 7 && e < 4) last(a + 1, e);
          z[e >> 1][e & 1] = __builtin_amdgcn_exp2f(z[e >> 1][e & 1]);
          SCHED();
        }
#pragma unroll
        for (int e = 0; e < 4; ++e) z[e] = z[e] * kr[m] + kr[m];
        SCHED();
        if (a == 7) { cld(0, 4); cld(0, 5); cld(0, 6); cld(0, 7); SCHED(); }
#pragma unroll
        for (int e = 0; e < 8; ++e) {
          if (a < 7 && e < 4) corr(a + 1, e);
          z[e >> 1][e & 1] = __builtin_amdgcn_rcpf(z[e >> 1][e & 1]);
          SCHED();
        }
        u32x4 hv;
#pragma unroll
        for (int e = 0; e < 4; ++e) { const f32x2 hq = gu[e] * z[e]; hv[e] = pk(hq.x, hq.y); }
        if (EXP & 32) asm volatile("" :: "v"(hv)); else st16_sc1(rws, hv, vh + 1024 * a);
#define FLAGA 6
        if (a == 1) STAMP_NW(20);
        if (a == 3) STAMP_NW(21);
        if (a == FLAGA) {
          STAMP_NW(22);
          asm volatile("s_waitcnt vmcnt(%0)" :: "i"(FLAGA - 3) : "memory");
          if (lane < 16) st8_sc1(rws, tag, lane * 8, FL_2 + ((2 * mi + tg) * 28 + 2 * fj + fq) * FS);
          STAMP_NW(18);
        }
#define GATEA 6
        if (a == GATEA) post(sm + S_GP + 4 * v, 2);
        if (a == 5) hseen = peek(hpw);
        if (a == 6) await(hpw, hseen, 2);
        SCHED();
      }
      STAMP_NW(23); STAMP_NWEND();
      STAMP_AT(3);
    }
    f32x4 y[8];
    const u32 vo = (64 * ti2 + 8 * v + (lane >> 3)) * (D * 2) + n2 * 128 + (lane & 7) * 16;
    u32x4 xres[2];
    LAS char* const pw = sm + S_GP + 4 * (v ^ 1);
    u32 pseen, vzs, zsrc; LAS char *exw, *exr; u32x4 zo[4];
    STAMP_AT(4);
#pragma unroll
    for (int a = 0; a < 4; ++a) {
      auto cm = [&](int n, int i, int j) {
        const bf16x8 af = __builtin_shufflevector(A[a & 1][2 * i], A[a & 1][2 * i + 1], 0, 1, 2, 3, 4, 5, 6, 7);
        if (a == 0 && j - i <= 1 && j >= i) asm volatile("v_mfma_f32_16x16x32_bf16 %0, %1, %2, 0" : "=&v"(y[i + j]) : "v"(af), "v"(B[a & 1][j]));
        else if ((EXP & 16) && a) asm volatile("" : "+v"(y[i + j]) : "v"(af), "v"(B[a & 1][j]));
        else asm volatile("v_mfma_f32_16x16x32_bf16 %0, %1, %2, %0" : "+v"(y[i + j]) : "v"(af), "v"(B[a & 1][j]));
        SCHED();
        if (a < 3 && n >= 4 && n < 28) cld1(a + 1, n - 4);
        if (a == 3) {
          if (n == 4) { exw = sm + (L_K + (4 * c + ah) * KCP + 16 * lane); PIN(exw); }
          if (n == 8) { exr = sm + (L_K + (4 * c + (ah ^ 1)) * KCP + 16 * lane); PIN(exr); }
          if (n == 12) { zsrc = 64 * p4 + (lane & 60); PIN(zsrc); }
          if (n == 16) { vzs = WS_ZT + ch * 8192 + 4096 * ah + 16 * lane; PIN(vzs); }
          if (n == 20) pseen = peek(pw);
          if (n >= 22 && n < 30 && !(n & 1)) { zo[(n - 22) / 2].y = tg32; zo[(n - 22) / 2].w = tg32; PIN(zo[(n - 22) / 2]); }
        }
        SCHED();
      };
      cm(0, 0, 0); cm(1, 0, 1); cm(2, 1, 1); cm(3, 1, 0); cm(4, 0, 2); cm(5, 1, 2); cm(6, 2, 2); cm(7, 2, 0); cm(8, 2, 1);
      cm(9, 0, 3); cm(10, 1, 3); cm(11, 2, 3); cm(12, 3, 3); cm(13, 3, 0); cm(14, 3, 1); cm(15, 3, 2); cm(16, 0, 4); cm(17, 1, 4);
      cm(18, 2, 4); cm(19, 3, 4); cm(20, 4, 0); cm(21, 4, 1); cm(22, 4, 2); cm(23, 4, 3); cm(24, 0, 5); cm(25, 1, 5); cm(26, 2, 5);
      cm(27, 5, 0); cm(28, 5, 1); cm(29, 5, 2); cm(30, 0, 6); cm(31, 1, 6); cm(32, 6, 0); cm(33, 6, 1); cm(34, 0, 7); cm(35, 7, 0);
      if (a == 2) post(sm + S_GP + 4 * v, 3);
#define FLAG_AT 1
      if (a == FLAG_AT) {
        STAMP_AT(5);
        WAIT_V(0);
        if (!aux && lane < 16) st8_sc1(rws, tag, lane * 8, FL_2 + FL_2B + ((2 * mi + c) * 28 + 2 * fj + ah) * FS);
        xres[0] = __builtin_amdgcn_raw_buffer_load_b128(rx, vo, 0, 0);
        xres[1] = __builtin_amdgcn_raw_buffer_load_b128(rx, vo, 32 * D * 2, 0);
        STAMP_AT(6);
      }
    }
    STAMP_AT(7);
    asm volatile("s_nop 7" : "+v"(y[0]), "+v"(y[1]), "+v"(y[2]), "+v"(y[3]), "+v"(y[4]), "+v"(y[5]), "+v"(y[6]), "+v"(y[7]));
    await<0>(pw, pseen, 3);
    auto finish = [&](int send, int keep) {
#pragma unroll
      for (int i = 0; i < 4; ++i) *(LAS f32x4*)(exw + 1024 * i) = y[send + i];
      post(sm + S_GP + 4 * v, 4);
      await<0>(pw, peek(pw), 4);
      f32x4 t[4];
#pragma unroll
      for (int i = 0; i < 4; ++i) t[i] = *(LAS f32x4*)(exr + 1024 * i);
      SCHED();
#pragma unroll
      for (int i = 0; i < 4; ++i) {
        zo[i].x = __builtin_amdgcn_ds_bpermute(zsrc, pk(vadd(t[i][0], y[keep + i][0]), vadd(t[i][1], y[keep + i][1])));
        zo[i].z = __builtin_amdgcn_ds_bpermute(zsrc, pk(vadd(t[i][2], y[keep + i][2]), vadd(t[i][3], y[keep + i][3])));
      }
    };
    if (ah) finish(0, 4); else finish(4, 0);
    STAMP_AT(8);
#pragma unroll
    for (int i = 0; i < 4; ++i) {
      if (EXP & 1) asm volatile("" :: "v"(zo[i])); else st16_sc1(rws, zo[i], vzs + 1024 * i);
      SCHED();
    }
    STAMP_AT(9);
#define ZSLEEP 8
    u32 vz = WS_ZT + (64 * n2 + 16 * v + g4) * 8192 + 256 * ti2 + (lane & 15) * 16;
    u32x4 zz[4];
    const int tok = 8 * v + (lane >> 3), dc = lane & 7;
    const u32 stg = L_K + (4 * c + 3) * KCP + 2048 * ah;
    LAS char *sw = sm + (stg + 8 * lane), *tr = sm + (stg + q4 * 128 + 32 * g4 + 8 * p4), *rb = sm + (stg - KCP + 32 * lane);
    LAS char* rz = sm + (L_K + (4 * (dc >> 2) + 2) * KCP + 2048 * ((dc >> 1) & 1) + 32 * tok + 16 * (dc & 1));
    u32 t0 = RB + UNIT + UNIT * 2 * (dc >> 2) + tok * 128 + (((2 * (dc & 3)) ^ (tok & 7) ^ (dc >> 2)) << 4), t1 = t0 ^ 16u;
    u32 op0 = vo, op1 = vo + 32 * D * 2;
    const rsrc_t ro = __builtin_amdgcn_make_buffer_rsrc((void*)p.out, 0, L * D * 2, 0x00020000);
    f32x4 s0[2], s1[2];
#pragma unroll
    for (int i = 0; i < 2; ++i) {
      s0[i] = (f32x4){bflo(xres[i][0]), bfhi(xres[i][0]), bflo(xres[i][1]), bfhi(xres[i][1])};
      s1[i] = (f32x4){bflo(xres[i][2]), bfhi(xres[i][2]), bflo(xres[i][3]), bfhi(xres[i][3])};
    }
    asm volatile("" : "+v"(vz), "+v"(sw), "+v"(tr), "+v"(rb), "+v"(rz), "+v"(t0), "+v"(t1), "+v"(op0), "+v"(op1), "+v"(s0[0]), "+v"(s0[1]), "+v"(s1[0]), "+v"(s1[1]));
    if (b == 255 && v == 0) {
#pragma unroll 1
      for (int i = 0; i < 4; ++i) wait_flags<8>(rws, FL_A + (64 * i + lane) * 8, tlo, thi);
      if (lane == 0) st8_sc1(rws, tag, FL_E, 0);
    }
#define ZDELAY 12
    __builtin_amdgcn_s_sleep(ZDELAY);
    await(sy + S_HP, peek(sy + S_HP), 3);
    STAMP_AT(10);
#pragma unroll 1
    for (u32 s = 0; s < SPIN_CAP; ++s) {
      FENCE();
#pragma unroll
      for (int i = 0; i < 4; ++i) zz[i] = (EXP & 1) ? (u32x4){vz, tg32, vz, tg32} : ld16_sc1(rws, vz, 4 * 8192 * i);
      u32 d = 0;
#pragma unroll
      for (int i = 0; i < 4; ++i) {
        *(LAS u32*)(sw + 512 * i) = zz[i].x; *(LAS u32*)(sw + 512 * i + 4) = zz[i].z;
        d |= zz[i].y ^ tg32; PIN(d); d |= zz[i].w ^ tg32; PIN(d);
      }
      if (!__any(d != 0)) break;
      __builtin_amdgcn_s_sleep(ZSLEEP);
    }
    STAMP_AT(11);
    const bf16x8 ra = trfrag(tr, tr + 512), rc = trfrag(tr + 1024, tr + 1536);
    *(LAS bf16x8*)rb = ra;
    *(LAS bf16x8*)(rb + 16) = rc;
    post(sm + S_GP + 4 * v, 5);
    await<0>(sy + S_GP, peek(sy + S_GP), 5);
#pragma unroll
    for (int i = 0; i < 2; ++i) {
      const u32x4 z = *(LAS u32x4*)(rz + 1024 * i);
      s0[i] = (f32x4){vadd(s0[i][0], bflo(z[0])), vadd(s0[i][1], bfhi(z[0])), vadd(s0[i][2], bflo(z[1])), vadd(s0[i][3], bfhi(z[1]))};
      s1[i] = (f32x4){vadd(s1[i][0], bflo(z[2])), vadd(s1[i][1], bfhi(z[2])), vadd(s1[i][2], bflo(z[3])), vadd(s1[i][3], bfhi(z[3]))};
    }
    asm volatile("" : "+v"(s0[0]), "+v"(s0[1]), "+v"(s1[0]), "+v"(s1[1]));
    STAMP_AT(12);
#pragma unroll
    for (int i = 0; i < 2; ++i) {
      SCHED(); BARRIER(); SCHED();
      if (i == 0) STAMP_AT(13);
      f32x4 pa[4], pb[4];
#pragma unroll
      for (int g = 0; g < 4; ++g) { pa[g] = *(LAS f32x4*)(sm + t0 + UNIT * i + 4096 * g); pb[g] = *(LAS f32x4*)(sm + t1 + UNIT * i + 4096 * g); }
      SCHED();
#pragma unroll
      for (int g = 0; g < 4; ++g) { s0[i] += pa[g]; s1[i] += pb[g]; }
#define OAUX 16
      __builtin_amdgcn_raw_buffer_store_b128((u32x4){pk(s0[i][0], s0[i][1]), pk(s0[i][2], s0[i][3]), pk(s1[i][0], s1[i][1]), pk(s1[i][2], s1[i][3])}, ro, i ? op1 : op0, 0, OAUX);
    }
  }
  STAMP_AT(14); STAMP_OUT();
}

#define WSOFF 0
extern "C" void kernel_launch(void* const* d_in, const int* in_sizes, int n_in, void* d_out, int out_size, void* d_ws,
                              size_t ws_size, hipStream_t stream) {
  Params p;
  p.x = (const u16*)d_in[0]; p.are = (const u16*)d_in[1]; p.aim = (const u16*)d_in[2];
  p.bre = (const u16*)d_in[3]; p.bim = (const u16*)d_in[4]; p.cre = (const u16*)d_in[5];
  p.cim = (const u16*)d_in[6]; p.logdt = (const u16*)d_in[7]; p.dskip = (const u16*)d_in[8];
  p.wg = (const u16*)d_in[10]; p.wu = (const u16*)d_in[11]; p.wd = (const u16*)d_in[12];
  p.out = (u16*)d_out; p.ws = (char*)d_ws + ((size_t)WSOFF << 20);
  s4ffn_kernel<<<256, 512, 0, stream>>>(p);
}
```

```cpp
#include <hip/hip_runtime.h>
#include <stdint.h>

typedef unsigned short u16;
typedef unsigned u32;
typedef unsigned long long u64;
typedef __bf16 bf16x2 __attribute__((ext_vector_type(2)));
typedef __bf16 bf16x4 __attribute__((ext_vector_type(4)));
typedef __bf16 bf16x8 __attribute__((ext_vector_type(8)));
typedef float f32x2 __attribute__((ext_vector_type(2)));
typedef float f32x4 __attribute__((ext_vector_type(4)));
typedef float f32x16 __attribute__((ext_vector_type(16)));
typedef u32 u32x2 __attribute__((ext_vector_type(2)));
typedef u32 u32x4 __attribute__((ext_vector_type(4)));
#define LAS __attribute__((address_space(3)))
#define GAS __attribute__((address_space(1)))
#define DI __device__ __forceinline__
typedef __amdgpu_buffer_rsrc_t rsrc_t;

constexpr int L = 2048, D = 512, NM = 128, F = 1792;
constexpr u32 WS_H = 0, WS_U = 8u << 20, WS_ZT = 10u << 20, WS_ST = 14u << 20, WS_CS = WS_ST + 65536, WS_FL = 15u << 20, WS_END = 16u << 20;
constexpr u32 FL_E = WS_FL, FL_1 = WS_FL + 4096, FL_A = WS_FL + 8192, FL_2 = WS_FL + 131072, FL_2B = 131072, FS = 128;
constexpr u32 KCP = 4160, UNIT = 16384, RB = 32768, RING = 8 * UNIT;
constexpr u32 L_K = 0, L_U = 8 * KCP, US = 4608;
constexpr u32 S_GP = 4128, S_HP = 4144;
constexpr u32 L_P = 320, PS = 3840;
constexpr u32 L_NU0 = 15872, L_NUL = L_U + 2 * US;
constexpr u32 L_ST = 0;
constexpr u32 L_CS = KCP;
constexpr u32 CODE_BYTES = 30720;
constexpr u32 SPIN_CAP = 1u << 16;

struct Params {
  const u16 *x, *are, *aim, *bre, *bim, *cre, *cim, *logdt, *dskip, *wg, *wu, *wd;
  u16* out;
  char* ws;
};

#define STAMP_DECL u64 pa_ = 0, pb_ = 0; const u64 rt0_ = __builtin_amdgcn_s_memrealtime()
#define STAMP_AT(k) do { if (k == 0) asm volatile("s_memrealtime %0\n\ts_waitcnt lgkmcnt(0)" : "=s"(pa_) :: "memory"); if (k == 2) asm volatile("s_memrealtime %0\n\ts_waitcnt lgkmcnt(0)" : "=s"(pb_) :: "memory"); } while (0)
#define STAMP_NW(k) STAMP_AT(k)
#define STAMP_NWEND()
#define STAMP_RT(v)
#define STAMP_OUT() do { if (w == 4 && b == 101) { const u64 e_ = rt0_ + 3000 + (u32)(pb_ - pa_) / 1; while (__builtin_amdgcn_s_memrealtime() < e_) __builtin_amdgcn_s_sleep(1); } } while (0)

DI float bflo(u32 v) { return __builtin_bit_cast(float, v << 16); }
DI float bfhi(u32 v) { return __builtin_bit_cast(float, v & 0xffff0000u); }
DI u32 pk(float a, float b) { f32x2 f = {a, b}; return __builtin_bit_cast(u32, __builtin_convertvector(f, bf16x2)); }
DI float rbf(float a) { return bflo(pk(a, a)); }
DI void dma4(rsrc_t ra, rsrc_t rb, u32 lds, u32 v0, u32 v1, u32 v2, u32 v3, u32 soff) {
  asm volatile("s_mov_b32 m0, %0\n\ts_nop 0\n\tbuffer_load_dwordx4 %1, %5, %7 offen lds\n\tbuffer_load_dwordx4 %2, %5, %7 offen offset:1024 lds\n\t"
               "buffer_load_dwordx4 %3, %6, %7 offen offset:2048 lds\n\tbuffer_load_dwordx4 %4, %6, %7 offen offset:3072 lds"
               :: "s"(lds), "v"(v0), "v"(v1), "v"(v2), "v"(v3), "s"(ra), "s"(rb), "s"(soff));
}
DI void dma4s(rsrc_t r, u32 lds, u32 v0, u32 v1, u32 v2, u32 v3, u32 soff) {
  asm volatile("s_mov_b32 m0, %0\n\ts_nop 0\n\tbuffer_load_dwordx4 %1, %5, %6 offen sc1 lds\n\tbuffer_load_dwordx4 %2, %5, %6 offen offset:1024 sc1 lds\n\t"
               "buffer_load_dwordx4 %3, %5, %6 offen offset:2048 sc1 lds\n\tbuffer_load_dwordx4 %4, %5, %6 offen offset:3072 sc1 lds"
               :: "s"(lds), "v"(v0), "v"(v1), "v"(v2), "v"(v3), "s"(r), "s"(soff));
}
DI void dma1s(rsrc_t r, u32 lds, u32 v0, u32 soff) {
  asm volatile("s_mov_b32 m0, %0\n\ts_nop 0\n\tbuffer_load_dwordx4 %1, %2, %3 offen sc1 lds" :: "s"(lds), "v"(v0), "s"(r), "s"(soff));
}
#define WAIT_V(n) asm volatile("s_waitcnt vmcnt(" #n ")" ::: "memory")
#define WAIT_L() asm volatile("s_waitcnt lgkmcnt(0)" ::: "memory")
#define BARRIER() asm volatile("s_waitcnt lgkmcnt(0)\n\ts_barrier" ::: "memory")
#define FENCE() asm volatile("" ::: "memory")
#define SCHED() __builtin_amdgcn_sched_barrier(0)
DI bf16x8 ldfrag(LAS char* a) { return *(LAS bf16x8*)a; }
DI bf16x4 tr4(LAS char* a) { return __builtin_amdgcn_ds_read_tr16_b64_v4bf16((LAS bf16x4*)a); }
DI bf16x8 cat(bf16x4 lo, bf16x4 hi) { return __builtin_shufflevector(lo, hi, 0, 1, 2, 3, 4, 5, 6, 7); }
DI bf16x8 trfrag(LAS char* a0, LAS char* a1) {
  bf16x4 lo = __builtin_amdgcn_ds_read_tr16_b64_v4bf16((LAS bf16x4*)a0);
  bf16x4 hi = __builtin_amdgcn_ds_read_tr16_b64_v4bf16((LAS bf16x4*)a1);
  return __builtin_shufflevector(lo, hi, 0, 1, 2, 3, 4, 5, 6, 7);
}
#define MFMA32(a, b, c) __builtin_amdgcn_mfma_f32_32x32x16_bf16(a, b, c, 0, 0, 0)
#define MFMA16_0(d, a, b) asm volatile("v_mfma_f32_16x16x32_bf16 %0, %1, %2, 0" : "=&v"(d) : "v"(a), "v"(b))
#define MFMA16_I(d, a, b) asm volatile("v_mfma_f32_16x16x32_bf16 %0, %1, %2, %0" : "+v"(d) : "v"(a), "v"(b))
#define MFMA16(a, b, c) __builtin_amdgcn_mfma_f32_16x16x32_bf16(a, b, c, 0, 0, 0)
DI u32x2 ld8_sc1(rsrc_t r, u32 voff, u32 soff) { return __builtin_amdgcn_raw_buffer_load_b64(r, voff, soff, 16); }
DI u32x4 ld16_sc1(rsrc_t r, u32 voff, u32 soff) { return __builtin_amdgcn_raw_buffer_load_b128(r, voff, soff, 16); }
DI void st8_sc1(rsrc_t r, u32x2 v, u32 voff, u32 soff) { __builtin_amdgcn_raw_buffer_store_b64(v, r, voff, soff, 16); }
DI void st16_sc1(rsrc_t r, u32x4 v, u32 voff) { __builtin_amdgcn_raw_buffer_store_b128(v, r, voff, 0, 16); }
template <int SLEEP> DI void wait_flags(rsrc_t r, u32 voff, u32 tlo, u32 thi) {
#pragma unroll 1
  for (u32 s = 0; s < SPIN_CAP; ++s) {
    FENCE();
    u32x2 f = ld8_sc1(r, voff, 0);
    if (__all(f.x == tlo && f.y == thi)) break;
    __builtin_amdgcn_s_sleep(SLEEP);
  }
  WAIT_V(0);
}
DI void post(LAS char* word, u32 v) {
  asm volatile("s_mov_b64 exec, 1\n\tds_write_b32 %0, %1\n\ts_mov_b64 exec, -1" :: "v"(word), "v"(v) : "memory");
}
DI u32 peek(LAS char* word) { return *(volatile LAS u32*)word; }
template <int SLEEP = 1> DI void await(LAS char* word, u32 seen, u32 need) {
  if (__builtin_expect(__any(seen < need), 0)) {
#pragma unroll 1
    for (u32 s = 0; s < SPIN_CAP * 16; ++s) { __builtin_amdgcn_s_sleep(SLEEP); if (!__any(peek(word) < need)) break; }
  }
  FENCE();
}
#define DPPADD(name, ctl) DI float name(float x) { float r; asm("s_nop 1\n\tv_add_f32_dpp %0, %1, %1 " ctl " row_mask:0xf bank_mask:0xf" : "=v"(r) : "v"(x)); return r; }
DPPADD(add_x1, "quad_perm:[1,0,3,2]") DPPADD(add_x2, "quad_perm:[2,3,0,1]") DPPADD(add_m8, "row_half_mirror") DPPADD(add_x8, "row_ror:8") DPPADD(add_r4, "row_ror:4")
DI float fold32(float a, float b) {
  asm("s_nop 1\n\tv_permlane32_swap_b32 %0, %1\n\tv_add_f32 %0, %0, %1" : "+v"(a), "+v"(b)); return a;
}
DI float fold16(float a, float b) {
  asm("s_nop 1\n\tv_permlane16_swap_b32 %0, %1\n\tv_add_f32 %0, %0, %1" : "+v"(a), "+v"(b)); return a;
}
DI u32 vmov(u32 a) { u32 r; asm("v_mov_b32 %0, %1" : "=v"(r) : "v"(a)); return r; }
DI float vadd(float a, float b) { float r; asm("v_add_f32 %0, %1, %2" : "=v"(r) : "v"(a), "v"(b)); return r; }
#define PIN(x) asm volatile("" : "+v"(x))
struct cpx { float r, i; };
struct cp2 { f32x2 r, i; };
DI cpx cmul(cpx a, cpx b) { return {a.r * b.r - a.i * b.i, a.r * b.i + a.i * b.r}; }
DI cpx csq(cpx a) { return {a.r * a.r - a.i * a.i, (a.r + a.r) * a.i}; }
DI cp2 cmul2(cp2 a, cpx b) { return {a.r * b.r - a.i * b.i, a.i * b.r + a.r * b.i}; }
DI cpx cexpl(float ar2, float air, float l) {
  float m = __builtin_amdgcn_exp2f(ar2 * l), f = __builtin_amdgcn_fractf(air * l);
  return {m * __builtin_amdgcn_cosf(f), m * __builtin_amdgcn_sinf(f)};
}

#define EXP 0
__global__ __launch_bounds__(512, 2) void s4ffn_kernel(Params p) {
  __shared__ __attribute__((aligned(16))) char smem[163840];
  LAS char* sm = (LAS char*)smem;
  const u32 smb = (u32)(size_t)sm;
  const int tid = threadIdx.x, lane = tid & 63;
  const int w = __builtin_amdgcn_readfirstlane(tid >> 6), v = w & 3;
  const int b = blockIdx.x, xcd = b & 7, o = b >> 3;
  if ((w >> 1) == 1) {
    u64 pc; u32 d;
    asm volatile("s_getpc_b64 %0" : "=s"(pc));
    const u32 off = (2 * o + (w & 1)) * 512;
    asm volatile(".set pf_off, 0\n\t.rept 8\n\ts_load_dword %0, %1, pf_off\n\t.set pf_off, pf_off + 64\n\t.endr\n\ts_waitcnt lgkmcnt(0)"
                 : "=&s"(d) : "s"((pc & ~255ull) + (off < CODE_BYTES ? off : 0)));
  }
  const bool aux = o >= 28;
  const int ma = xcd >> 1, fh = xcd & 1;
  const int il = o / 7, jl = o - il * 7;
  const int mi = 4 * ma + il, fj = 7 * fh + jl;
  const int ti2 = 4 * xcd + (o >> 3), n2 = o & 7;
  const int ch0 = 64 * xcd + 2 * o;
  const int hh = lane >> 5, blk = (lane >> 4) & 1, q4 = (lane >> 2) & 3, p4 = lane & 3, g4 = lane >> 4;
  STAMP_DECL; STAMP_RT(rt0_); STAMP_AT(0);
#define DESCRIPTORS() const rsrc_t rx = __builtin_amdgcn_make_buffer_rsrc((void*)p.x, 0, L * D * 2, 0x00020000), \
  rws = __builtin_amdgcn_make_buffer_rsrc((void*)p.ws, 0, WS_END, 0x00020000)
  u64 ev;
#define LOAD_EPOCH() asm volatile("s_load_dwordx2 %0, %1, 0\n\ts_waitcnt lgkmcnt(0)" : "=&s"(ev) : "s"(p.ws + FL_E) : "memory")
#define MAKE_TAG() const u32 tlo = (u32)(ev + 0x9E3779B97F4A7C15ull), thi = (u32)((ev + 0x9E3779B97F4A7C15ull) >> 32), tg32 = tlo ^ thi; const u32x2 tag = {tlo, thi}
  LAS char* const sy = sm + 4 * p4;
  if (w == 3 && lane < 2) *(LAS u32x4*)(sm + S_GP + 16 * lane) = (u32x4)(0u);

  if (__builtin_expect(aux, 0)) {
    DESCRIPTORS();
    const int ab = 8 * ma + 4 * fh + o - 28, rb = 8 * ab + w;
    u32x4 xr[8];
#pragma unroll
    for (int i = 0; i < 8; ++i) xr[i] = __builtin_amdgcn_raw_buffer_load_b128(rx, lane * 16 + 1024 * (i & 3), (8 * rb + (i & 4)) * 1024, 0);
    LOAD_EPOCH();
    MAKE_TAG();
          u32 one = 0x3f803f80u;
          asm("" : "+v"(one));
          float su[8], sq[8];
#pragma unroll
          for (int r = 0; r < 8; ++r) {
            su[r] = sq[r] = 0.f;
#pragma unroll
            for (int e = 0; e < 4; ++e) {
              const u32 d = xr[r][e];
              su[r] = __builtin_amdgcn_fdot2_f32_bf16(__builtin_bit_cast(bf16x2, d), __builtin_bit_cast(bf16x2, one), su[r], false);
              sq[r] = __builtin_amdgcn_fdot2_f32_bf16(__builtin_bit_cast(bf16x2, d), __builtin_bit_cast(bf16x2, d), sq[r], false);
            }
          }
          asm volatile("s_nop 2" : "+v"(su[0]), "+v"(su[1]), "+v"(su[2]), "+v"(su[3]), "+v"(su[4]), "+v"(su[5]), "+v"(su[6]), "+v"(su[7]),
                                   "+v"(sq[0]), "+v"(sq[1]), "+v"(sq[2]), "+v"(sq[3]), "+v"(sq[4]), "+v"(sq[5]), "+v"(sq[6]), "+v"(sq[7]));
          auto total = [&](const float (&a)[8]) {
            const float lo = add_x8(fold16(fold32(a[0], a[2]), fold32(a[1], a[3]))), hi = add_x8(fold16(fold32(a[4], a[6]), fold32(a[5], a[7])));
            return add_m8(add_x2(add_x1(lane & 8 ? hi : lo)));
          };
          const float mean = total(su) * (1.f / D), nsq = -mean * mean;
          const float var = rbf(__builtin_fmaxf(__builtin_fmaf(total(sq), 1.f / D, nsq), 0.f));
          const u32 gran = pk(mean, __builtin_amdgcn_sqrtf(rbf(var + 1e-5f)));
          const float r = __builtin_amdgcn_rcpf(bfhi(gran)), mr = -bflo(gran) * r;
          u32 n[8][4];
#pragma unroll
          for (int i = 0; i < 4; ++i) {
            const int l0 = 16 * (2 * i & 3) + 8 * (i >> 1);
            auto at = [&](float t, int l) { return __builtin_bit_cast(float, __builtin_amdgcn_readlane(__builtin_bit_cast(int, t), l)); };
            const f32x2 r2 = {at(r, l0), at(r, l0 + 16)}, m2 = {at(mr, l0), at(mr, l0 + 16)};
#pragma unroll
            for (int e = 0; e < 4; ++e) {
              const u32 a = xr[2 * i][e], c = xr[2 * i + 1][e];
              const f32x2 lo = (f32x2){bflo(a), bflo(c)} * r2 + m2, hi = (f32x2){bfhi(a), bfhi(c)} * r2 + m2;
              n[2 * e][i] = pk(lo.x, lo.y); n[2 * e + 1][i] = pk(hi.x, hi.y);
            }
          }
    if ((lane & 7) == 0) st8_sc1(rws, (u32x2){gran, tg32}, 8 * g4 + 4 * (lane & 8), WS_ST + 64 * rb);
#pragma unroll
    for (int e = 0; e < 8; ++e) *(LAS u32x4*)(sm + RB + (8 * lane + e) * 128 + ((w ^ (lane & 7)) << 4)) = (u32x4){n[e][0], n[e][1], n[e][2], n[e][3]};
    STAMP_AT(18);
    BARRIER();
#pragma unroll
    for (int e = 0; e < 8; ++e) {
      const u32 row = 64 * w + 8 * e + (lane >> 3);
      st16_sc1(rws, *(LAS u32x4*)(sm + RB + row * 128 + (((lane & 7) ^ e) << 4)), WS_U + row * 4096 + ab * 128 + (lane & 7) * 16);
    }
    WAIT_V(0);
    if (lane == 0) st8_sc1(rws, tag, FL_1 + (w * 32 + ab) * 8, 0);
    STAMP_AT(19);
  }

  if (w < 4) {
    const int k = v & 1;
    u32 vx0 = (lane >> 3) * 1024 + (((lane & 7) ^ g4) << 4), vx1 = (vx0 ^ 64) + 7168, vx2 = vx0 + 14336, vx3 = vx1 + 14336;
    u32 vg0 = g4 * (F * 2) + (((((lane & 15) >> 1) ^ g4) << 5) | ((lane & 1) << 4)), vg1 = vg0 + 4 * F * 2 - 1024;
    u32 vu0 = vg0 ^ 128, vu1 = vu0 + 4 * F * 2 - 1024;
    u32 sx0 = (mi * 128 + 64 * v) * 1024, sw0 = 16 * v * (F * 2) + fj * 256;
    asm volatile("" : "+v"(vx0), "+v"(vx1), "+v"(vx2), "+v"(vx3), "+v"(vg0), "+v"(vg1), "+v"(vu0), "+v"(vu1));
    asm volatile("" : "+s"(sx0), "+s"(sw0));
    SCHED();
    DESCRIPTORS();
    const rsrc_t rwd = __builtin_amdgcn_make_buffer_rsrc((void*)p.wd, 0, D * F * 2, 0x00020000);
#define ROT2 0
    const int rot2 = ROT2;
    const u32 dq = smb + RB + 4096 * v;
    const u32 vd0 = (lane >> 3) * (D * 2) + (((lane & 7) ^ (g4 << 1)) << 4), vd1 = vd0 + 7168, vd2 = vd0 + 14336, vd3 = vd0 + 21504;
    auto ks_of = [&](int q) { int r = rot2; if (!__builtin_constant_p(r)) asm volatile("" : "+s"(r)); int j = q % 7 + r; j = j >= 7 ? j - 7 : j; return 28 * v + 4 * j + 2 * (q / 7); };
    const u32 sd0 = 28 * v * (16 * D * 2) + n2 * 128;
    auto wdq = [&](int q, int slot) {
      u32 so = sd0;
      asm volatile("" : "+s"(so));
      dma4(rwd, rwd, dq + slot * UNIT, vd0, vd1, vd2, vd3, so + (ks_of(q) - 28 * v) * (16 * D * 2));
    };
    const u32 wo0 = RB + 4096 * v + hh * 1024 + (4 * blk + q4) * 128 + ((2 * blk + (q4 >> 1)) << 5) + p4 * 8;
    auto whalf = [&](int slot, int ct, int t) { return tr4(sm + (wo0 ^ (ct << 5)) + slot * (int)UNIT + 2048 * t); };
    bf16x8 wr_[7][4];
    auto take = [&](int q, int slot) {
#pragma unroll
      for (int e = 0; e < 4; ++e) wr_[q][e] = cat(whalf(slot, e, 0), whalf(slot, e, 1));
    };
    auto last7 = [&] {
    wdq(7, 1);
    {
      const u32 r = lane < 24 ? lane : 0, j = 4 + (r >> 3) + rot2, ks = 4 * (j >= 7 ? j - 7 : j);
      asm volatile("s_mov_b32 m0, %0\n\ts_nop 0\n\tbuffer_load_dword %1, %2, %3 offen lds" :: "s"(smb + L_NUL), "v"((16 * ks + (r & 7)) * (D * 2)), "s"(rwd), "s"((28 * v + 2) * (16 * D * 2) + (ti2 & 3) * 8192 + n2 * 128));
    }
    };
    u32x4 kc[4][4];
    if (v < 2 && !aux) {
        const rsrc_t rwg = __builtin_amdgcn_make_buffer_rsrc((void*)p.wg, 0, D * F * 2, 0x00020000);
        const rsrc_t rwu2 = __builtin_amdgcn_make_buffer_rsrc((void*)((const char*)p.wu - 2048), 0, D * F * 2 + 2048, 0x00020000);
        u32 dst = 8192 * v;
        auto ux = [&](u32 c) {
          const u32 s = sx0 + ((2 * ma + c) & 7) * 128;
          dma4(rx, rx, smb + RB + dst, vx0, vx1, vx2, vx3, s); dma4(rx, rx, smb + RB + dst + 4096, vx0, vx1, vx2, vx3, s + 32768);
          dst = (dst + UNIT) & (RING - 1);
        };
        auto uw = [&](u32 c, u32 h) {
          const u32 s = sw0 + (2 * ((2 * ma + c) & 7) + h) * (32 * F * 2);
          dma4(rwg, rwu2, smb + RB + dst, vg0, vg1, vg0, vg1, s); dma4(rwg, rwu2, smb + RB + dst + 4096, vu0, vu1, vu0, vu1, s + 8 * F * 2);
          dst = (dst + UNIT) & (RING - 1);
        };
#define LDELAY 4
      __builtin_amdgcn_s_sleep(LDELAY);
      ux(0); uw(0, 0);
      LOAD_EPOCH();
      uw(0, 1); ux(1); uw(1, 0);
#pragma unroll
      for (int c = 0; c < 7; ++c) {
        if (c) { uw(c, 1); ux(c + 1); WAIT_V(16); take(c - 1, -1); uw(c + 1, 0); WAIT_L(); SCHED(); }
        wdq(c, -1);
        WAIT_V(20);
        if (c == 0) STAMP_AT(1);
        __builtin_amdgcn_s_barrier();
      }
      uw(7, 1);
      last7();
      WAIT_V(5);
    } else {
      const bool wrk = v >= 2, csm = wrk && !aux;
      const int mat = il >> 1, r8 = (lane >> 2) & 7;
      LAS char* const cp = sm + RB + UNIT + hh * 4096 + mat * 2048 + r8 * 256 + (((4 * (il & 1) + 2 * k + (p4 >> 1)) ^ ((r8 & 3) + 4 * hh)) << 5) + (lane & 1) * 16;
      const u32 vf1 = FL_1 + (32 * xcd + (lane & 31)) * 8;
      auto stats = [&] {
        dma4s(rws, smb + L_CS + 4096 * k, lane * 16, lane * 16, lane * 16, lane * 16, WS_CS + (fj * 2 + k) * 4096);
        if (k) dma1s(rws, smb + L_ST, lane * 16, WS_ST + mi * 1024);
      };
      float s[8]; u32x4 ct[8]; u32x2 f1; u32 dsk; bool f1bad;
      auto cadd = [&](int i) {
#pragma unroll
        for (int e = 0; e < 4; ++e) {
          if (i == 0) { s[2 * e] = bflo(ct[0][e]); s[2 * e + 1] = bfhi(ct[0][e]); }
          else asm("v_add_f32 %0, %0, %2\n\tv_add_f32 %1, %1, %3" : "+v"(s[2 * e]), "+v"(s[2 * e + 1]) : "v"(bflo(ct[i][e])), "v"(bfhi(ct[i][e])));
        }
      };
#define TCH 3
#define TDELAY 4
      dsk = p.dskip[ch0 + k];
      if (TDELAY) __builtin_amdgcn_s_sleep(TDELAY);
      if ((TCH & 1) && v == 2) asm volatile("s_mov_b32 m0, %0\n\ts_nop 0\n\tbuffer_load_dword %1, %2, %3 offen lds" :: "s"(smb + L_NU0), "v"(lane < 56 ? lane * 128 : 0), "s"(rwd), "s"(b * 7168));
      if ((TCH & 2) && v == 3 && !aux) {
        const rsrc_t rw = __builtin_amdgcn_make_buffer_rsrc((void*)(mat ? p.wu : p.wg), 0, D * F * 2, 0x00020000);
        asm volatile("s_mov_b32 m0, %0\n\ts_nop 0\n\tbuffer_load_dword %1, %2, %3 offen lds" :: "s"(smb + L_NU0), "v"((lane & 31) * (F * 2)), "s"(rw), "s"((128 * ma + 96) * (F * 2) + fj * 256 + (il & 1) * 128));
      }
      if (!aux) LOAD_EPOCH();
      MAKE_TAG();
#define WDELAY 8
      __builtin_amdgcn_s_sleep(WDELAY);
      wdq(0, -1);
#pragma unroll
      for (int c = 0; c < 7; ++c) {
        if (c) {
          if ((c == 2 && v == 3) || (c == 3 && csm)) WAIT_V(1); else if (c == 6 && csm) WAIT_V(5); else WAIT_V(0);
          if (c == 1) PIN(dsk);
          take(c - 1, -1);
          WAIT_L(); SCHED();
        }
        if (!aux || c == 2) __builtin_amdgcn_s_barrier();
        FENCE(); SCHED();
#define KPRIO 1
#define STC 5
        __builtin_amdgcn_s_setprio(KPRIO);
        if (c == 6 && wrk) f1bad = !__all(f1.x == tlo && f1.y == thi);
        if (c) wdq(c, -1);
        if (c == 6) last7();
        if (c == 1 && v == 3 && lane == 0) st8_sc1(rws, tag, FL_A + b * 8, 0);
        if (c == STC && csm) stats();
        if (c == 5 && wrk) f1 = ld8_sc1(rws, vf1, 0);
        if (c == 6 && wrk) {
          if (f1bad) wait_flags<8>(rws, vf1, tlo, thi);
          FENCE();
          dma4s(rws, smb + L_U + US * k + 512, lane * 16, lane * 16, lane * 16, lane * 16, WS_U + (ch0 + k) * 4096);
        }
        __builtin_amdgcn_s_setprio(0);
        SCHED();
        if (c < 2 && csm) {
#pragma unroll
          for (int i = 0; i < 4; ++i) ct[4 * c + i] = *(LAS u32x4*)(cp + 3 * UNIT * c + 8192 * i);
#pragma unroll
          for (int i = 0; i < 4; ++i) cadd(4 * c + i);
          asm volatile("" : "+v"(s[0]), "+v"(s[1]), "+v"(s[2]), "+v"(s[3]), "+v"(s[4]), "+v"(s[5]), "+v"(s[6]), "+v"(s[7]));
        }
        if (c == 3 && wrk) {
        float kv[16];
        LAS char* pa = sm + (k ? L_P + 2 * PS : 0), *pb = sm + L_P + (2 * k + 1) * PS;
#pragma unroll
        for (int g = 0; g < 3; ++g) {
          const f32x4 a = *(LAS f32x4*)(pa + 1024 * g + 16 * lane), d = *(LAS f32x4*)(pb + 1024 * g + 16 * lane);
#pragma unroll
          for (int e = 0; e < 4; ++e) asm("v_add_f32 %0, %1, %2" : "=v"(kv[4 * g + e]) : "v"(a[e]), "v"(d[e]));
          asm("v_add_f32 %0, %1, %2" : "=v"(kv[12 + g]) : "v"(*(LAS float*)(pa + 3072 + 256 * g + 4 * lane)), "v"(*(LAS float*)(pb + 3072 + 256 * g + 4 * lane)));
        }
        const bool odd = lane & 1;
        if (lane == 0) kv[0] += bflo(dsk);
        kc[0][0] = (u32x4){pk(kv[0], kv[1]), pk(kv[2], kv[3]), pk(kv[4], kv[5]), pk(kv[6], kv[7])};
        kc[1][0] = (u32x4){pk(kv[8], kv[8]), pk(kv[8], kv[9]), pk(kv[9], kv[9]), pk(kv[10], kv[10])};
        kc[2][0] = (u32x4){pk(kv[11], kv[11]), pk(kv[11], kv[11]), pk(odd ? kv[12] : kv[11], kv[12]), pk(kv[12], kv[12])};
        kc[3][0] = (u32x4){pk(kv[13], kv[13]), pk(kv[13], kv[13]), pk(odd ? kv[14] : kv[13], kv[14]), pk(kv[14], kv[14])};
          asm volatile("" : "+v"(kc[0][0]), "+v"(kc[1][0]), "+v"(kc[2][0]), "+v"(kc[3][0]));
        }
        if (c == 4 && wrk) {
#pragma unroll
        for (int g = 0; g < 4; ++g) {
          const u32x4 cu = kc[g][0];
          int px = 0, py = 0;
          if (g) {
            px = __builtin_amdgcn_update_dpp(0, (int)kc[g ? g - 1 : 0][0].z, 0x13C, 0xf, 0xf, false);
            py = __builtin_amdgcn_update_dpp(0, (int)kc[g ? g - 1 : 0][0].w, 0x13C, 0xf, 0xf, false);
          }
          px = __builtin_amdgcn_update_dpp(px, (int)cu.z, 0x138, 0xf, 0xf, false);
          py = __builtin_amdgcn_update_dpp(py, (int)cu.w, 0x138, 0xf, 0xf, false);
          const u32 s0 = __builtin_amdgcn_alignbit(py, px, 16), s1 = __builtin_amdgcn_alignbit(cu.x, py, 16), s2 = __builtin_amdgcn_alignbit(cu.y, cu.x, 16),
                    s3 = __builtin_amdgcn_alignbit(cu.z, cu.y, 16), s4 = __builtin_amdgcn_alignbit(cu.w, cu.z, 16);
          kc[g][1] = (u32x4){s1, s2, s3, s4}; kc[g][2] = (u32x4){(u32)py, vmov(cu.x), vmov(cu.y), vmov(cu.z)}; kc[g][3] = (u32x4){s0, vmov(s1), vmov(s2), vmov(s3)};
          asm volatile("" : "+v"(kc[g][0]), "+v"(kc[g][1]), "+v"(kc[g][2]), "+v"(kc[g][3]));
        }
          STAMP_AT(2);
        }
        if (c == 2 && csm) {
          const float lo = add_x8(add_r4(fold16(fold32(s[0], s[4]), fold32(s[2], s[6])))), hi = add_x8(add_r4(fold16(fold32(s[1], s[5]), fold32(s[3], s[7]))));
          s[0] = lane & 8 ? hi : lo;
          const float th = rbf(s[0]);
          if (!(lane & 4)) st8_sc1(rws, (u32x2){pk(th, s[0] - th), tg32}, 8 * (8 * p4 + 4 * hh + 2 * blk + ((lane >> 3) & 1)), WS_CS + (((fj * 2 + mat) * 4 + ma) * 128 + 64 * (il & 1) + 32 * k) * 8);
        }
        if (c == STC + 1 && wrk && !aux) {
#pragma unroll 1
        for (u32 s = 0; s < SPIN_CAP; ++s) {
          u32 d = 0;
#pragma unroll
          for (int i = 0; i < 5; ++i) {
            const u32x4 t = *(LAS u32x4*)(sm + (i < 4 || !k ? L_CS + 4096 * k + 1024 * (i & 3) : L_ST) + 16 * lane);
            d |= (t.y ^ tg32) | (t.w ^ tg32);
          }
          if (!__any(d != 0)) break;
          stats();
          WAIT_V(0);
        }
        }
      }
    }
    STAMP_AT(4);
    MAKE_TAG();
    STAMP_AT(6);
    BARRIER();
    wdq(8, 2); wdq(9, 3); wdq(10, 4);
    WAIT_V(17); take(6, -1);
    LAS char* kd = sm + L_K + 4 * KCP * k + 16 * lane;
    LAS char* kz = sm + (lane < 32 ? L_U + US * k + 16 * lane : L_K + (4 * k + ((lane >> 1) & 3)) * KCP + 16 * (lane & 1));
    asm volatile("" : "+v"(kd), "+v"(kz));
    u32x4 zero = (u32x4)(0u);
    u32 two = 2;
    LAS char* hpw = sm + S_HP + 4 * v;
    asm volatile("" : "+v"(zero), "+v"(two), "+v"(hpw));
    STAMP_AT(5);
    SCHED();
    BARRIER();
    STAMP_AT(7);
    if (v >= 2) {
      WAIT_V(0);
      *(LAS u32x4*)kz = zero;
#pragma unroll
      for (int e = 0; e < 16; ++e) *(LAS u32x4*)(kd + 32 + KCP * (e & 3) + 1024 * (e >> 2)) = kc[e >> 2][e & 3];
      post(hpw, two);
      WAIT_L();
    } else __builtin_amdgcn_s_sleep(8);
    STAMP_AT(8);
#define LATEQ 1
#define WPRIO 2
    if (!LATEQ) { wdq(11, 5); wdq(12, 6); wdq(13, 7); }
    __builtin_amdgcn_s_setprio(WPRIO);
#define HDELAY 0
#define AUXSLEEP 32
    if (!aux) { await<4>(sy + S_GP, peek(sy + S_GP), 2); __builtin_amdgcn_s_sleep(HDELAY); }
    else __builtin_amdgcn_s_sleep(AUXSLEEP);
    if (LATEQ == 1) { __builtin_amdgcn_s_setprio(0); wdq(11, 5); wdq(12, 6); wdq(13, 7); }
    STAMP_AT(9);
    const u32 vf2 = FL_2 + (ti2 * 28 + 7 * v + (lane < 7 ? lane : 0)) * FS;
#define APOLL 1
    if (APOLL) {
#pragma unroll 1
      for (u32 s = 0; s < SPIN_CAP; ++s) {
        u32x2 f = tag;
        if (lane < 7) { f = (u32x2)(0u); asm volatile("buffer_atomic_or_x2 %0, %1, %2, 0 offen sc0 sc1\n\ts_waitcnt vmcnt(0)" : "+v"(f) : "v"(vf2), "s"(rws) : "memory"); }
        if ((EXP & 4) || __all(f.x == tlo && f.y == thi)) break;
        __builtin_amdgcn_s_sleep(2);
      }
      FENCE();
    } else wait_flags<8>(rws, vf2, tlo, thi);
    __builtin_amdgcn_s_setprio(0);
    STAMP_AT(10);
    f32x4 acc[4][4];
#define PD 12
#define ZAT 9
    u32x4 hf[56];
    u32 hs;
    auto hload = [&](int n) { if (!(n & 3)) hs = (ti2 * 112 + ks_of(n >> 2)) * 2048; hf[n] = ld16_sc1(rws, lane * 16 + 1024 * (n & 3), hs); };
#pragma unroll
    for (int n = 0; n < PD; ++n) hload(n);
    bf16x4 wl[2][8];
    u32x2 fb; u32 pw_[4], wb_[4];
    SCHED();
#pragma unroll
    for (int i = 0; i < 14; ++i) {
#pragma unroll
      for (int n = 0; n < 16; ++n) {
        const int m = n >> 2, ct = m & 1 ? 3 - (n & 3) : n & 3, nx = 4 * i + m + PD;
        const bf16x8 a = i < 7 ? wr_[i < 7 ? i : 0][ct] : cat(wl[i & 1][2 * ct], wl[i & 1][2 * ct + 1]);
        const bf16x8 bb = __builtin_bit_cast(bf16x8, hf[4 * i + m]);
        if (i == 0) MFMA16_0(acc[ct][m], a, bb); else MFMA16_I(acc[ct][m], a, bb);
        SCHED();
#define LG 2
        if ((n & 3) == LG && nx < 56) hload(nx);
        if ((n & 3) != LG && n < 11 && i >= 6 && i < 13) {
          const int e = n - (n + 3 - LG) / 4;
          if (e < 8) wl[(i + 1) & 1][e] = tr4(sm + wb_[e >> 1] + (i < 9 ? i - 5 : i - 9) * (int)UNIT + 2048 * (e & 1));
        }
        if ((i == 5 || i == 8) && n >= 12) { wb_[n & 3] = (wo0 ^ ((n & 3) << 5)) + (i == 8 ? 4 * UNIT : 0); PIN(wb_[n & 3]); }
        if ((n & 3) == (LG + 3) % 4) {
          if (nx == 20) fb = ld8_sc1(rws, vf2, FL_2B);
          if (nx == 28) {
            if (__builtin_expect(!__all(fb.x == tlo && fb.y == thi), 0)) wait_flags<4>(rws, vf2 + FL_2B, tlo, thi);
            FENCE();
          }
          if (i == ZAT && m == 3) post(sm + S_HP + 4 * v, 3);
          if (i == 13) { pw_[m] = (RB + UNIT + 4096 * v + (lane & 15) * 128 + ((g4 ^ (lane & 7)) << 4)) ^ ((m & 1) << 6) ^ ((m >> 1) << 4); PIN(pw_[m]); }
        }
        SCHED();
      }
      if (i == 3) STAMP_AT(15);
      if (i == 6) STAMP_AT(16);
      if (i == 10) STAMP_AT(17);
    }
    STAMP_AT(11);
#define BAT 8
    asm volatile("s_nop 7" : "+v"(acc[0][3]), "+v"(acc[1][3]), "+v"(acc[2][3]), "+v"(acc[3][3]));
#pragma unroll
    for (int a = 0; a < 16; ++a) {
      const int ct = a & 3, m = a >> 2;
      if (a == BAT) { SCHED(); __builtin_amdgcn_s_barrier(); SCHED(); }
      *(LAS f32x4*)(sm + pw_[ct] + UNIT * (2 * (ct >> 1) + (m >> 1)) + 2048 * (m & 1)) = acc[ct][m];
    }
    STAMP_AT(12);
    BARRIER();
  } else {
    __builtin_amdgcn_s_setprio(1);
    const int c = v >> 1, ah = v & 1, ch = ch0 + c;
    u32 gi = (ch * NM + 64 * ah + lane) * 2, gd = ch * 2;
    asm volatile("" : "+v"(gi), "+v"(gd));
    SCHED();
    auto at = [](const u16* q, u32 off) { return (u32)*(const u16*)((const char*)q + off); };
    const u32 r6 = at(p.logdt, gd), r0 = at(p.are, gi), r1 = at(p.aim, gi), r2 = at(p.bre, gi), r3 = at(p.bim, gi), r4 = at(p.cre, gi), r5 = at(p.cim, gi);
    SCHED();
    STAMP_AT(2);
    DESCRIPTORS();
    const int tg = c, fq = ah;
    u32 xa[2], wa[4], iw[4], ir[2];
    {
      const u32 row = 64 * tg + (lane & 15), f = (row >> 1) & 7, scr = RB + 5 * UNIT + 4096 * v, cki = 2 * blk + (p4 >> 1);
#pragma unroll
      for (int s = 0; s < 4; ++s) {
        if (s < 2) xa[s] = RB + row * 128 + (((4 * s + g4) ^ f) << 4);
        wa[s] = RB + g4 * 4096 + q4 * 256 + (((4 * fq + s) ^ (q4 + 4 * blk)) << 5) + p4 * 8;
        iw[s] = scr + lane * 64 + ((s ^ ((lane >> 1) & 3)) << 4);
        asm volatile("" : "+v"(xa[s & 1]), "+v"(wa[s]), "+v"(iw[s]));
      }
      ir[0] = scr + (8 * hh + q4) * 64 + ((cki ^ (q4 >> 1)) << 4) + 8 * (p4 & 1);
      ir[1] = scr + (8 * hh + 4 + q4) * 64 + ((cki ^ (2 + (q4 >> 1))) << 4) + 8 * (p4 & 1);
      asm volatile("" : "+v"(ir[0]), "+v"(ir[1]));
    }
    LAS char* ka = sm + L_K + (4 * c + q4) * KCP + 2 * (28 + 128 * ah + 4 * p4 - 8 * g4);
    LAS char* ua = sm + L_U + US * c + 2 * (240 - 96 - 128 * ah + 16 * (lane & 15) + 8 * g4);
    LAS char *hpw = sm + S_HP + 8 + 4 * (lane & 1), *kp4 = sm + ((v ? L_P + PS * v : 0) + 16 * lane), *kp1 = sm + ((v ? L_P + PS * v : 0) + 3072 + 4 * lane);
    asm volatile("" : "+v"(ka), "+v"(ua), "+v"(hpw), "+v"(kp4), "+v"(kp1));
    u64 ev2;
    asm volatile("s_load_dwordx2 %0, %1, 0" : "=&s"(ev2) : "s"(p.ws + FL_E) : "memory");
    SCHED();
    STAMP_AT(15);
    STAMP_AT(16);
    bf16x8 yf[8], xf[8];
    {
      const float dt = rbf(__builtin_amdgcn_exp2f(bflo(r6) * 1.44269504088896f));
      const float ar2 = dt * bflo(r0) * 1.44269504088896f, air = dt * bflo(r1) * 0.159154943091895f;
      auto put = [&](int img, int g, u32x4 re, u32x4 im) {
        if (EXP & 8) return;
        *(LAS u32x4*)(sm + iw[g] + 2 * img * UNIT) = re; *(LAS u32x4*)(sm + iw[g] + (1 - img) * UNIT) = im;
        SCHED();
      };
      auto get = [&](int img, int s) { const u32 d = (s < 4 ? 2 * img : 1 - img) * UNIT + 1024 * (s & 3); return trfrag(sm + ir[0] + d, sm + ir[1] + d); };
#define P2_(a) pk((a).x, (a).y)
#define P4_(a, b, c, d) (u32x4){P2_(a), P2_(b), P2_(c), P2_(d)}
      const cpx e8 = cexpl(ar2, air, 8.f);
      {
        const cpx s1 = {e8.r, -e8.i}, s2 = csq(s1), s4 = csq(s2), s8 = cexpl(ar2, -air, 64.f), s16 = csq(s8);
        const cp2 y0 = {{1.f, s1.r}, {0.f, s1.i}}, y1 = cmul2(y0, s2), y2 = cmul2(y0, s4), y3 = cmul2(y1, s4);
        SCHED();
        asm volatile("s_waitcnt lgkmcnt(0)" : "+s"(ev2) :: "memory");
        if (!aux) ev = ev2;
        put(0, 0, P4_(y0.r, y1.r, y2.r, y3.r), P4_(y0.i, y1.i, y2.i, y3.i));
        const cp2 y4 = cmul2(y0, s8), y5 = cmul2(y1, s8), y6 = cmul2(y2, s8), y7 = cmul2(y3, s8);
        put(0, 1, P4_(y4.r, y5.r, y6.r, y7.r), P4_(y4.i, y5.i, y6.i, y7.i));
        const cp2 y8 = cmul2(y0, s16), y9 = cmul2(y1, s16), ya = cmul2(y2, s16), yb = cmul2(y3, s16);
        put(0, 2, P4_(y8.r, y9.r, ya.r, yb.r), P4_(y8.i, y9.i, ya.i, yb.i));
        const cp2 yc = cmul2(y4, s16), yd = cmul2(y5, s16), ye = cmul2(y6, s16), yg = cmul2(y7, s16);
        put(0, 3, P4_(yc.r, yd.r, ye.r, yg.r), P4_(yc.i, yd.i, ye.i, yg.i));
      }
      {
        const cpx cf = cmul({bflo(r4), bflo(r5)}, {dt * bflo(r2), dt * bflo(r3)});
        const cpx e1 = cexpl(ar2, air, 1.f), e256 = cexpl(ar2, air, 256.f);
        const cpx e2 = csq(e1), e4 = csq(e2), e512 = csq(e256), e1024 = csq(e512);
        const cp2 cb = cmul2({{e1.r, e256.r}, {e1.i, e256.i}}, cf);
        const cp2 a0 = {{cf.r, cb.r.x}, {cf.i, cb.i.x}}, a1 = cmul2(a0, e2);
        const cp2 t0 = cmul2({{e2.r, e4.r}, {e2.i, e4.i}}, {cb.r.y, cb.i.y});
#define XA(m) (u32x4){P2_(a0.m), P2_(a1.m), pk(cb.m.y, cb.m.y), P2_(t0.m)}
#pragma unroll
        for (int s = 0; s < 4; ++s) yf[s] = get(0, s);
        put(1, 0, XA(r), XA(i));
        const cp2 a2 = cmul2(a0, e4), a3 = cmul2(a1, e4), t1 = cmul2(t0, e4);
#define XB(m) (u32x4){P2_(a2.m), P2_(a3.m), pk(t0.m.y, t0.m.y), P2_(t1.m)}
        put(1, 1, XB(r), XB(i));
#pragma unroll
        for (int s = 4; s < 8; ++s) yf[s] = get(0, s);
        const cp2 q0 = {{cf.r, cb.r.y}, {cf.i, cb.i.y}}, q1 = cmul2(q0, e512), q2 = cmul2(q0, e1024);
        const cp2 u4 = cmul2(q1, e4), u8 = cmul2(q1, e8);
#define XC(m) (u32x4){pk(q1.m.x, u4.m.x), pk(u8.m.x, q2.m.x), pk(q1.m.y, u4.m.y), pk(u8.m.y, q2.m.y)}
        put(1, 2, XC(r), XC(i));
        const cp2 q3 = cmul2(q1, e1024), v8 = cmul2(q2, e8), w8 = cmul2(q3, e8);
#define XD(m) (u32x4){pk(v8.m.x, q3.m.x), pk(w8.m.x, 0.f), pk(v8.m.y, q3.m.y), pk(w8.m.y, 0.f)}
        put(1, 3, XD(r), XD(i));
#pragma unroll
        for (int s = 0; s < 8; ++s) xf[s] = get(1, s);
      }

      STAMP_AT(17);
    }
    f32x16 kacc = (f32x16)(0.f);
#pragma unroll
    for (int s = 0; s < 8; ++s) kacc = MFMA32(xf[s], yf[s], kacc);
    auto kstore = [&](int g) {
      if (g < 3) *(LAS f32x4*)(kp4 + 1024 * g) = (f32x4){kacc[4 * g], kacc[4 * g + 1], kacc[4 * g + 2], kacc[4 * g + 3]};
      else { *(LAS float*)kp1 = kacc[12]; *(LAS float*)(kp1 + 256) = kacc[13]; *(LAS float*)(kp1 + 512) = kacc[14]; }
    };
    kstore(0); kstore(1); kstore(2); kstore(3);
    STAMP_AT(1);
    bf16x8 X0[4]; bf16x4 W0[16];
    BARRIER();
    MAKE_TAG();
    W0[0] = tr4(sm + UNIT + wa[0]); W0[1] = tr4(sm + UNIT + wa[0] + 1024);
#pragma unroll
    for (int t = 0; t < 4; ++t) X0[t] = ldfrag(sm + xa[0] + 2048 * t);
#pragma unroll
    for (int t = 2; t < 16; ++t) W0[t] = tr4(sm + UNIT + wa[t >> 2] + 1024 * (t & 3));
    SCHED();
    bf16x4 A[2][16]; bf16x8 B[2][8];
    auto cld1 = [&](int a, int n) {
      const int i = n / 3, r = n % 3;
      if (r < 2) A[a & 1][2 * i + r] = __builtin_amdgcn_ds_read_tr16_b64_v4bf16((LAS bf16x4*)(ka + 64 * a + 512 * i + 8 - 8 * r));
      else B[a & 1][i] = ldfrag(ua + 64 * (3 - a) + 512 * i);
    };
    auto cld = [&](int a, int i) { cld1(a, 3 * i); cld1(a, 3 * i + 1); cld1(a, 3 * i + 2); };
    if (aux) { BARRIER(); BARRIER();
      await(hpw, 0, 2);
#pragma unroll
      for (int i = 0; i < 8; ++i) cld(0, i);
    }
    else {
      f32x4 acc[4][8];
      bf16x8 X1[4]; bf16x4 W1[16];
      auto step = [&](const bf16x8 (&Xc)[4], const bf16x4 (&Wc)[16], bf16x8 (&Xn)[4], bf16x4 (&Wn)[16], u32 xb, u32 wb, int s, bool first, auto mid) {
        u32 xp, wp;
#pragma unroll
        for (int n = 0; n < 32; ++n) {
          const int t = n >> 2, r = n & 3, m = t & 1 ? 3 - r : r;
          const bf16x8 wf = cat(Wc[2 * t], Wc[2 * t + 1]);
          if (first) MFMA16_0(acc[m][t], wf, Xc[m]); else MFMA16_I(acc[m][t], wf, Xc[m]);
          SCHED();
          mid(n);
          if (n == 1) { xp = xb + xa[s]; PIN(xp); }
          if (n == 2 || n == 8 || n == 15 || n == 22) { wp = wb + wa[n / 7]; PIN(wp); }
          if (n >= 4 && n < 8) Wn[n - 4] = tr4(sm + wp + 1024 * (n - 4));
          if (n >= 9 && n < 13) Wn[n - 5] = tr4(sm + wp + 1024 * (n - 9));
          if (n == 13 || n == 14) Xn[n - 13] = ldfrag(sm + xp + 2048 * (n - 13));
          if (n >= 16 && n < 20) Wn[n - 8] = tr4(sm + wp + 1024 * (n - 16));
          if (n == 20 || n == 21) Xn[n - 18] = ldfrag(sm + xp + 2048 * (n - 18));
          if (n >= 23 && n < 27) Wn[n - 11] = tr4(sm + wp + 1024 * (n - 23));
          SCHED();
        }
      };
      u32 xb = 0;
      step(X0, W0, X1, W1, xb, 2 * UNIT, 1, true, [&](int) {});
#pragma unroll 1
      for (int cc = 0; cc < 6; ++cc) {
        xb = (xb + 3 * UNIT) & (RING - 1);
        step(X1, W1, X0, W0, xb, (xb + UNIT) & (RING - 1), 0, false, [&](int n) { if (n == 3) BARRIER(); });
        step(X0, W0, X1, W1, xb, (xb + 2 * UNIT) & (RING - 1), 1, false, [&](int) {});
      }
      step(X1, W1, X0, W0, 5 * UNIT, 6 * UNIT, 0, false, [&](int n) { if (n == 3) BARRIER(); });
      u32 st[4]; u32x2 cs[4]; u32x4 XM[4];
      float ka[4], kb[4], kr[4];
      step(X0, W0, X1, W1, 5 * UNIT, 7 * UNIT, 1, false, [&](int n) {
        if (n == 0) { LAS char* a = sm + L_ST + 8 * (64 * tg + (lane & 15)); st[0] = *(LAS u32*)a; st[1] = *(LAS u32*)(a + 128); st[2] = *(LAS u32*)(a + 256); st[3] = *(LAS u32*)(a + 384); }
        if (n >= 27 && n < 31) { LAS char* a = sm + L_CS + 1024 * g4 + 8 * (64 * fq + (lane & 15)) + 128 * (n - 27); cs[n - 27] = (u32x2){*(LAS u32*)a, *(LAS u32*)(a + 4096)}; }
        if (n >= 8) {
          const int t = (n - 8) / 6, r = (n - 8) % 6;
          if (r == 0) { kr[t] = bfhi(st[t]); ka[t] = __builtin_amdgcn_rcpf(kr[t]); PIN(kr[t]); PIN(ka[t]); }
          if (r == 1) { const u32 mm = ((st[t] & 0xffffu) ^ 0x8000u) * 0x10001u; XM[t].x = mm; PIN(XM[t]); }
          if (r == 2) { kb[t] = -2.f * 0.7978845608028654f * 1.44269504088896f * ka[t]; ka[t] *= ka[t]; PIN(kb[t]); PIN(ka[t]); }
          if (r == 3) { ka[t] *= 0.044715f; kr[t] *= kr[t]; PIN(ka[t]); PIN(kr[t]); }
          if (r == 4) { ka[t] *= kb[t]; XM[t].w = XM[t].x; PIN(ka[t]); PIN(XM[t]); }
          if (r == 5) { XM[t].y = 0; XM[t].z = 0; PIN(XM[t]); }
        }
      });
      STAMP_NW(19);
      auto last = [&](int a, int e) { const int t = (a & 4) + e; const bf16x8 wf = cat(W1[2 * t], W1[2 * t + 1]); MFMA16_I(acc[a & 3][t], wf, X1[a & 3]); SCHED(); };
      auto corr = [&](int a, int e) {
        const int t = (a & 4) + e;
        const u32x4 xm = XM[a & 3];
        const u32x2 xh = e & 1 ? (u32x2){xm.z, xm.w} : (u32x2){xm.x, xm.y};
        asm volatile("v_mfma_f32_16x16x16_bf16 %0, %1, %2, %0" : "+v"(acc[a & 3][t]) : "v"(cs[t >> 1]), "v"(xh));
        SCHED();
      };
#pragma unroll
      for (int e = 0; e < 4; ++e) last(0, e);
#pragma unroll
      for (int e = 0; e < 4; ++e) corr(0, e);
      asm volatile("s_nop 7" : "+v"(acc[0][0]), "+v"(acc[0][1]), "+v"(acc[0][2]), "+v"(acc[0][3]));
      u32 hseen;
      const u32 vh = ((2 * mi + tg) * 112 + 8 * fj + 4 * fq) * 2048 + lane * 16;
      SCHED();
#pragma unroll
      for (int a = 0; a < 8; ++a) {
        const int m = a & 3;
        f32x2 z[4], gu[4];
#pragma unroll
        for (int e = 0; e < 4; ++e) {
          const f32x4 gq = acc[m][(a & 4) + (e & 2)], uq = acc[m][(a & 4) + (e & 2) + 1];
          const f32x2 g = {gq[2 * (e & 1)], gq[2 * (e & 1) + 1]}, u = {uq[2 * (e & 1)], uq[2 * (e & 1) + 1]};
          z[e] = (g * g * ka[m] + kb[m]) * g; gu[e] = g * u;
        }
        SCHED();
        if (a == 7) { cld(0, 0); cld(0, 1); cld(0, 2); cld(0, 3); SCHED(); }
#pragma unroll
        for (int e = 0; e < 8; ++e) {
          if (a < 7 && e < 4) last(a + 1, e);
          z[e >> 1][e & 1] = __builtin_amdgcn_exp2f(z[e >> 1][e & 1]);
          SCHED();
        }
#pragma unroll
        for (int e = 0; e < 4; ++e) z[e] = z[e] * kr[m] + kr[m];
        SCHED();
        if (a == 7) { cld(0, 4); cld(0, 5); cld(0, 6); cld(0, 7); SCHED(); }
#pragma unroll
        for (int e = 0; e < 8; ++e) {
          if (a < 7 && e < 4) corr(a + 1, e);
          z[e >> 1][e & 1] = __builtin_amdgcn_rcpf(z[e >> 1][e & 1]);
          SCHED();
        }
        u32x4 hv;
#pragma unroll
        for (int e = 0; e < 4; ++e) { const f32x2 hq = gu[e] * z[e]; hv[e] = pk(hq.x, hq.y); }
        if (EXP & 32) asm volatile("" :: "v"(hv)); else st16_sc1(rws, hv, vh + 1024 * a);
#define FLAGA 6
#define B8AT 1
        if (a == B8AT) BARRIER();
        if (a == 1) STAMP_NW(20);
        if (a == 3) STAMP_NW(21);
        if (a == FLAGA) {
          STAMP_NW(22);
          asm volatile("s_waitcnt vmcnt(%0)" :: "i"(FLAGA - 3) : "memory");
          if (lane < 16) st8_sc1(rws, tag, lane * 8, FL_2 + ((2 * mi + tg) * 28 + 2 * fj + fq) * FS);
          STAMP_NW(18);
        }
#define GATEA 6
        if (a == GATEA) post(sm + S_GP + 4 * v, 2);
        if (a == 5) hseen = peek(hpw);
        if (a == 6) await(hpw, hseen, 2);
        SCHED();
      }
      STAMP_NW(23); STAMP_NWEND();
      STAMP_AT(3);
    }
    f32x4 y[8];
    const u32 vo = (64 * ti2 + 8 * v + (lane >> 3)) * (D * 2) + n2 * 128 + (lane & 7) * 16;
    u32x4 xres[2];
    LAS char* const pw = sm + S_GP + 4 * (v ^ 1);
    u32 pseen, vzs, zsrc; LAS char *exw, *exr; u32x4 zo[4];
    STAMP_AT(4);
#pragma unroll
    for (int a = 0; a < 4; ++a) {
      auto cm = [&](int n, int i, int j) {
        const bf16x8 af = __builtin_shufflevector(A[a & 1][2 * i], A[a & 1][2 * i + 1], 0, 1, 2, 3, 4, 5, 6, 7);
        if (a == 0 && j - i <= 1 && j >= i) asm volatile("v_mfma_f32_16x16x32_bf16 %0, %1, %2, 0" : "=&v"(y[i + j]) : "v"(af), "v"(B[a & 1][j]));
        else if ((EXP & 16) && a) asm volatile("" : "+v"(y[i + j]) : "v"(af), "v"(B[a & 1][j]));
        else asm volatile("v_mfma_f32_16x16x32_bf16 %0, %1, %2, %0" : "+v"(y[i + j]) : "v"(af), "v"(B[a & 1][j]));
        SCHED();
        if (a < 3 && n >= 4 && n < 28) cld1(a + 1, n - 4);
        if (a == 3) {
          if (n == 4) { exw = sm + (L_K + (4 * c + ah) * KCP + 16 * lane); PIN(exw); }
          if (n == 8) { exr = sm + (L_K + (4 * c + (ah ^ 1)) * KCP + 16 * lane); PIN(exr); }
          if (n == 12) { zsrc = 64 * p4 + (lane & 60); PIN(zsrc); }
          if (n == 16) { vzs = WS_ZT + ch * 8192 + 4096 * ah + 16 * lane; PIN(vzs); }
          if (n == 20) pseen = peek(pw);
          if (n >= 22 && n < 30 && !(n & 1)) { zo[(n - 22) / 2].y = tg32; zo[(n - 22) / 2].w = tg32; PIN(zo[(n - 22) / 2]); }
        }
        SCHED();
      };
      cm(0, 0, 0); cm(1, 0, 1); cm(2, 1, 1); cm(3, 1, 0); cm(4, 0, 2); cm(5, 1, 2); cm(6, 2, 2); cm(7, 2, 0); cm(8, 2, 1);
      cm(9, 0, 3); cm(10, 1, 3); cm(11, 2, 3); cm(12, 3, 3); cm(13, 3, 0); cm(14, 3, 1); cm(15, 3, 2); cm(16, 0, 4); cm(17, 1, 4);
      cm(18, 2, 4); cm(19, 3, 4); cm(20, 4, 0); cm(21, 4, 1); cm(22, 4, 2); cm(23, 4, 3); cm(24, 0, 5); cm(25, 1, 5); cm(26, 2, 5);
      cm(27, 5, 0); cm(28, 5, 1); cm(29, 5, 2); cm(30, 0, 6); cm(31, 1, 6); cm(32, 6, 0); cm(33, 6, 1); cm(34, 0, 7); cm(35, 7, 0);
      if (a == 2) post(sm + S_GP + 4 * v, 3);
#define FLAG_AT 1
      if (a == FLAG_AT) {
        STAMP_AT(5);
        WAIT_V(0);
        if (!aux && lane < 16) st8_sc1(rws, tag, lane * 8, FL_2 + FL_2B + ((2 * mi + c) * 28 + 2 * fj + ah) * FS);
        xres[0] = __builtin_amdgcn_raw_buffer_load_b128(rx, vo, 0, 0);
        xres[1] = __builtin_amdgcn_raw_buffer_load_b128(rx, vo, 32 * D * 2, 0);
        STAMP_AT(6);
      }
    }
    STAMP_AT(7);
    asm volatile("s_nop 7" : "+v"(y[0]), "+v"(y[1]), "+v"(y[2]), "+v"(y[3]), "+v"(y[4]), "+v"(y[5]), "+v"(y[6]), "+v"(y[7]));
    await<0>(pw, pseen, 3);
    auto finish = [&](int send, int keep) {
#pragma unroll
      for (int i = 0; i < 4; ++i) *(LAS f32x4*)(exw + 1024 * i) = y[send + i];
      post(sm + S_GP + 4 * v, 4);
      await<0>(pw, peek(pw), 4);
      f32x4 t[4];
#pragma unroll
      for (int i = 0; i < 4; ++i) t[i] = *(LAS f32x4*)(exr + 1024 * i);
      SCHED();
#pragma unroll
      for (int i = 0; i < 4; ++i) {
        zo[i].x = __builtin_amdgcn_ds_bpermute(zsrc, pk(vadd(t[i][0], y[keep + i][0]), vadd(t[i][1], y[keep + i][1])));
        zo[i].z = __builtin_amdgcn_ds_bpermute(zsrc, pk(vadd(t[i][2], y[keep + i][2]), vadd(t[i][3], y[keep + i][3])));
      }
    };
    if (ah) finish(0, 4); else finish(4, 0);
    STAMP_AT(8);
#pragma unroll
    for (int i = 0; i < 4; ++i) {
      if (EXP & 1) asm volatile("" :: "v"(zo[i])); else st16_sc1(rws, zo[i], vzs + 1024 * i);
      SCHED();
    }
    STAMP_AT(9);
#define ZSLEEP 8
    u32 vz = WS_ZT + (64 * n2 + 16 * v + g4) * 8192 + 256 * ti2 + (lane & 15) * 16;
    u32x4 zz[4];
    const int tok = 8 * v + (lane >> 3), dc = lane & 7;
    const u32 stg = L_K + (4 * c + 3) * KCP + 2048 * ah;
    LAS char *sw = sm + (stg + 8 * lane), *tr = sm + (stg + q4 * 128 + 32 * g4 + 8 * p4), *rb = sm + (stg - KCP + 32 * lane);
    LAS char* rz = sm + (L_K + (4 * (dc >> 2) + 2) * KCP + 2048 * ((dc >> 1) & 1) + 32 * tok + 16 * (dc & 1));
    u32 t0 = RB + UNIT + UNIT * 2 * (dc >> 2) + tok * 128 + (((2 * (dc & 3)) ^ (tok & 7) ^ (dc >> 2)) << 4), t1 = t0 ^ 16u;
    u32 op0 = vo, op1 = vo + 32 * D * 2;
    const rsrc_t ro = __builtin_amdgcn_make_buffer_rsrc((void*)p.out, 0, L * D * 2, 0x00020000);
    f32x4 s0[2], s1[2];
#pragma unroll
    for (int i = 0; i < 2; ++i) {
      s0[i] = (f32x4){bflo(xres[i][0]), bfhi(xres[i][0]), bflo(xres[i][1]), bfhi(xres[i][1])};
      s1[i] = (f32x4){bflo(xres[i][2]), bfhi(xres[i][2]), bflo(xres[i][3]), bfhi(xres[i][3])};
    }
    asm volatile("" : "+v"(vz), "+v"(sw), "+v"(tr), "+v"(rb), "+v"(rz), "+v"(t0), "+v"(t1), "+v"(op0), "+v"(op1), "+v"(s0[0]), "+v"(s0[1]), "+v"(s1[0]), "+v"(s1[1]));
    if (b == 255 && v == 0) {
#pragma unroll 1
      for (int i = 0; i < 4; ++i) wait_flags<8>(rws, FL_A + (64 * i + lane) * 8, tlo, thi);
      if (lane == 0) st8_sc1(rws, tag, FL_E, 0);
    }
#define ZDELAY 12
    __builtin_amdgcn_s_sleep(ZDELAY);
    await(sy + S_HP, peek(sy + S_HP), 3);
    STAMP_AT(10);
#pragma unroll 1
    for (u32 s = 0; s < SPIN_CAP; ++s) {
      FENCE();
#pragma unroll
      for (int i = 0; i < 4; ++i) zz[i] = (EXP & 1) ? (u32x4){vz, tg32, vz, tg32} : ld16_sc1(rws, vz, 4 * 8192 * i);
      u32 d = 0;
#pragma unroll
      for (int i = 0; i < 4; ++i) {
        *(LAS u32*)(sw + 512 * i) = zz[i].x; *(LAS u32*)(sw + 512 * i + 4) = zz[i].z;
        d |= zz[i].y ^ tg32; PIN(d); d |= zz[i].w ^ tg32; PIN(d);
      }
      if (!__any(d != 0)) break;
      __builtin_amdgcn_s_sleep(ZSLEEP);
    }
    STAMP_AT(11);
    const bf16x8 ra = trfrag(tr, tr + 512), rc = trfrag(tr + 1024, tr + 1536);
    *(LAS bf16x8*)rb = ra;
    *(LAS bf16x8*)(rb + 16) = rc;
    STAMP_AT(12);
    BARRIER();
#pragma unroll
    for (int i = 0; i < 2; ++i) {
      const u32x4 z = *(LAS u32x4*)(rz + 1024 * i);
      s0[i] += (f32x4){bflo(z[0]), bfhi(z[0]), bflo(z[1]), bfhi(z[1])};
      s1[i] += (f32x4){bflo(z[2]), bfhi(z[2]), bflo(z[3]), bfhi(z[3])};
    }
    asm volatile("" : "+v"(s0[0]), "+v"(s0[1]), "+v"(s1[0]), "+v"(s1[1]));
    SCHED(); BARRIER(); SCHED();
    STAMP_AT(13);
    f32x4 pa[2][4], pb[2][4];
#pragma unroll
    for (int g = 0; g < 8; ++g) { pa[g >> 2][g & 3] = *(LAS f32x4*)(sm + t0 + 4096 * g); pb[g >> 2][g & 3] = *(LAS f32x4*)(sm + t1 + 4096 * g); }
    SCHED();
#pragma unroll
    for (int i = 0; i < 2; ++i) {
#pragma unroll
      for (int g = 0; g < 4; ++g) { s0[i] += pa[i][g]; s1[i] += pb[i][g]; }
      st16_sc1(ro, (u32x4){pk(s0[i][0], s0[i][1]), pk(s0[i][2], s0[i][3]), pk(s1[i][0], s1[i][1]), pk(s1[i][2], s1[i][3])}, i ? op1 : op0);
      SCHED();
    }
  }
  STAMP_AT(14); STAMP_OUT();
}

#define WSOFF 0
extern "C" void kernel_launch(void* const* d_in, const int* in_sizes, int n_in, void* d_out, int out_size, void* d_ws,
                              size_t ws_size, hipStream_t stream) {
  Params p;
  p.x = (const u16*)d_in[0]; p.are = (const u16*)d_in[1]; p.aim = (const u16*)d_in[2];
  p.bre = (const u16*)d_in[3]; p.bim = (const u16*)d_in[4]; p.cre = (const u16*)d_in[5];
  p.cim = (const u16*)d_in[6]; p.logdt = (const u16*)d_in[7]; p.dskip = (const u16*)d_in[8];
  p.wg = (const u16*)d_in[10]; p.wu = (const u16*)d_in[11]; p.wd = (const u16*)d_in[12];
  p.out = (u16*)d_out; p.ws = (char*)d_ws + ((size_t)WSOFF << 20);
  s4ffn_kernel<<<256, 512, 0, stream>>>(p);
}
```

```cpp
#include <hip/hip_runtime.h>
#include <stdint.h>

typedef unsigned short u16;
typedef unsigned u32;
typedef unsigned long long u64;
typedef __bf16 bf16x2 __attribute__((ext_vector_type(2)));
typedef __bf16 bf16x4 __attribute__((ext_vector_type(4)));
typedef __bf16 bf16x8 __attribute__((ext_vector_type(8)));
typedef float f32x2 __attribute__((ext_vector_type(2)));
typedef float f32x4 __attribute__((ext_vector_type(4)));
typedef float f32x16 __attribute__((ext_vector_type(16)));
typedef u32 u32x2 __attribute__((ext_vector_type(2)));
typedef u32 u32x4 __attribute__((ext_vector_type(4)));
#define LAS __attribute__((address_space(3)))
#define GAS __attribute__((address_space(1)))
#define DI __device__ __forceinline__
typedef __amdgpu_buffer_rsrc_t rsrc_t;

constexpr int L = 2048, D = 512, NM = 128, F = 1792;
constexpr u32 WS_H = 0, WS_U = 8u << 20, WS_ZT = 10u << 20, WS_ST = 14u << 20, WS_CS = WS_ST + 65536, WS_FL = 15u << 20, WS_END = 16u << 20;
constexpr u32 FL_E = WS_FL, FL_1 = WS_FL + 4096, FL_A = WS_FL + 8192, FL_2 = WS_FL + 131072, FL_2B = 131072, FS = 128;
constexpr u32 KCP = 4160, UNIT = 16384, RB = 32768, RING = 8 * UNIT;
constexpr u32 L_K = 0, L_U = 8 * KCP, US = 4608;
constexpr u32 S_GP = 4128, S_HP = 4144;
constexpr u32 L_P = 320, PS = 3840;
constexpr u32 L_NU0 = 15872, L_NUL = L_U + 2 * US;
constexpr u32 L_ST = 0;
constexpr u32 L_CS = KCP;
constexpr u32 CODE_BYTES = 30720;
constexpr u32 SPIN_CAP = 1u << 16;

struct Params {
  const u16 *x, *are, *aim, *bre, *bim, *cre, *cim, *logdt, *dskip, *wg, *wu, *wd;
  u16* out;
  char* ws;
};

#define STAMP_DECL
#define STAMP_AT(k)
#define STAMP_RT(v)
#define STAMP_NW(k)
#define STAMP_NWEND()
#define STAMP_OUT()

DI float bflo(u32 v) { return __builtin_bit_cast(float, v << 16); }
DI float bfhi(u32 v) { return __builtin_bit_cast(float, v & 0xffff0000u); }
DI u32 pk(float a, float b) { f32x2 f = {a, b}; return __builtin_bit_cast(u32, __builtin_convertvector(f, bf16x2)); }
DI float rbf(float a) { return bflo(pk(a, a)); }
DI void dma4(rsrc_t ra, rsrc_t rb, u32 lds, u32 v0, u32 v1, u32 v2, u32 v3, u32 soff) {
  asm volatile("s_mov_b32 m0, %0\n\ts_nop 0\n\tbuffer_load_dwordx4 %1, %5, %7 offen lds\n\tbuffer_load_dwordx4 %2, %5, %7 offen offset:1024 lds\n\t"
               "buffer_load_dwordx4 %3, %6, %7 offen offset:2048 lds\n\tbuffer_load_dwordx4 %4, %6, %7 offen offset:3072 lds"
               :: "s"(lds), "v"(v0), "v"(v1), "v"(v2), "v"(v3), "s"(ra), "s"(rb), "s"(soff));
}
DI void dma4s(rsrc_t r, u32 lds, u32 v0, u32 v1, u32 v2, u32 v3, u32 soff) {
  asm volatile("s_mov_b32 m0, %0\n\ts_nop 0\n\tbuffer_load_dwordx4 %1, %5, %6 offen sc1 lds\n\tbuffer_load_dwordx4 %2, %5, %6 offen offset:1024 sc1 lds\n\t"
               "buffer_load_dwordx4 %3, %5, %6 offen offset:2048 sc1 lds\n\tbuffer_load_dwordx4 %4, %5, %6 offen offset:3072 sc1 lds"
               :: "s"(lds), "v"(v0), "v"(v1), "v"(v2), "v"(v3), "s"(r), "s"(soff));
}
DI void dma1s(rsrc_t r, u32 lds, u32 v0, u32 soff) {
  asm volatile("s_mov_b32 m0, %0\n\ts_nop 0\n\tbuffer_load_dwordx4 %1, %2, %3 offen sc1 lds" :: "s"(lds), "v"(v0), "s"(r), "s"(soff));
}
#define WAIT_V(n) asm volatile("s_waitcnt vmcnt(" #n ")" ::: "memory")
#define WAIT_L() asm volatile("s_waitcnt lgkmcnt(0)" ::: "memory")
#define BARRIER() asm volatile("s_waitcnt lgkmcnt(0)\n\ts_barrier" ::: "memory")
#define FENCE() asm volatile("" ::: "memory")
#define SCHED() __builtin_amdgcn_sched_barrier(0)
DI bf16x8 ldfrag(LAS char* a) { return *(LAS bf16x8*)a; }
DI bf16x4 tr4(LAS char* a) { return __builtin_amdgcn_ds_read_tr16_b64_v4bf16((LAS bf16x4*)a); }
DI bf16x8 cat(bf16x4 lo, bf16x4 hi) { return __builtin_shufflevector(lo, hi, 0, 1, 2, 3, 4, 5, 6, 7); }
DI bf16x8 trfrag(LAS char* a0, LAS char* a1) {
  bf16x4 lo = __builtin_amdgcn_ds_read_tr16_b64_v4bf16((LAS bf16x4*)a0);
  bf16x4 hi = __builtin_amdgcn_ds_read_tr16_b64_v4bf16((LAS bf16x4*)a1);
  return __builtin_shufflevector(lo, hi, 0, 1, 2, 3, 4, 5, 6, 7);
}
#define MFMA32(a, b, c) __builtin_amdgcn_mfma_f32_32x32x16_bf16(a, b, c, 0, 0, 0)
#define MFMA16_0(d, a, b) asm volatile("v_mfma_f32_16x16x32_bf16 %0, %1, %2, 0" : "=&v"(d) : "v"(a), "v"(b))
#define MFMA16_I(d, a, b) asm volatile("v_mfma_f32_16x16x32_bf16 %0, %1, %2, %0" : "+v"(d) : "v"(a), "v"(b))
#define MFMA16(a, b, c) __builtin_amdgcn_mfma_f32_16x16x32_bf16(a, b, c, 0, 0, 0)
DI u32x2 ld8_sc1(rsrc_t r, u32 voff, u32 soff) { return __builtin_amdgcn_raw_buffer_load_b64(r, voff, soff, 16); }
DI u32x4 ld16_sc1(rsrc_t r, u32 voff, u32 soff) { return __builtin_amdgcn_raw_buffer_load_b128(r, voff, soff, 16); }
DI void st8_sc1(rsrc_t r, u32x2 v, u32 voff, u32 soff) { __builtin_amdgcn_raw_buffer_store_b64(v, r, voff, soff, 16); }
DI void st16_sc1(rsrc_t r, u32x4 v, u32 voff) { __builtin_amdgcn_raw_buffer_store_b128(v, r, voff, 0, 16); }
template <int SLEEP> DI void wait_flags(rsrc_t r, u32 voff, u32 tlo, u32 thi) {
#pragma unroll 1
  for (u32 s = 0; s < SPIN_CAP; ++s) {
    FENCE();
    u32x2 f = ld8_sc1(r, voff, 0);
    if (__all(f.x == tlo && f.y == thi)) break;
    __builtin_amdgcn_s_sleep(SLEEP);
  }
  WAIT_V(0);
}
DI void post(LAS char* word, u32 v) {
  asm volatile("s_mov_b64 exec, 1\n\tds_write_b32 %0, %1\n\ts_mov_b64 exec, -1" :: "v"(word), "v"(v) : "memory");
}
DI u32 peek(LAS char* word) { return *(volatile LAS u32*)word; }
template <int SLEEP = 1> DI void await(LAS char* word, u32 seen, u32 need) {
  if (__builtin_expect(__any(seen < need), 0)) {
#pragma unroll 1
    for (u32 s = 0; s < SPIN_CAP * 16; ++s) { __builtin_amdgcn_s_sleep(SLEEP); if (!__any(peek(word) < need)) break; }
  }
  FENCE();
}
#define DPPADD(name, ctl) DI float name(float x) { float r; asm("s_nop 1\n\tv_add_f32_dpp %0, %1, %1 " ctl " row_mask:0xf bank_mask:0xf" : "=v"(r) : "v"(x)); return r; }
DPPADD(add_x1, "quad_perm:[1,0,3,2]") DPPADD(add_x2, "quad_perm:[2,3,0,1]") DPPADD(add_m8, "row_half_mirror") DPPADD(add_x8, "row_ror:8") DPPADD(add_r4, "row_ror:4")
DI float fold32(float a, float b) {
  asm("s_nop 1\n\tv_permlane32_swap_b32 %0, %1\n\tv_add_f32 %0, %0, %1" : "+v"(a), "+v"(b)); return a;
}
DI float fold16(float a, float b) {
  asm("s_nop 1\n\tv_permlane16_swap_b32 %0, %1\n\tv_add_f32 %0, %0, %1" : "+v"(a), "+v"(b)); return a;
}
DI u32 vmov(u32 a) { u32 r; asm("v_mov_b32 %0, %1" : "=v"(r) : "v"(a)); return r; }
DI float vadd(float a, float b) { float r; asm("v_add_f32 %0, %1, %2" : "=v"(r) : "v"(a), "v"(b)); return r; }
#define PIN(x) asm volatile("" : "+v"(x))
struct cpx { float r, i; };
struct cp2 { f32x2 r, i; };
DI cpx cmul(cpx a, cpx b) { return {a.r * b.r - a.i * b.i, a.r * b.i + a.i * b.r}; }
DI cpx csq(cpx a) { return {a.r * a.r - a.i * a.i, (a.r + a.r) * a.i}; }
DI cp2 cmul2(cp2 a, cpx b) { return {a.r * b.r - a.i * b.i, a.i * b.r + a.r * b.i}; }
DI cpx cexpl(float ar2, float air, float l) {
  float m = __builtin_amdgcn_exp2f(ar2 * l), f = __builtin_amdgcn_fractf(air * l);
  return {m * __builtin_amdgcn_cosf(f), m * __builtin_amdgcn_sinf(f)};
}

#define EXP 0
__global__ __launch_bounds__(512, 2) void s4ffn_kernel(Params p) {
  __shared__ __attribute__((aligned(16))) char smem[163840];
  LAS char* sm = (LAS char*)smem;
  const u32 smb = (u32)(size_t)sm;
  const int tid = threadIdx.x, lane = tid & 63;
  const int w = __builtin_amdgcn_readfirstlane(tid >> 6), v = w & 3;
  const int b = blockIdx.x, xcd = b & 7, o = b >> 3;
  if ((w >> 1) == 1) {
    u64 pc; u32 d;
    asm volatile("s_getpc_b64 %0" : "=s"(pc));
    const u32 off = (2 * o + (w & 1)) * 512;
    asm volatile(".set pf_off, 0\n\t.rept 8\n\ts_load_dword %0, %1, pf_off\n\t.set pf_off, pf_off + 64\n\t.endr\n\ts_waitcnt lgkmcnt(0)"
                 : "=&s"(d) : "s"((pc & ~255ull) + (off < CODE_BYTES ? off : 0)));
  }
  const bool aux = o >= 28;
  const int ma = xcd >> 1, fh = xcd & 1;
  const int il = o / 7, jl = o - il * 7;
  const int mi = 4 * ma + il, fj = 7 * fh + jl;
  const int ti2 = 4 * xcd + (o >> 3), n2 = o & 7;
  const int ch0 = 64 * xcd + 2 * o;
  const int hh = lane >> 5, blk = (lane >> 4) & 1, q4 = (lane >> 2) & 3, p4 = lane & 3, g4 = lane >> 4;
  STAMP_DECL; STAMP_RT(rt0_); STAMP_AT(0);
#define DESCRIPTORS() const rsrc_t rx = __builtin_amdgcn_make_buffer_rsrc((void*)p.x, 0, L * D * 2, 0x00020000), \
  rws = __builtin_amdgcn_make_buffer_rsrc((void*)p.ws, 0, WS_END, 0x00020000)
  u64 ev;
#define LOAD_EPOCH() asm volatile("s_load_dwordx2 %0, %1, 0\n\ts_waitcnt lgkmcnt(0)" : "=&s"(ev) : "s"(p.ws + FL_E) : "memory")
#define MAKE_TAG() const u32 tlo = (u32)(ev + 0x9E3779B97F4A7C15ull), thi = (u32)((ev + 0x9E3779B97F4A7C15ull) >> 32), tg32 = tlo ^ thi; const u32x2 tag = {tlo, thi}
  LAS char* const sy = sm + 4 * p4;
  if (w == 3 && lane < 2) *(LAS u32x4*)(sm + S_GP + 16 * lane) = (u32x4)(0u);

  if (__builtin_expect(aux, 0)) {
    DESCRIPTORS();
    const int ab = 8 * ma + 4 * fh + o - 28, rb = 8 * ab + w;
    u32x4 xr[8];
#pragma unroll
    for (int i = 0; i < 8; ++i) xr[i] = __builtin_amdgcn_raw_buffer_load_b128(rx, lane * 16 + 1024 * (i & 3), (8 * rb + (i & 4)) * 1024, 0);
    LOAD_EPOCH();
    MAKE_TAG();
          u32 one = 0x3f803f80u;
          asm("" : "+v"(one));
          float su[8], sq[8];
#pragma unroll
          for (int r = 0; r < 8; ++r) {
            su[r] = sq[r] = 0.f;
#pragma unroll
            for (int e = 0; e < 4; ++e) {
              const u32 d = xr[r][e];
              su[r] = __builtin_amdgcn_fdot2_f32_bf16(__builtin_bit_cast(bf16x2, d), __builtin_bit_cast(bf16x2, one), su[r], false);
              sq[r] = __builtin_amdgcn_fdot2_f32_bf16(__builtin_bit_cast(bf16x2, d), __builtin_bit_cast(bf16x2, d), sq[r], false);
            }
          }
          asm volatile("s_nop 2" : "+v"(su[0]), "+v"(su[1]), "+v"(su[2]), "+v"(su[3]), "+v"(su[4]), "+v"(su[5]), "+v"(su[6]), "+v"(su[7]),
                                   "+v"(sq[0]), "+v"(sq[1]), "+v"(sq[2]), "+v"(sq[3]), "+v"(sq[4]), "+v"(sq[5]), "+v"(sq[6]), "+v"(sq[7]));
          auto total = [&](const float (&a)[8]) {
            const float lo = add_x8(fold16(fold32(a[0], a[2]), fold32(a[1], a[3]))), hi = add_x8(fold16(fold32(a[4], a[6]), fold32(a[5], a[7])));
            return add_m8(add_x2(add_x1(lane & 8 ? hi : lo)));
          };
          const float mean = total(su) * (1.f / D), nsq = -mean * mean;
          const float var = rbf(__builtin_fmaxf(__builtin_fmaf(total(sq), 1.f / D, nsq), 0.f));
          const u32 gran = pk(mean, __builtin_amdgcn_sqrtf(rbf(var + 1e-5f)));
          const float r = __builtin_amdgcn_rcpf(bfhi(gran)), mr = -bflo(gran) * r;
          u32 n[8][4];
#pragma unroll
          for (int i = 0; i < 4; ++i) {
            const int l0 = 16 * (2 * i & 3) + 8 * (i >> 1);
            auto at = [&](float t, int l) { return __builtin_bit_cast(float, __builtin_amdgcn_readlane(__builtin_bit_cast(int, t), l)); };
            const f32x2 r2 = {at(r, l0), at(r, l0 + 16)}, m2 = {at(mr, l0), at(mr, l0 + 16)};
#pragma unroll
            for (int e = 0; e < 4; ++e) {
              const u32 a = xr[2 * i][e], c = xr[2 * i + 1][e];
              const f32x2 lo = (f32x2){bflo(a), bflo(c)} * r2 + m2, hi = (f32x2){bfhi(a), bfhi(c)} * r2 + m2;
              n[2 * e][i] = pk(lo.x, lo.y); n[2 * e + 1][i] = pk(hi.x, hi.y);
            }
          }
    if ((lane & 7) == 0) st8_sc1(rws, (u32x2){gran, tg32}, 8 * g4 + 4 * (lane & 8), WS_ST + 64 * rb);
#pragma unroll
    for (int e = 0; e < 8; ++e) *(LAS u32x4*)(sm + RB + (8 * lane + e) * 128 + ((w ^ (lane & 7)) << 4)) = (u32x4){n[e][0], n[e][1], n[e][2], n[e][3]};
    STAMP_AT(18);
    BARRIER();
#pragma unroll
    for (int e = 0; e < 8; ++e) {
      const u32 row = 64 * w + 8 * e + (lane >> 3);
      st16_sc1(rws, *(LAS u32x4*)(sm + RB + row * 128 + (((lane & 7) ^ e) << 4)), WS_U + row * 4096 + ab * 128 + (lane & 7) * 16);
    }
    WAIT_V(0);
    if (lane == 0) st8_sc1(rws, tag, FL_1 + (w * 32 + ab) * 8, 0);
    STAMP_AT(19);
  }

  if (w < 4) {
    const int k = v & 1;
    u32 vx0 = (lane >> 3) * 1024 + (((lane & 7) ^ g4) << 4), vx1 = (vx0 ^ 64) + 7168, vx2 = vx0 + 14336, vx3 = vx1 + 14336;
    u32 vg0 = g4 * (F * 2) + (((((lane & 15) >> 1) ^ g4) << 5) | ((lane & 1) << 4)), vg1 = vg0 + 4 * F * 2 - 1024;
    u32 vu0 = vg0 ^ 128, vu1 = vu0 + 4 * F * 2 - 1024;
    u32 sx0 = (mi * 128 + 64 * v) * 1024, sw0 = 16 * v * (F * 2) + fj * 256;
    asm volatile("" : "+v"(vx0), "+v"(vx1), "+v"(vx2), "+v"(vx3), "+v"(vg0), "+v"(vg1), "+v"(vu0), "+v"(vu1));
    asm volatile("" : "+s"(sx0), "+s"(sw0));
    SCHED();
    DESCRIPTORS();
    const rsrc_t rwd = __builtin_amdgcn_make_buffer_rsrc((void*)p.wd, 0, D * F * 2, 0x00020000);
#define ROT2 0
    const int rot2 = ROT2;
    const u32 dq = smb + RB + 4096 * v;
    const u32 vd0 = (lane >> 3) * (D * 2) + (((lane & 7) ^ (g4 << 1)) << 4), vd1 = vd0 + 7168, vd2 = vd0 + 14336, vd3 = vd0 + 21504;
    auto ks_of = [&](int q) { int r = rot2; if (!__builtin_constant_p(r)) asm volatile("" : "+s"(r)); int j = q % 7 + r; j = j >= 7 ? j - 7 : j; return 28 * v + 4 * j + 2 * (q / 7); };
    const u32 sd0 = 28 * v * (16 * D * 2) + n2 * 128;
    auto wdq = [&](int q, int slot) {
      u32 so = sd0;
      asm volatile("" : "+s"(so));
      dma4(rwd, rwd, dq + slot * UNIT, vd0, vd1, vd2, vd3, so + (ks_of(q) - 28 * v) * (16 * D * 2));
    };
    const u32 wo0 = RB + 4096 * v + hh * 1024 + (4 * blk + q4) * 128 + ((2 * blk + (q4 >> 1)) << 5) + p4 * 8;
    auto whalf = [&](int slot, int ct, int t) { return tr4(sm + (wo0 ^ (ct << 5)) + slot * (int)UNIT + 2048 * t); };
    bf16x8 wr_[7][4];
    auto take = [&](int q, int slot) {
#pragma unroll
      for (int e = 0; e < 4; ++e) wr_[q][e] = cat(whalf(slot, e, 0), whalf(slot, e, 1));
    };
    auto last7 = [&] {
    wdq(7, 1);
    {
      const u32 r = lane < 24 ? lane : 0, j = 4 + (r >> 3) + rot2, ks = 4 * (j >= 7 ? j - 7 : j);
      asm volatile("s_mov_b32 m0, %0\n\ts_nop 0\n\tbuffer_load_dword %1, %2, %3 offen lds" :: "s"(smb + L_NUL), "v"((16 * ks + (r & 7)) * (D * 2)), "s"(rwd), "s"((28 * v + 2) * (16 * D * 2) + (ti2 & 3) * 8192 + n2 * 128));
    }
    };
    u32x4 kc[4][4];
    u64 fbad = 0;
    if (v < 2 && !aux) {
        const rsrc_t rwg = __builtin_amdgcn_make_buffer_rsrc((void*)p.wg, 0, D * F * 2, 0x00020000);
        const rsrc_t rwu2 = __builtin_amdgcn_make_buffer_rsrc((void*)((const char*)p.wu - 2048), 0, D * F * 2 + 2048, 0x00020000);
        u32 dst = 8192 * v;
        auto ux = [&](u32 c) {
          const u32 s = sx0 + ((2 * ma + c) & 7) * 128;
          dma4(rx, rx, smb + RB + dst, vx0, vx1, vx2, vx3, s); dma4(rx, rx, smb + RB + dst + 4096, vx0, vx1, vx2, vx3, s + 32768);
          dst = (dst + UNIT) & (RING - 1);
        };
        auto uw = [&](u32 c, u32 h) {
          const u32 s = sw0 + (2 * ((2 * ma + c) & 7) + h) * (32 * F * 2);
          dma4(rwg, rwu2, smb + RB + dst, vg0, vg1, vg0, vg1, s); dma4(rwg, rwu2, smb + RB + dst + 4096, vu0, vu1, vu0, vu1, s + 8 * F * 2);
          dst = (dst + UNIT) & (RING - 1);
        };
#define LDELAY 4
      __builtin_amdgcn_s_sleep(LDELAY);
      ux(0); uw(0, 0);
      LOAD_EPOCH();
      uw(0, 1); ux(1); uw(1, 0);
#pragma unroll
      for (int c = 0; c < 7; ++c) {
        if (c) { uw(c, 1); ux(c + 1); WAIT_V(16); take(c - 1, -1); uw(c + 1, 0); WAIT_L(); SCHED(); }
        wdq(c, -1);
        WAIT_V(20);
        if (c == 0) STAMP_AT(1);
        __builtin_amdgcn_s_barrier();
      }
      uw(7, 1);
      last7();
      WAIT_V(5);
    } else {
      const bool wrk = v >= 2, csm = wrk && !aux;
      const int mat = il >> 1, r8 = (lane >> 2) & 7;
      LAS char* const cp = sm + RB + UNIT + hh * 4096 + mat * 2048 + r8 * 256 + (((4 * (il & 1) + 2 * k + (p4 >> 1)) ^ ((r8 & 3) + 4 * hh)) << 5) + (lane & 1) * 16;
      const u32 vf1 = (hh ? FL_A + 512 * n2 + 256 * k : FL_1 + 256 * xcd) + (lane & 31) * 8;
      auto stats = [&] {
        dma4s(rws, smb + L_CS + 4096 * k, lane * 16, lane * 16, lane * 16, lane * 16, WS_CS + (fj * 2 + k) * 4096);
        if (k) dma1s(rws, smb + L_ST, lane * 16, WS_ST + mi * 1024);
      };
      float s[8]; u32x4 ct[8]; u32x2 f1; u32 dsk;
      auto cadd = [&](int i) {
#pragma unroll
        for (int e = 0; e < 4; ++e) {
          if (i == 0) { s[2 * e] = bflo(ct[0][e]); s[2 * e + 1] = bfhi(ct[0][e]); }
          else asm("v_add_f32 %0, %0, %2\n\tv_add_f32 %1, %1, %3" : "+v"(s[2 * e]), "+v"(s[2 * e + 1]) : "v"(bflo(ct[i][e])), "v"(bfhi(ct[i][e])));
        }
      };
#define TCH 3
#define TDELAY 4
      dsk = p.dskip[ch0 + k];
      if (TDELAY) __builtin_amdgcn_s_sleep(TDELAY);
      if ((TCH & 1) && v == 2) asm volatile("s_mov_b32 m0, %0\n\ts_nop 0\n\tbuffer_load_dword %1, %2, %3 offen lds" :: "s"(smb + L_NU0), "v"(lane < 56 ? lane * 128 : 0), "s"(rwd), "s"(b * 7168));
      if ((TCH & 2) && v == 3 && !aux) {
        const rsrc_t rw = __builtin_amdgcn_make_buffer_rsrc((void*)(mat ? p.wu : p.wg), 0, D * F * 2, 0x00020000);
        asm volatile("s_mov_b32 m0, %0\n\ts_nop 0\n\tbuffer_load_dword %1, %2, %3 offen lds" :: "s"(smb + L_NU0), "v"((lane & 31) * (F * 2)), "s"(rw), "s"((128 * ma + 96) * (F * 2) + fj * 256 + (il & 1) * 128));
      }
      if (!aux) LOAD_EPOCH();
      MAKE_TAG();
#define WDELAY 8
      __builtin_amdgcn_s_sleep(WDELAY);
      wdq(0, -1);
#define CDELAY 0
      if (wrk) {
        __builtin_amdgcn_s_sleep(CDELAY);
#pragma unroll
        for (int i = 0; i < 4; ++i) st16_sc1(rws, (u32x4)(~0u), WS_ZT + (ch0 + k) * 4096 + lane * 16 + 1024 * i);
      }
#pragma unroll
      for (int c = 0; c < 7; ++c) {
        if (c) {
          if ((c == 2 && wrk) || (c == 3 && csm)) WAIT_V(1); else if (c == 6 && csm) WAIT_V(5); else WAIT_V(0);
          if (c == 1) PIN(dsk);
          take(c - 1, -1);
          WAIT_L(); SCHED();
        }
        if (!aux || c == 2) __builtin_amdgcn_s_barrier();
        FENCE(); SCHED();
#define KPRIO 1
#define STC 5
        __builtin_amdgcn_s_setprio(KPRIO);
        if (c == 6 && wrk) fbad = __builtin_amdgcn_ballot_w64(f1.x != tlo || f1.y != thi);
        if (c) wdq(c, -1);
        if (c == 6) last7();
        if (c == 1 && wrk && lane == 0) st8_sc1(rws, tag, FL_A + (ch0 + k) * 8, 0);
        if (c == STC && csm) stats();
        if (c == 5 && wrk) f1 = ld8_sc1(rws, vf1, 0);
        if (c == 6 && wrk) {
          if ((u32)fbad) wait_flags<8>(rws, FL_1 + 256 * xcd + (lane & 31) * 8, tlo, thi);
          FENCE();
          dma4s(rws, smb + L_U + US * k + 512, lane * 16, lane * 16, lane * 16, lane * 16, WS_U + (ch0 + k) * 4096);
        }
        __builtin_amdgcn_s_setprio(0);
        SCHED();
        if (c < 2 && csm) {
#pragma unroll
          for (int i = 0; i < 4; ++i) ct[4 * c + i] = *(LAS u32x4*)(cp + 3 * UNIT * c + 8192 * i);
#pragma unroll
          for (int i = 0; i < 4; ++i) cadd(4 * c + i);
          asm volatile("" : "+v"(s[0]), "+v"(s[1]), "+v"(s[2]), "+v"(s[3]), "+v"(s[4]), "+v"(s[5]), "+v"(s[6]), "+v"(s[7]));
        }
        if (c == 3 && wrk) {
        float kv[16];
        LAS char* pa = sm + (k ? L_P + 2 * PS : 0), *pb = sm + L_P + (2 * k + 1) * PS;
#pragma unroll
        for (int g = 0; g < 3; ++g) {
          const f32x4 a = *(LAS f32x4*)(pa + 1024 * g + 16 * lane), d = *(LAS f32x4*)(pb + 1024 * g + 16 * lane);
#pragma unroll
          for (int e = 0; e < 4; ++e) asm("v_add_f32 %0, %1, %2" : "=v"(kv[4 * g + e]) : "v"(a[e]), "v"(d[e]));
          asm("v_add_f32 %0, %1, %2" : "=v"(kv[12 + g]) : "v"(*(LAS float*)(pa + 3072 + 256 * g + 4 * lane)), "v"(*(LAS float*)(pb + 3072 + 256 * g + 4 * lane)));
        }
        const bool odd = lane & 1;
        if (lane == 0) kv[0] += bflo(dsk);
        kc[0][0] = (u32x4){pk(kv[0], kv[1]), pk(kv[2], kv[3]), pk(kv[4], kv[5]), pk(kv[6], kv[7])};
        kc[1][0] = (u32x4){pk(kv[8], kv[8]), pk(kv[8], kv[9]), pk(kv[9], kv[9]), pk(kv[10], kv[10])};
        kc[2][0] = (u32x4){pk(kv[11], kv[11]), pk(kv[11], kv[11]), pk(odd ? kv[12] : kv[11], kv[12]), pk(kv[12], kv[12])};
        kc[3][0] = (u32x4){pk(kv[13], kv[13]), pk(kv[13], kv[13]), pk(odd ? kv[14] : kv[13], kv[14]), pk(kv[14], kv[14])};
          asm volatile("" : "+v"(kc[0][0]), "+v"(kc[1][0]), "+v"(kc[2][0]), "+v"(kc[3][0]));
        }
        if (c == 4 && wrk) {
#pragma unroll
        for (int g = 0; g < 4; ++g) {
          const u32x4 cu = kc[g][0];
          int px = 0, py = 0;
          if (g) {
            px = __builtin_amdgcn_update_dpp(0, (int)kc[g ? g - 1 : 0][0].z, 0x13C, 0xf, 0xf, false);
            py = __builtin_amdgcn_update_dpp(0, (int)kc[g ? g - 1 : 0][0].w, 0x13C, 0xf, 0xf, false);
          }
          px = __builtin_amdgcn_update_dpp(px, (int)cu.z, 0x138, 0xf, 0xf, false);
          py = __builtin_amdgcn_update_dpp(py, (int)cu.w, 0x138, 0xf, 0xf, false);
          const u32 s0 = __builtin_amdgcn_alignbit(py, px, 16), s1 = __builtin_amdgcn_alignbit(cu.x, py, 16), s2 = __builtin_amdgcn_alignbit(cu.y, cu.x, 16),
                    s3 = __builtin_amdgcn_alignbit(cu.z, cu.y, 16), s4 = __builtin_amdgcn_alignbit(cu.w, cu.z, 16);
          kc[g][1] = (u32x4){s1, s2, s3, s4}; kc[g][2] = (u32x4){(u32)py, vmov(cu.x), vmov(cu.y), vmov(cu.z)}; kc[g][3] = (u32x4){s0, vmov(s1), vmov(s2), vmov(s3)};
          asm volatile("" : "+v"(kc[g][0]), "+v"(kc[g][1]), "+v"(kc[g][2]), "+v"(kc[g][3]));
        }
          STAMP_AT(2);
        }
        if (c == 2 && csm) {
          const float lo = add_x8(add_r4(fold16(fold32(s[0], s[4]), fold32(s[2], s[6])))), hi = add_x8(add_r4(fold16(fold32(s[1], s[5]), fold32(s[3], s[7]))));
          s[0] = lane & 8 ? hi : lo;
          const float th = rbf(s[0]);
          if (!(lane & 4)) st8_sc1(rws, (u32x2){pk(th, s[0] - th), tg32}, 8 * (8 * p4 + 4 * hh + 2 * blk + ((lane >> 3) & 1)), WS_CS + (((fj * 2 + mat) * 4 + ma) * 128 + 64 * (il & 1) + 32 * k) * 8);
        }
        if (c == STC + 1 && wrk && !aux) {
#pragma unroll 1
        for (u32 s = 0; s < SPIN_CAP; ++s) {
          u32 d = 0;
#pragma unroll
          for (int i = 0; i < 5; ++i) {
            const u32x4 t = *(LAS u32x4*)(sm + (i < 4 || !k ? L_CS + 4096 * k + 1024 * (i & 3) : L_ST) + 16 * lane);
            d |= (t.y ^ tg32) | (t.w ^ tg32);
          }
          if (!__any(d != 0)) break;
          stats();
          WAIT_V(0);
        }
        }
      }
    }
    STAMP_AT(4);
    MAKE_TAG();
    STAMP_AT(6);
    BARRIER();
    wdq(8, 2); wdq(9, 3); wdq(10, 4);
    WAIT_V(17); take(6, -1);
    LAS char* kd = sm + L_K + 4 * KCP * k + 16 * lane;
    LAS char* kz = sm + (lane < 32 ? L_U + US * k + 16 * lane : L_K + (4 * k + ((lane >> 1) & 3)) * KCP + 16 * (lane & 1));
    asm volatile("" : "+v"(kd), "+v"(kz));
    u32x4 zero = (u32x4)(0u);
    u32 two = 2;
    LAS char* hpw = sm + S_HP + 4 * v;
    asm volatile("" : "+v"(zero), "+v"(two), "+v"(hpw));
    STAMP_AT(5);
    SCHED();
    BARRIER();
    STAMP_AT(7);
    if (v >= 2) {
      WAIT_V(0);
      *(LAS u32x4*)kz = zero;
#pragma unroll
      for (int e = 0; e < 16; ++e) *(LAS u32x4*)(kd + 32 + KCP * (e & 3) + 1024 * (e >> 2)) = kc[e >> 2][e & 3];
      post(hpw, two);
      WAIT_L();
      if (fbad >> 32) wait_flags<8>(rws, FL_A + 512 * n2 + 256 * k + (lane & 31) * 8, tlo, thi);
    } else __builtin_amdgcn_s_sleep(8);
    STAMP_AT(8);
#define LATEQ 1
#define WPRIO 2
    if (!LATEQ) { wdq(11, 5); wdq(12, 6); wdq(13, 7); }
    __builtin_amdgcn_s_setprio(WPRIO);
#define HDELAY 0
#define AUXSLEEP 32
    if (!aux) { await<4>(sy + S_GP, peek(sy + S_GP), 2); __builtin_amdgcn_s_sleep(HDELAY); }
    else __builtin_amdgcn_s_sleep(AUXSLEEP);
    if (LATEQ == 1) { __builtin_amdgcn_s_setprio(0); wdq(11, 5); wdq(12, 6); wdq(13, 7); }
    STAMP_AT(9);
    const u32 vf2 = FL_2 + (ti2 * 28 + 7 * v + (lane < 7 ? lane : 0)) * FS;
#define APOLL 1
    if (APOLL) {
#pragma unroll 1
      for (u32 s = 0; s < SPIN_CAP; ++s) {
        u32x2 f = tag;
        if (lane < 7) { f = (u32x2)(0u); asm volatile("buffer_atomic_or_x2 %0, %1, %2, 0 offen sc0 sc1\n\ts_waitcnt vmcnt(0)" : "+v"(f) : "v"(vf2), "s"(rws) : "memory"); }
        if ((EXP & 4) || __all(f.x == tlo && f.y == thi)) break;
        __builtin_amdgcn_s_sleep(2);
      }
      FENCE();
    } else wait_flags<8>(rws, vf2, tlo, thi);
    __builtin_amdgcn_s_setprio(0);
    STAMP_AT(10);
    f32x4 acc[4][4];
#define PD 12
#define ZAT 8
    u32x4 hf[56];
    u32 hs;
    auto hload = [&](int n) { if (!(n & 3)) hs = (ti2 * 112 + ks_of(n >> 2)) * 2048; hf[n] = ld16_sc1(rws, lane * 16 + 1024 * (n & 3), hs); };
#pragma unroll
    for (int n = 0; n < PD; ++n) hload(n);
    bf16x4 wl[2][8];
    u32x2 fb; u32 pw_[4], wb_[4];
    SCHED();
#pragma unroll
    for (int i = 0; i < 14; ++i) {
#pragma unroll
      for (int n = 0; n < 16; ++n) {
        const int m = n >> 2, ct = m & 1 ? 3 - (n & 3) : n & 3, nx = 4 * i + m + PD;
        const bf16x8 a = i < 7 ? wr_[i < 7 ? i : 0][ct] : cat(wl[i & 1][2 * ct], wl[i & 1][2 * ct + 1]);
        const bf16x8 bb = __builtin_bit_cast(bf16x8, hf[4 * i + m]);
        if (i == 0) MFMA16_0(acc[ct][m], a, bb); else MFMA16_I(acc[ct][m], a, bb);
        SCHED();
#define LG 2
        if ((n & 3) == LG && nx < 56) hload(nx);
        if ((n & 3) != LG && n < 11 && i >= 6 && i < 13) {
          const int e = n - (n + 3 - LG) / 4;
          if (e < 8) wl[(i + 1) & 1][e] = tr4(sm + wb_[e >> 1] + (i < 9 ? i - 5 : i - 9) * (int)UNIT + 2048 * (e & 1));
        }
        if ((i == 5 || i == 8) && n >= 12) { wb_[n & 3] = (wo0 ^ ((n & 3) << 5)) + (i == 8 ? 4 * UNIT : 0); PIN(wb_[n & 3]); }
        if ((n & 3) == (LG + 3) % 4) {
          if (nx == 20) fb = ld8_sc1(rws, vf2, FL_2B);
          if (nx == 28) {
            if (__builtin_expect(!__all(fb.x == tlo && fb.y == thi), 0)) wait_flags<4>(rws, vf2 + FL_2B, tlo, thi);
            FENCE();
          }
          if (i == ZAT && m == 3) post(sm + S_HP + 4 * v, 3);
          if (i == 13) { pw_[m] = (RB + UNIT + 4096 * v + (lane & 15) * 128 + ((g4 ^ (lane & 7)) << 4)) ^ ((m & 1) << 6) ^ ((m >> 1) << 4); PIN(pw_[m]); }
        }
        SCHED();
      }
      if (i == 3) STAMP_AT(15);
      if (i == 6) STAMP_AT(16);
      if (i == 10) STAMP_AT(17);
    }
    STAMP_AT(11);
#define BAT 8
    asm volatile("s_nop 7" : "+v"(acc[0][3]), "+v"(acc[1][3]), "+v"(acc[2][3]), "+v"(acc[3][3]));
#pragma unroll
    for (int a = 0; a < 16; ++a) {
      const int ct = a & 3, m = a >> 2;
      if (a == BAT) { SCHED(); __builtin_amdgcn_s_barrier(); SCHED(); }
      *(LAS f32x4*)(sm + pw_[ct] + UNIT * (2 * (ct >> 1) + (m >> 1)) + 2048 * (m & 1)) = acc[ct][m];
    }
    STAMP_AT(12);
    BARRIER();
  } else {
    __builtin_amdgcn_s_setprio(1);
    const int c = v >> 1, ah = v & 1, ch = ch0 + c;
    u32 gi = (ch * NM + 64 * ah + lane) * 2, gd = ch * 2;
    asm volatile("" : "+v"(gi), "+v"(gd));
    SCHED();
    auto at = [](const u16* q, u32 off) { return (u32)*(const u16*)((const char*)q + off); };
    const u32 r6 = at(p.logdt, gd), r0 = at(p.are, gi), r1 = at(p.aim, gi), r2 = at(p.bre, gi), r3 = at(p.bim, gi), r4 = at(p.cre, gi), r5 = at(p.cim, gi);
    SCHED();
    STAMP_AT(2);
    DESCRIPTORS();
    const int tg = c, fq = ah;
    u32 xa[2], wa[4], iw[4], ir[2];
    {
      const u32 row = 64 * tg + (lane & 15), f = (row >> 1) & 7, scr = RB + 5 * UNIT + 4096 * v, cki = 2 * blk + (p4 >> 1);
#pragma unroll
      for (int s = 0; s < 4; ++s) {
        if (s < 2) xa[s] = RB + row * 128 + (((4 * s + g4) ^ f) << 4);
        wa[s] = RB + g4 * 4096 + q4 * 256 + (((4 * fq + s) ^ (q4 + 4 * blk)) << 5) + p4 * 8;
        iw[s] = scr + lane * 64 + ((s ^ ((lane >> 1) & 3)) << 4);
        asm volatile("" : "+v"(xa[s & 1]), "+v"(wa[s]), "+v"(iw[s]));
      }
      ir[0] = scr + (8 * hh + q4) * 64 + ((cki ^ (q4 >> 1)) << 4) + 8 * (p4 & 1);
      ir[1] = scr + (8 * hh + 4 + q4) * 64 + ((cki ^ (2 + (q4 >> 1))) << 4) + 8 * (p4 & 1);
      asm volatile("" : "+v"(ir[0]), "+v"(ir[1]));
    }
    LAS char* ka = sm + L_K + (4 * c + q4) * KCP + 2 * (28 + 128 * ah + 4 * p4 - 8 * g4);
    LAS char* ua = sm + L_U + US * c + 2 * (240 - 96 - 128 * ah + 16 * (lane & 15) + 8 * g4);
    LAS char *hpw = sm + S_HP + 8 + 4 * (lane & 1), *kp4 = sm + ((v ? L_P + PS * v : 0) + 16 * lane), *kp1 = sm + ((v ? L_P + PS * v : 0) + 3072 + 4 * lane);
    asm volatile("" : "+v"(ka), "+v"(ua), "+v"(hpw), "+v"(kp4), "+v"(kp1));
    u64 ev2;
    asm volatile("s_load_dwordx2 %0, %1, 0" : "=&s"(ev2) : "s"(p.ws + FL_E) : "memory");
    SCHED();
    STAMP_AT(15);
    STAMP_AT(16);
    bf16x8 yf[8], xf[8];
    {
      const float dt = rbf(__builtin_amdgcn_exp2f(bflo(r6) * 1.44269504088896f));
      const float ar2 = dt * bflo(r0) * 1.44269504088896f, air = dt * bflo(r1) * 0.159154943091895f;
      auto put = [&](int img, int g, u32x4 re, u32x4 im) {
        if (EXP & 8) return;
        *(LAS u32x4*)(sm + iw[g] + 2 * img * UNIT) = re; *(LAS u32x4*)(sm + iw[g] + (1 - img) * UNIT) = im;
        SCHED();
      };
      auto get = [&](int img, int s) { const u32 d = (s < 4 ? 2 * img : 1 - img) * UNIT + 1024 * (s & 3); return trfrag(sm + ir[0] + d, sm + ir[1] + d); };
#define P2_(a) pk((a).x, (a).y)
#define P4_(a, b, c, d) (u32x4){P2_(a), P2_(b), P2_(c), P2_(d)}
      const cpx e8 = cexpl(ar2, air, 8.f);
      {
        const cpx s1 = {e8.r, -e8.i}, s2 = csq(s1), s4 = csq(s2), s8 = cexpl(ar2, -air, 64.f), s16 = csq(s8);
        const cp2 y0 = {{1.f, s1.r}, {0.f, s1.i}}, y1 = cmul2(y0, s2), y2 = cmul2(y0, s4), y3 = cmul2(y1, s4);
        SCHED();
        asm volatile("s_waitcnt lgkmcnt(0)" : "+s"(ev2) :: "memory");
        if (!aux) ev = ev2;
        put(0, 0, P4_(y0.r, y1.r, y2.r, y3.r), P4_(y0.i, y1.i, y2.i, y3.i));
        const cp2 y4 = cmul2(y0, s8), y5 = cmul2(y1, s8), y6 = cmul2(y2, s8), y7 = cmul2(y3, s8);
        put(0, 1, P4_(y4.r, y5.r, y6.r, y7.r), P4_(y4.i, y5.i, y6.i, y7.i));
        const cp2 y8 = cmul2(y0, s16), y9 = cmul2(y1, s16), ya = cmul2(y2, s16), yb = cmul2(y3, s16);
        put(0, 2, P4_(y8.r, y9.r, ya.r, yb.r), P4_(y8.i, y9.i, ya.i, yb.i));
        const cp2 yc = cmul2(y4, s16), yd = cmul2(y5, s16), ye = cmul2(y6, s16), yg = cmul2(y7, s16);
        put(0, 3, P4_(yc.r, yd.r, ye.r, yg.r), P4_(yc.i, yd.i, ye.i, yg.i));
      }
      {
        const cpx cf = cmul({bflo(r4), bflo(r5)}, {dt * bflo(r2), dt * bflo(r3)});
        const cpx e1 = cexpl(ar2, air, 1.f), e256 = cexpl(ar2, air, 256.f);
        const cpx e2 = csq(e1), e4 = csq(e2), e512 = csq(e256), e1024 = csq(e512);
        const cp2 cb = cmul2({{e1.r, e256.r}, {e1.i, e256.i}}, cf);
        const cp2 a0 = {{cf.r, cb.r.x}, {cf.i, cb.i.x}}, a1 = cmul2(a0, e2);
        const cp2 t0 = cmul2({{e2.r, e4.r}, {e2.i, e4.i}}, {cb.r.y, cb.i.y});
#define XA(m) (u32x4){P2_(a0.m), P2_(a1.m), pk(cb.m.y, cb.m.y), P2_(t0.m)}
#pragma unroll
        for (int s = 0; s < 4; ++s) yf[s] = get(0, s);
        put(1, 0, XA(r), XA(i));
        const cp2 a2 = cmul2(a0, e4), a3 = cmul2(a1, e4), t1 = cmul2(t0, e4);
#define XB(m) (u32x4){P2_(a2.m), P2_(a3.m), pk(t0.m.y, t0.m.y), P2_(t1.m)}
        put(1, 1, XB(r), XB(i));
#pragma unroll
        for (int s = 4; s < 8; ++s) yf[s] = get(0, s);
        const cp2 q0 = {{cf.r, cb.r.y}, {cf.i, cb.i.y}}, q1 = cmul2(q0, e512), q2 = cmul2(q0, e1024);
        const cp2 u4 = cmul2(q1, e4), u8 = cmul2(q1, e8);
#define XC(m) (u32x4){pk(q1.m.x, u4.m.x), pk(u8.m.x, q2.m.x), pk(q1.m.y, u4.m.y), pk(u8.m.y, q2.m.y)}
        put(1, 2, XC(r), XC(i));
        const cp2 q3 = cmul2(q1, e1024), v8 = cmul2(q2, e8), w8 = cmul2(q3, e8);
#define XD(m) (u32x4){pk(v8.m.x, q3.m.x), pk(w8.m.x, 0.f), pk(v8.m.y, q3.m.y), pk(w8.m.y, 0.f)}
        put(1, 3, XD(r), XD(i));
#pragma unroll
        for (int s = 0; s < 8; ++s) xf[s] = get(1, s);
      }

      STAMP_AT(17);
    }
    f32x16 kacc = (f32x16)(0.f);
#pragma unroll
    for (int s = 0; s < 8; ++s) kacc = MFMA32(xf[s], yf[s], kacc);
    auto kstore = [&](int g) {
      if (g < 3) *(LAS f32x4*)(kp4 + 1024 * g) = (f32x4){kacc[4 * g], kacc[4 * g + 1], kacc[4 * g + 2], kacc[4 * g + 3]};
      else { *(LAS float*)kp1 = kacc[12]; *(LAS float*)(kp1 + 256) = kacc[13]; *(LAS float*)(kp1 + 512) = kacc[14]; }
    };
    kstore(0); kstore(1); kstore(2); kstore(3);
    STAMP_AT(1);
    bf16x8 X0[4]; bf16x4 W0[16];
    BARRIER();
    MAKE_TAG();
    W0[0] = tr4(sm + UNIT + wa[0]); W0[1] = tr4(sm + UNIT + wa[0] + 1024);
#pragma unroll
    for (int t = 0; t < 4; ++t) X0[t] = ldfrag(sm + xa[0] + 2048 * t);
#pragma unroll
    for (int t = 2; t < 16; ++t) W0[t] = tr4(sm + UNIT + wa[t >> 2] + 1024 * (t & 3));
    SCHED();
    bf16x4 A[2][16]; bf16x8 B[2][8];
    auto cld1 = [&](int a, int n) {
      const int i = n / 3, r = n % 3;
      if (r < 2) A[a & 1][2 * i + r] = __builtin_amdgcn_ds_read_tr16_b64_v4bf16((LAS bf16x4*)(ka + 64 * a + 512 * i + 8 - 8 * r));
      else B[a & 1][i] = ldfrag(ua + 64 * (3 - a) + 512 * i);
    };
    auto cld = [&](int a, int i) { cld1(a, 3 * i); cld1(a, 3 * i + 1); cld1(a, 3 * i + 2); };
    if (aux) { BARRIER(); BARRIER();
      await(hpw, 0, 2);
#pragma unroll
      for (int i = 0; i < 8; ++i) cld(0, i);
    }
    else {
      f32x4 acc[4][8];
      bf16x8 X1[4]; bf16x4 W1[16];
      auto step = [&](const bf16x8 (&Xc)[4], const bf16x4 (&Wc)[16], bf16x8 (&Xn)[4], bf16x4 (&Wn)[16], u32 xb, u32 wb, int s, bool first, auto mid) {
        u32 xp, wp;
#pragma unroll
        for (int n = 0; n < 32; ++n) {
          const int t = n >> 2, r = n & 3, m = t & 1 ? 3 - r : r;
          const bf16x8 wf = cat(Wc[2 * t], Wc[2 * t + 1]);
          if (first) MFMA16_0(acc[m][t], wf, Xc[m]); else MFMA16_I(acc[m][t], wf, Xc[m]);
          SCHED();
          mid(n);
          if (n == 1) { xp = xb + xa[s]; PIN(xp); }
          if (n == 2 || n == 8 || n == 15 || n == 22) { wp = wb + wa[n / 7]; PIN(wp); }
          if (n >= 4 && n < 8) Wn[n - 4] = tr4(sm + wp + 1024 * (n - 4));
          if (n >= 9 && n < 13) Wn[n - 5] = tr4(sm + wp + 1024 * (n - 9));
          if (n == 13 || n == 14) Xn[n - 13] = ldfrag(sm + xp + 2048 * (n - 13));
          if (n >= 16 && n < 20) Wn[n - 8] = tr4(sm + wp + 1024 * (n - 16));
          if (n == 20 || n == 21) Xn[n - 18] = ldfrag(sm + xp + 2048 * (n - 18));
          if (n >= 23 && n < 27) Wn[n - 11] = tr4(sm + wp + 1024 * (n - 23));
          SCHED();
        }
      };
      u32 xb = 0;
      step(X0, W0, X1, W1, xb, 2 * UNIT, 1, true, [&](int) {});
#pragma unroll 1
      for (int cc = 0; cc < 6; ++cc) {
        xb = (xb + 3 * UNIT) & (RING - 1);
        step(X1, W1, X0, W0, xb, (xb + UNIT) & (RING - 1), 0, false, [&](int n) { if (n == 3) BARRIER(); });
        step(X0, W0, X1, W1, xb, (xb + 2 * UNIT) & (RING - 1), 1, false, [&](int) {});
      }
      step(X1, W1, X0, W0, 5 * UNIT, 6 * UNIT, 0, false, [&](int n) { if (n == 3) BARRIER(); });
      u32 st[4]; u32x2 cs[4]; u32x4 XM[4];
      float ka[4], kb[4], kr[4];
      step(X0, W0, X1, W1, 5 * UNIT, 7 * UNIT, 1, false, [&](int n) {
        if (n == 0) { LAS char* a = sm + L_ST + 8 * (64 * tg + (lane & 15)); st[0] = *(LAS u32*)a; st[1] = *(LAS u32*)(a + 128); st[2] = *(LAS u32*)(a + 256); st[3] = *(LAS u32*)(a + 384); }
        if (n >= 27 && n < 31) { LAS char* a = sm + L_CS + 1024 * g4 + 8 * (64 * fq + (lane & 15)) + 128 * (n - 27); cs[n - 27] = (u32x2){*(LAS u32*)a, *(LAS u32*)(a + 4096)}; }
        if (n >= 8) {
          const int t = (n - 8) / 6, r = (n - 8) % 6;
          if (r == 0) { kr[t] = bfhi(st[t]); ka[t] = __builtin_amdgcn_rcpf(kr[t]); PIN(kr[t]); PIN(ka[t]); }
          if (r == 1) { const u32 mm = ((st[t] & 0xffffu) ^ 0x8000u) * 0x10001u; XM[t].x = mm; PIN(XM[t]); }
          if (r == 2) { kb[t] = -2.f * 0.7978845608028654f * 1.44269504088896f * ka[t]; ka[t] *= ka[t]; PIN(kb[t]); PIN(ka[t]); }
          if (r == 3) { ka[t] *= 0.044715f; kr[t] *= kr[t]; PIN(ka[t]); PIN(kr[t]); }
          if (r == 4) { ka[t] *= kb[t]; XM[t].w = XM[t].x; PIN(ka[t]); PIN(XM[t]); }
          if (r == 5) { XM[t].y = 0; XM[t].z = 0; PIN(XM[t]); }
        }
      });
      STAMP_NW(19);
      auto last = [&](int a, int e) { const int t = (a & 4) + e; const bf16x8 wf = cat(W1[2 * t], W1[2 * t + 1]); MFMA16_I(acc[a & 3][t], wf, X1[a & 3]); SCHED(); };
      auto corr = [&](int a, int e) {
        const int t = (a & 4) + e;
        const u32x4 xm = XM[a & 3];
        const u32x2 xh = e & 1 ? (u32x2){xm.z, xm.w} : (u32x2){xm.x, xm.y};
        asm volatile("v_mfma_f32_16x16x16_bf16 %0, %1, %2, %0" : "+v"(acc[a & 3][t]) : "v"(cs[t >> 1]), "v"(xh));
        SCHED();
      };
#pragma unroll
      for (int e = 0; e < 4; ++e) last(0, e);
#pragma unroll
      for (int e = 0; e < 4; ++e) corr(0, e);
      asm volatile("s_nop 7" : "+v"(acc[0][0]), "+v"(acc[0][1]), "+v"(acc[0][2]), "+v"(acc[0][3]));
      u32 hseen;
      const u32 vh = ((2 * mi + tg) * 112 + 8 * fj + 4 * fq) * 2048 + lane * 16;
      SCHED();
#pragma unroll
      for (int a = 0; a < 8; ++a) {
        const int m = a & 3;
        f32x2 z[4], gu[4];
#pragma unroll
        for (int e = 0; e < 4; ++e) {
          const f32x4 gq = acc[m][(a & 4) + (e & 2)], uq = acc[m][(a & 4) + (e & 2) + 1];
          const f32x2 g = {gq[2 * (e & 1)], gq[2 * (e & 1) + 1]}, u = {uq[2 * (e & 1)], uq[2 * (e & 1) + 1]};
          z[e] = (g * g * ka[m] + kb[m]) * g; gu[e] = g * u;
        }
        SCHED();
        if (a == 7) { cld(0, 0); cld(0, 1); cld(0, 2); cld(0, 3); SCHED(); }
#pragma unroll
        for (int e = 0; e < 8; ++e) {
          if (a < 7 && e < 4) last(a + 1, e);
          z[e >> 1][e & 1] = __builtin_amdgcn_exp2f(z[e >> 1][e & 1]);
          SCHED();
        }
#pragma unroll
        for (int e = 0; e < 4; ++e) z[e] = z[e] * kr[m] + kr[m];
        SCHED();
        if (a == 7) { cld(0, 4); cld(0, 5); cld(0, 6); cld(0, 7); SCHED(); }
#pragma unroll
        for (int e = 0; e < 8; ++e) {
          if (a < 7 && e < 4) corr(a + 1, e);
          z[e >> 1][e & 1] = __builtin_amdgcn_rcpf(z[e >> 1][e & 1]);
          SCHED();
        }
        u32x4 hv;
#pragma unroll
        for (int e = 0; e < 4; ++e) { const f32x2 hq = gu[e] * z[e]; hv[e] = pk(hq.x, hq.y); }
        if (EXP & 32) asm volatile("" :: "v"(hv)); else st16_sc1(rws, hv, vh + 1024 * a);
#define FLAGA 6
#define B8AT 1
        if (a == B8AT) BARRIER();
        if (a == 1) STAMP_NW(20);
        if (a == 3) STAMP_NW(21);
        if (a == FLAGA) {
          STAMP_NW(22);
          asm volatile("s_waitcnt vmcnt(%0)" :: "i"(FLAGA - 3) : "memory");
          if (lane < 16) st8_sc1(rws, tag, lane * 8, FL_2 + ((2 * mi + tg) * 28 + 2 * fj + fq) * FS);
          STAMP_NW(18);
        }
#define GATEA 6
        if (a == GATEA) post(sm + S_GP + 4 * v, 2);
        if (a == 5) hseen = peek(hpw);
        if (a == 6) await(hpw, hseen, 2);
        SCHED();
      }
      STAMP_NW(23); STAMP_NWEND();
      STAMP_AT(3);
    }
    f32x4 y[8];
    const u32 vo = (64 * ti2 + 8 * v + (lane >> 3)) * (D * 2) + n2 * 128 + (lane & 7) * 16;
    u32x4 xres[2];
    LAS char* const pw = sm + S_GP + 4 * (v ^ 1);
    u32 pseen, vzs, zsrc; LAS char *exw, *exr; u32x2 zo[4];
    STAMP_AT(4);
#pragma unroll
    for (int a = 0; a < 4; ++a) {
      auto cm = [&](int n, int i, int j) {
        const bf16x8 af = __builtin_shufflevector(A[a & 1][2 * i], A[a & 1][2 * i + 1], 0, 1, 2, 3, 4, 5, 6, 7);
        if (a == 0 && j - i <= 1 && j >= i) asm volatile("v_mfma_f32_16x16x32_bf16 %0, %1, %2, 0" : "=&v"(y[i + j]) : "v"(af), "v"(B[a & 1][j]));
        else if ((EXP & 16) && a) asm volatile("" : "+v"(y[i + j]) : "v"(af), "v"(B[a & 1][j]));
        else asm volatile("v_mfma_f32_16x16x32_bf16 %0, %1, %2, %0" : "+v"(y[i + j]) : "v"(af), "v"(B[a & 1][j]));
        SCHED();
        if (a < 3 && n >= 4 && n < 28) cld1(a + 1, n - 4);
        if (a == 3) {
          if (n == 4) { exw = sm + (L_K + (4 * c + ah) * KCP + 16 * lane); PIN(exw); }
          if (n == 8) { exr = sm + (L_K + (4 * c + (ah ^ 1)) * KCP + 16 * lane); PIN(exr); }
          if (n == 12) { zsrc = 64 * p4 + (lane & 60); PIN(zsrc); }
          if (n == 16) { vzs = WS_ZT + ch * 4096 + 2048 * ah + 8 * lane; PIN(vzs); }
          if (n == 20) pseen = peek(pw);
        }
        SCHED();
      };
      cm(0, 0, 0); cm(1, 0, 1); cm(2, 1, 1); cm(3, 1, 0); cm(4, 0, 2); cm(5, 1, 2); cm(6, 2, 2); cm(7, 2, 0); cm(8, 2, 1);
      cm(9, 0, 3); cm(10, 1, 3); cm(11, 2, 3); cm(12, 3, 3); cm(13, 3, 0); cm(14, 3, 1); cm(15, 3, 2); cm(16, 0, 4); cm(17, 1, 4);
      cm(18, 2, 4); cm(19, 3, 4); cm(20, 4, 0); cm(21, 4, 1); cm(22, 4, 2); cm(23, 4, 3); cm(24, 0, 5); cm(25, 1, 5); cm(26, 2, 5);
      cm(27, 5, 0); cm(28, 5, 1); cm(29, 5, 2); cm(30, 0, 6); cm(31, 1, 6); cm(32, 6, 0); cm(33, 6, 1); cm(34, 0, 7); cm(35, 7, 0);
      if (a == 2) post(sm + S_GP + 4 * v, 3);
#define FLAG_AT 1
      if (a == FLAG_AT) {
        STAMP_AT(5);
        WAIT_V(0);
        if (!aux && lane < 16) st8_sc1(rws, tag, lane * 8, FL_2 + FL_2B + ((2 * mi + c) * 28 + 2 * fj + ah) * FS);
        xres[0] = __builtin_amdgcn_raw_buffer_load_b128(rx, vo, 0, 0);
        xres[1] = __builtin_amdgcn_raw_buffer_load_b128(rx, vo, 32 * D * 2, 0);
        STAMP_AT(6);
      }
    }
    STAMP_AT(7);
    asm volatile("s_nop 7" : "+v"(y[0]), "+v"(y[1]), "+v"(y[2]), "+v"(y[3]), "+v"(y[4]), "+v"(y[5]), "+v"(y[6]), "+v"(y[7]));
    await<0>(pw, pseen, 3);
    auto finish = [&](int send, int keep) {
#pragma unroll
      for (int i = 0; i < 4; ++i) *(LAS f32x4*)(exw + 1024 * i) = y[send + i];
      post(sm + S_GP + 4 * v, 4);
      await<0>(pw, peek(pw), 4);
      f32x4 t[4];
#pragma unroll
      for (int i = 0; i < 4; ++i) t[i] = *(LAS f32x4*)(exr + 1024 * i);
      SCHED();
#pragma unroll
      for (int i = 0; i < 4; ++i) {
        zo[i].x = __builtin_amdgcn_ds_bpermute(zsrc, min(pk(vadd(t[i][0], y[keep + i][0]), vadd(t[i][1], y[keep + i][1])), ~1u));
        zo[i].y = __builtin_amdgcn_ds_bpermute(zsrc, min(pk(vadd(t[i][2], y[keep + i][2]), vadd(t[i][3], y[keep + i][3])), ~1u));
      }
    };
    if (ah) finish(0, 4); else finish(4, 0);
    STAMP_AT(8);
#pragma unroll
    for (int i = 0; i < 4; ++i) {
      if (EXP & 1) asm volatile("" :: "v"(zo[i])); else st8_sc1(rws, zo[i], vzs + 512 * i, 0);
      SCHED();
    }
    STAMP_AT(9);
#define ZSLEEP 8
    u32 vz = WS_ZT + (64 * n2 + 16 * v + (lane >> 3)) * 4096 + 128 * ti2 + (lane & 7) * 16;
    const int tok = 8 * v + (lane >> 3), dc = lane & 7;
    const u32 stg = L_K + (4 * c + 3) * KCP + 2048 * ah;
    LAS char *sw = sm + (stg + 16 * lane), *tr = sm + (stg + q4 * 128 + 32 * g4 + 8 * p4), *rb = sm + (stg - KCP + 32 * lane);
    LAS char* rz = sm + (L_K + (4 * (dc >> 2) + 2) * KCP + 2048 * ((dc >> 1) & 1) + 32 * tok + 16 * (dc & 1));
    u32 t0 = RB + UNIT + UNIT * 2 * (dc >> 2) + tok * 128 + (((2 * (dc & 3)) ^ (tok & 7) ^ (dc >> 2)) << 4), t1 = t0 ^ 16u;
    u32 op0 = vo, op1 = vo + 32 * D * 2;
    const rsrc_t ro = __builtin_amdgcn_make_buffer_rsrc((void*)p.out, 0, L * D * 2, 0x00020000);
    f32x4 s0[2], s1[2];
#pragma unroll
    for (int i = 0; i < 2; ++i) {
      s0[i] = (f32x4){bflo(xres[i][0]), bfhi(xres[i][0]), bflo(xres[i][1]), bfhi(xres[i][1])};
      s1[i] = (f32x4){bflo(xres[i][2]), bfhi(xres[i][2]), bflo(xres[i][3]), bfhi(xres[i][3])};
    }
    asm volatile("" : "+v"(vz), "+v"(sw), "+v"(tr), "+v"(rb), "+v"(rz), "+v"(t0), "+v"(t1), "+v"(op0), "+v"(op1), "+v"(s0[0]), "+v"(s0[1]), "+v"(s1[0]), "+v"(s1[1]));
    if (b == 255 && v == 0) {
#pragma unroll 1
      for (int i = 0; i < 4; ++i) wait_flags<8>(rws, FL_A + (64 * i + lane) * 16, tlo, thi);
      if (lane == 0) st8_sc1(rws, tag, FL_E, 0);
    }
#define ZDELAY 12
    __builtin_amdgcn_s_sleep(ZDELAY);
    await(sy + S_HP, peek(sy + S_HP), 3);
    STAMP_AT(10);
#pragma unroll 1
    for (u32 s = 0; s < SPIN_CAP; ++s) {
      FENCE();
      const u32x4 z0 = ld16_sc1(rws, vz, 0), z1 = ld16_sc1(rws, vz, 8 * 4096);
      *(LAS u32x4*)sw = z0;
      *(LAS u32x4*)(sw + 1024) = z1;
      if (!__any(max(max(max(z0.x, z0.y), max(z0.z, z0.w)), max(max(z1.x, z1.y), max(z1.z, z1.w))) == ~0u)) break;
      __builtin_amdgcn_s_sleep(ZSLEEP);
    }
    STAMP_AT(11);
    const bf16x8 ra = trfrag(tr, tr + 512), rc = trfrag(tr + 1024, tr + 1536);
    *(LAS bf16x8*)rb = ra;
    *(LAS bf16x8*)(rb + 16) = rc;
    STAMP_AT(12);
    BARRIER();
#pragma unroll
    for (int i = 0; i < 2; ++i) {
      const u32x4 z = *(LAS u32x4*)(rz + 1024 * i);
      s0[i] += (f32x4){bflo(z[0]), bfhi(z[0]), bflo(z[1]), bfhi(z[1])};
      s1[i] += (f32x4){bflo(z[2]), bfhi(z[2]), bflo(z[3]), bfhi(z[3])};
    }
    asm volatile("" : "+v"(s0[0]), "+v"(s0[1]), "+v"(s1[0]), "+v"(s1[1]));
    SCHED(); BARRIER(); SCHED();
    STAMP_AT(13);
    f32x4 pa[2][4], pb[2][4];
#pragma unroll
    for (int g = 0; g < 8; ++g) { pa[g >> 2][g & 3] = *(LAS f32x4*)(sm + t0 + 4096 * g); pb[g >> 2][g & 3] = *(LAS f32x4*)(sm + t1 + 4096 * g); }
    SCHED();
#pragma unroll
    for (int i = 0; i < 2; ++i) {
#pragma unroll
      for (int g = 0; g < 4; ++g) { s0[i] += pa[i][g]; s1[i] += pb[i][g]; }
      st16_sc1(ro, (u32x4){pk(s0[i][0], s0[i][1]), pk(s0[i][2], s0[i][3]), pk(s1[i][0], s1[i][1]), pk(s1[i][2], s1[i][3])}, i ? op1 : op0);
      SCHED();
    }
  }
  STAMP_AT(14); STAMP_OUT();
}

#define WSOFF 0
extern "C" void kernel_launch(void* const* d_in, const int* in_sizes, int n_in, void* d_out, int out_size, void* d_ws,
                              size_t ws_size, hipStream_t stream) {
  Params p;
  p.x = (const u16*)d_in[0]; p.are = (const u16*)d_in[1]; p.aim = (const u16*)d_in[2];
  p.bre = (const u16*)d_in[3]; p.bim = (const u16*)d_in[4]; p.cre = (const u16*)d_in[5];
  p.cim = (const u16*)d_in[6]; p.logdt = (const u16*)d_in[7]; p.dskip = (const u16*)d_in[8];
  p.wg = (const u16*)d_in[10]; p.wu = (const u16*)d_in[11]; p.wd = (const u16*)d_in[12];
  p.out = (u16*)d_out; p.ws = (char*)d_ws + ((size_t)WSOFF << 20);
  s4ffn_kernel<<<256, 512, 0, stream>>>(p);
}
```

```cpp
#include <hip/hip_runtime.h>
#include <stdint.h>

typedef unsigned short u16;
typedef unsigned u32;
typedef unsigned long long u64;
typedef __bf16 bf16x2 __attribute__((ext_vector_type(2)));
typedef __bf16 bf16x4 __attribute__((ext_vector_type(4)));
typedef __bf16 bf16x8 __attribute__((ext_vector_type(8)));
typedef float f32x2 __attribute__((ext_vector_type(2)));
typedef float f32x4 __attribute__((ext_vector_type(4)));
typedef float f32x16 __attribute__((ext_vector_type(16)));
typedef u32 u32x2 __attribute__((ext_vector_type(2)));
typedef u32 u32x4 __attribute__((ext_vector_type(4)));
#define LAS __attribute__((address_space(3)))
#define GAS __attribute__((address_space(1)))
#define DI __device__ __forceinline__
typedef __amdgpu_buffer_rsrc_t rsrc_t;

constexpr int L = 2048, D = 512, NM = 128, F = 1792;
constexpr u32 WS_H = 0, WS_U = 8u << 20, WS_ZT = 10u << 20, WS_ST = 14u << 20, WS_CS = WS_ST + 65536, WS_FL = 15u << 20, WS_END = 16u << 20;
constexpr u32 FL_E = WS_FL, FL_1 = WS_FL + 4096, FL_A = WS_FL + 8192, FL_2 = WS_FL + 131072, FL_2B = 131072, FS = 128;
constexpr u32 KCP = 4160, UNIT = 16384, RB = 32768, RING = 8 * UNIT;
constexpr u32 L_K = 0, L_U = 8 * KCP, US = 4608;
constexpr u32 S_GP = 4128, S_HP = 4144;
constexpr u32 L_P = 320, PS = 3840;
constexpr u32 L_NU0 = 15872, L_NUL = L_U + 2 * US;
constexpr u32 L_ST = 0;
constexpr u32 L_CS = KCP;
constexpr u32 CODE_BYTES = 30720;
constexpr u32 SPIN_CAP = 1u << 16;

struct Params {
  const u16 *x, *are, *aim, *bre, *bim, *cre, *cim, *logdt, *dskip, *wg, *wu, *wd;
  u16* out;
  char* ws;
};

#define STAMP_DECL
#define STAMP_AT(k)
#define STAMP_RT(v)
#define STAMP_NW(k)
#define STAMP_NWEND()
#define STAMP_OUT()

DI float bflo(u32 v) { return __builtin_bit_cast(float, v << 16); }
DI float bfhi(u32 v) { return __builtin_bit_cast(float, v & 0xffff0000u); }
DI u32 pk(float a, float b) { f32x2 f = {a, b}; return __builtin_bit_cast(u32, __builtin_convertvector(f, bf16x2)); }
DI float rbf(float a) { return bflo(pk(a, a)); }
DI void dma4(rsrc_t ra, rsrc_t rb, u32 lds, u32 v0, u32 v1, u32 v2, u32 v3, u32 soff) {
  asm volatile("s_mov_b32 m0, %0\n\ts_nop 0\n\tbuffer_load_dwordx4 %1, %5, %7 offen lds\n\tbuffer_load_dwordx4 %2, %5, %7 offen offset:1024 lds\n\t"
               "buffer_load_dwordx4 %3, %6, %7 offen offset:2048 lds\n\tbuffer_load_dwordx4 %4, %6, %7 offen offset:3072 lds"
               :: "s"(lds), "v"(v0), "v"(v1), "v"(v2), "v"(v3), "s"(ra), "s"(rb), "s"(soff));
}
DI void dma4s(rsrc_t r, u32 lds, u32 v0, u32 v1, u32 v2, u32 v3, u32 soff) {
  asm volatile("s_mov_b32 m0, %0\n\ts_nop 0\n\tbuffer_load_dwordx4 %1, %5, %6 offen sc1 lds\n\tbuffer_load_dwordx4 %2, %5, %6 offen offset:1024 sc1 lds\n\t"
               "buffer_load_dwordx4 %3, %5, %6 offen offset:2048 sc1 lds\n\tbuffer_load_dwordx4 %4, %5, %6 offen offset:3072 sc1 lds"
               :: "s"(lds), "v"(v0), "v"(v1), "v"(v2), "v"(v3), "s"(r), "s"(soff));
}
DI void dma1s(rsrc_t r, u32 lds, u32 v0, u32 soff) {
  asm volatile("s_mov_b32 m0, %0\n\ts_nop 0\n\tbuffer_load_dwordx4 %1, %2, %3 offen sc1 lds" :: "s"(lds), "v"(v0), "s"(r), "s"(soff));
}
#define WAIT_V(n) asm volatile("s_waitcnt vmcnt(" #n ")" ::: "memory")
#define WAIT_L() asm volatile("s_waitcnt lgkmcnt(0)" ::: "memory")
#define BARRIER() asm volatile("s_waitcnt lgkmcnt(0)\n\ts_barrier" ::: "memory")
#define FENCE() asm volatile("" ::: "memory")
#define SCHED() __builtin_amdgcn_sched_barrier(0)
DI bf16x8 ldfrag(LAS char* a) { return *(LAS bf16x8*)a; }
DI bf16x4 tr4(LAS char* a) { return __builtin_amdgcn_ds_read_tr16_b64_v4bf16((LAS bf16x4*)a); }
DI bf16x8 cat(bf16x4 lo, bf16x4 hi) { return __builtin_shufflevector(lo, hi, 0, 1, 2, 3, 4, 5, 6, 7); }
DI bf16x8 trfrag(LAS char* a0, LAS char* a1) {
  bf16x4 lo = __builtin_amdgcn_ds_read_tr16_b64_v4bf16((LAS bf16x4*)a0);
  bf16x4 hi = __builtin_amdgcn_ds_read_tr16_b64_v4bf16((LAS bf16x4*)a1);
  return __builtin_shufflevector(lo, hi, 0, 1, 2, 3, 4, 5, 6, 7);
}
#define MFMA32(a, b, c) __builtin_amdgcn_mfma_f32_32x32x16_bf16(a, b, c, 0, 0, 0)
#define MFMA16_0(d, a, b) asm volatile("v_mfma_f32_16x16x32_bf16 %0, %1, %2, 0" : "=&v"(d) : "v"(a), "v"(b))
#define MFMA16_I(d, a, b) asm volatile("v_mfma_f32_16x16x32_bf16 %0, %1, %2, %0" : "+v"(d) : "v"(a), "v"(b))
#define MFMA16(a, b, c) __builtin_amdgcn_mfma_f32_16x16x32_bf16(a, b, c, 0, 0, 0)
DI u32x2 ld8_sc1(rsrc_t r, u32 voff, u32 soff) { return __builtin_amdgcn_raw_buffer_load_b64(r, voff, soff, 16); }
DI u32x4 ld16_sc1(rsrc_t r, u32 voff, u32 soff) { return __builtin_amdgcn_raw_buffer_load_b128(r, voff, soff, 16); }
DI void st8_sc1(rsrc_t r, u32x2 v, u32 voff, u32 soff) { __builtin_amdgcn_raw_buffer_store_b64(v, r, voff, soff, 16); }
DI void st16_sc1(rsrc_t r, u32x4 v, u32 voff) { __builtin_amdgcn_raw_buffer_store_b128(v, r, voff, 0, 16); }
template <int SLEEP> DI void wait_flags(rsrc_t r, u32 voff, u32 tlo, u32 thi) {
#pragma unroll 1
  for (u32 s = 0; s < SPIN_CAP; ++s) {
    FENCE();
    u32x2 f = ld8_sc1(r, voff, 0);
    if (__all(f.x == tlo && f.y == thi)) break;
    __builtin_amdgcn_s_sleep(SLEEP);
  }
  WAIT_V(0);
}
DI void post(LAS char* word, u32 v) {
  asm volatile("s_mov_b64 exec, 1\n\tds_write_b32 %0, %1\n\ts_mov_b64 exec, -1" :: "v"(word), "v"(v) : "memory");
}
DI u32 peek(LAS char* word) { return *(volatile LAS u32*)word; }
template <int SLEEP = 1> DI void await(LAS char* word, u32 seen, u32 need) {
  if (__builtin_expect(__any(seen < need), 0)) {
#pragma unroll 1
    for (u32 s = 0; s < SPIN_CAP * 16; ++s) { __builtin_amdgcn_s_sleep(SLEEP); if (!__any(peek(word) < need)) break; }
  }
  FENCE();
}
#define DPPADD(name, ctl) DI float name(float x) { float r; asm("s_nop 1\n\tv_add_f32_dpp %0, %1, %1 " ctl " row_mask:0xf bank_mask:0xf" : "=v"(r) : "v"(x)); return r; }
DPPADD(add_x1, "quad_perm:[1,0,3,2]") DPPADD(add_x2, "quad_perm:[2,3,0,1]") DPPADD(add_m8, "row_half_mirror") DPPADD(add_x8, "row_ror:8") DPPADD(add_r4, "row_ror:4")
DI float fold32(float a, float b) {
  asm("s_nop 1\n\tv_permlane32_swap_b32 %0, %1\n\tv_add_f32 %0, %0, %1" : "+v"(a), "+v"(b)); return a;
}
DI float fold16(float a, float b) {
  asm("s_nop 1\n\tv_permlane16_swap_b32 %0, %1\n\tv_add_f32 %0, %0, %1" : "+v"(a), "+v"(b)); return a;
}
DI u32 vmov(u32 a) { u32 r; asm("v_mov_b32 %0, %1" : "=v"(r) : "v"(a)); return r; }
DI float vadd(float a, float b) { float r; asm("v_add_f32 %0, %1, %2" : "=v"(r) : "v"(a), "v"(b)); return r; }
#define PIN(x) asm volatile("" : "+v"(x))
struct cpx { float r, i; };
struct cp2 { f32x2 r, i; };
DI cpx cmul(cpx a, cpx b) { return {a.r * b.r - a.i * b.i, a.r * b.i + a.i * b.r}; }
DI cpx csq(cpx a) { return {a.r * a.r - a.i * a.i, (a.r + a.r) * a.i}; }
DI cp2 cmul2(cp2 a, cpx b) { return {a.r * b.r - a.i * b.i, a.i * b.r + a.r * b.i}; }
DI cpx cexpl(float ar2, float air, float l) {
  float m = __builtin_amdgcn_exp2f(ar2 * l), f = __builtin_amdgcn_fractf(air * l);
  return {m * __builtin_amdgcn_cosf(f), m * __builtin_amdgcn_sinf(f)};
}

#define EXP 0
__global__ __launch_bounds__(512, 2) void s4ffn_kernel(Params p) {
  __shared__ __attribute__((aligned(16))) char smem[163840];
  LAS char* sm = (LAS char*)smem;
  const u32 smb = (u32)(size_t)sm;
  const int tid = threadIdx.x, lane = tid & 63;
  const int w = __builtin_amdgcn_readfirstlane(tid >> 6), v = w & 3;
  const int b = blockIdx.x, xcd = b & 7, o = b >> 3;
  if ((w >> 1) == 1) {
    u64 pc; u32 d;
    asm volatile("s_getpc_b64 %0" : "=s"(pc));
    const u32 off = (2 * o + (w & 1)) * 512;
    asm volatile(".set pf_off, 0\n\t.rept 8\n\ts_load_dword %0, %1, pf_off\n\t.set pf_off, pf_off + 64\n\t.endr\n\ts_waitcnt lgkmcnt(0)"
                 : "=&s"(d) : "s"((pc & ~255ull) + (off < CODE_BYTES ? off : 0)));
  }
  const bool aux = o >= 28;
  const int ma = xcd >> 1, fh = xcd & 1;
  const int il = o / 7, jl = o - il * 7;
  const int mi = 4 * ma + il, fj = 7 * fh + jl;
  const int ti2 = 4 * xcd + (o >> 3), n2 = o & 7;
  const int ch0 = 64 * xcd + 2 * o;
  const int hh = lane >> 5, blk = (lane >> 4) & 1, q4 = (lane >> 2) & 3, p4 = lane & 3, g4 = lane >> 4;
  STAMP_DECL; STAMP_RT(rt0_); STAMP_AT(0);
#define DESCRIPTORS() const rsrc_t rx = __builtin_amdgcn_make_buffer_rsrc((void*)p.x, 0, L * D * 2, 0x00020000), \
  rws = __builtin_amdgcn_make_buffer_rsrc((void*)p.ws, 0, WS_END, 0x00020000)
  u64 ev;
#define LOAD_EPOCH() asm volatile("s_load_dwordx2 %0, %1, 0\n\ts_waitcnt lgkmcnt(0)" : "=&s"(ev) : "s"(p.ws + FL_E) : "memory")
#define MAKE_TAG() const u32 tlo = (u32)(ev + 0x9E3779B97F4A7C15ull), thi = (u32)((ev + 0x9E3779B97F4A7C15ull) >> 32), tg32 = tlo ^ thi; const u32x2 tag = {tlo, thi}
  LAS char* const sy = sm + 4 * p4;
  if (w == 3 && lane < 2) *(LAS u32x4*)(sm + S_GP + 16 * lane) = (u32x4)(0u);

  if (__builtin_expect(aux, 0)) {
    DESCRIPTORS();
    const int ab = 8 * ma + 4 * fh + o - 28, rb = 8 * ab + w;
    u32x4 xr[8];
#pragma unroll
    for (int i = 0; i < 8; ++i) xr[i] = __builtin_amdgcn_raw_buffer_load_b128(rx, lane * 16 + 1024 * (i & 3), (8 * rb + (i & 4)) * 1024, 0);
    LOAD_EPOCH();
    MAKE_TAG();
          u32 one = 0x3f803f80u;
          asm("" : "+v"(one));
          float su[8], sq[8];
#pragma unroll
          for (int r = 0; r < 8; ++r) {
            su[r] = sq[r] = 0.f;
#pragma unroll
            for (int e = 0; e < 4; ++e) {
              const u32 d = xr[r][e];
              su[r] = __builtin_amdgcn_fdot2_f32_bf16(__builtin_bit_cast(bf16x2, d), __builtin_bit_cast(bf16x2, one), su[r], false);
              sq[r] = __builtin_amdgcn_fdot2_f32_bf16(__builtin_bit_cast(bf16x2, d), __builtin_bit_cast(bf16x2, d), sq[r], false);
            }
          }
          asm volatile("s_nop 2" : "+v"(su[0]), "+v"(su[1]), "+v"(su[2]), "+v"(su[3]), "+v"(su[4]), "+v"(su[5]), "+v"(su[6]), "+v"(su[7]),
                                   "+v"(sq[0]), "+v"(sq[1]), "+v"(sq[2]), "+v"(sq[3]), "+v"(sq[4]), "+v"(sq[5]), "+v"(sq[6]), "+v"(sq[7]));
          auto total = [&](const float (&a)[8]) {
            const float lo = add_x8(fold16(fold32(a[0], a[2]), fold32(a[1], a[3]))), hi = add_x8(fold16(fold32(a[4], a[6]), fold32(a[5], a[7])));
            return add_m8(add_x2(add_x1(lane & 8 ? hi : lo)));
          };
          const float mean = total(su) * (1.f / D), nsq = -mean * mean;
          const float var = rbf(__builtin_fmaxf(__builtin_fmaf(total(sq), 1.f / D, nsq), 0.f));
          const u32 gran = pk(mean, __builtin_amdgcn_sqrtf(rbf(var + 1e-5f)));
          const float r = __builtin_amdgcn_rcpf(bfhi(gran)), mr = -bflo(gran) * r;
          u32 n[8][4];
#pragma unroll
          for (int i = 0; i < 4; ++i) {
            const int l0 = 16 * (2 * i & 3) + 8 * (i >> 1);
            auto at = [&](float t, int l) { return __builtin_bit_cast(float, __builtin_amdgcn_readlane(__builtin_bit_cast(int, t), l)); };
            const f32x2 r2 = {at(r, l0), at(r, l0 + 16)}, m2 = {at(mr, l0), at(mr, l0 + 16)};
#pragma unroll
            for (int e = 0; e < 4; ++e) {
              const u32 a = xr[2 * i][e], c = xr[2 * i + 1][e];
              const f32x2 lo = (f32x2){bflo(a), bflo(c)} * r2 + m2, hi = (f32x2){bfhi(a), bfhi(c)} * r2 + m2;
              n[2 * e][i] = pk(lo.x, lo.y); n[2 * e + 1][i] = pk(hi.x, hi.y);
            }
          }
    if ((lane & 7) == 0) st8_sc1(rws, (u32x2){gran, tg32}, 8 * g4 + 4 * (lane & 8), WS_ST + 64 * rb);
#pragma unroll
    for (int e = 0; e < 8; ++e) *(LAS u32x4*)(sm + RB + (8 * lane + e) * 128 + ((w ^ (lane & 7)) << 4)) = (u32x4){n[e][0], n[e][1], n[e][2], n[e][3]};
    STAMP_AT(18);
    BARRIER();
#pragma unroll
    for (int e = 0; e < 8; ++e) {
      const u32 row = 64 * w + 8 * e + (lane >> 3);
      st16_sc1(rws, *(LAS u32x4*)(sm + RB + row * 128 + (((lane & 7) ^ e) << 4)), WS_U + row * 4096 + ab * 128 + (lane & 7) * 16);
    }
    WAIT_V(0);
    if (lane == 0) st8_sc1(rws, tag, FL_1 + (w * 32 + ab) * 8, 0);
    STAMP_AT(19);
  }

  if (w < 4) {
    const int k = v & 1;
    u32 vx0 = (lane >> 3) * 1024 + (((lane & 7) ^ g4) << 4), vx1 = (vx0 ^ 64) + 7168, vx2 = vx0 + 14336, vx3 = vx1 + 14336;
    u32 vg0 = g4 * (F * 2) + (((((lane & 15) >> 1) ^ g4) << 5) | ((lane & 1) << 4)), vg1 = vg0 + 4 * F * 2 - 1024;
    u32 vu0 = vg0 ^ 128, vu1 = vu0 + 4 * F * 2 - 1024;
    u32 sx0 = (mi * 128 + 64 * v) * 1024, sw0 = 16 * v * (F * 2) + fj * 256;
    asm volatile("" : "+v"(vx0), "+v"(vx1), "+v"(vx2), "+v"(vx3), "+v"(vg0), "+v"(vg1), "+v"(vu0), "+v"(vu1));
    asm volatile("" : "+s"(sx0), "+s"(sw0));
    SCHED();
    DESCRIPTORS();
    const rsrc_t rwd = __builtin_amdgcn_make_buffer_rsrc((void*)p.wd, 0, D * F * 2, 0x00020000);
#define ROT2 0
    const int rot2 = ROT2;
    const u32 dq = smb + RB + 4096 * v;
    const u32 vd0 = (lane >> 3) * (D * 2) + (((lane & 7) ^ (g4 << 1)) << 4), vd1 = vd0 + 7168, vd2 = vd0 + 14336, vd3 = vd0 + 21504;
    auto ks_of = [&](int q) { int r = rot2; if (!__builtin_constant_p(r)) asm volatile("" : "+s"(r)); int j = q % 7 + r; j = j >= 7 ? j - 7 : j; return 28 * v + 4 * j + 2 * (q / 7); };
    const u32 sd0 = 28 * v * (16 * D * 2) + n2 * 128;
    auto wdq = [&](int q, int slot) {
      u32 so = sd0;
      asm volatile("" : "+s"(so));
      dma4(rwd, rwd, dq + slot * UNIT, vd0, vd1, vd2, vd3, so + (ks_of(q) - 28 * v) * (16 * D * 2));
    };
    const u32 wo0 = RB + 4096 * v + hh * 1024 + (4 * blk + q4) * 128 + ((2 * blk + (q4 >> 1)) << 5) + p4 * 8;
    auto whalf = [&](int slot, int ct, int t) { return tr4(sm + (wo0 ^ (ct << 5)) + slot * (int)UNIT + 2048 * t); };
    bf16x8 wr_[7][4];
    auto take = [&](int q, int slot) {
#pragma unroll
      for (int e = 0; e < 4; ++e) wr_[q][e] = cat(whalf(slot, e, 0), whalf(slot, e, 1));
    };
    auto last7 = [&] {
    wdq(7, 1);
    {
      const u32 r = lane < 24 ? lane : 0, j = 4 + (r >> 3) + rot2, ks = 4 * (j >= 7 ? j - 7 : j);
      asm volatile("s_mov_b32 m0, %0\n\ts_nop 0\n\tbuffer_load_dword %1, %2, %3 offen lds" :: "s"(smb + L_NUL), "v"((16 * ks + (r & 7)) * (D * 2)), "s"(rwd), "s"((28 * v + 2) * (16 * D * 2) + (ti2 & 3) * 8192 + n2 * 128));
    }
    };
    u32x4 kc[4][4];
    u64 fbad = 0;
    if (v < 2 && !aux) {
        const rsrc_t rwg = __builtin_amdgcn_make_buffer_rsrc((void*)p.wg, 0, D * F * 2, 0x00020000);
        const rsrc_t rwu2 = __builtin_amdgcn_make_buffer_rsrc((void*)((const char*)p.wu - 2048), 0, D * F * 2 + 2048, 0x00020000);
        u32 dst = 8192 * v;
        auto ux = [&](u32 c) {
          const u32 s = sx0 + ((2 * ma + c) & 7) * 128;
          dma4(rx, rx, smb + RB + dst, vx0, vx1, vx2, vx3, s); dma4(rx, rx, smb + RB + dst + 4096, vx0, vx1, vx2, vx3, s + 32768);
          dst = (dst + UNIT) & (RING - 1);
        };
        auto uw = [&](u32 c, u32 h) {
          const u32 s = sw0 + (2 * ((2 * ma + c) & 7) + h) * (32 * F * 2);
          dma4(rwg, rwu2, smb + RB + dst, vg0, vg1, vg0, vg1, s); dma4(rwg, rwu2, smb + RB + dst + 4096, vu0, vu1, vu0, vu1, s + 8 * F * 2);
          dst = (dst + UNIT) & (RING - 1);
        };
      dst += 2 * UNIT;
      LOAD_EPOCH();
      uw(0, 1); ux(1); uw(1, 0);
#pragma unroll
      for (int c = 0; c < 7; ++c) {
        if (c) { uw(c, 1); ux(c + 1); WAIT_V(16); take(c - 1, -1); uw(c + 1, 0); WAIT_L(); SCHED(); }
        wdq(c, -1);
        WAIT_V(20);
        if (c == 0) STAMP_AT(1);
        __builtin_amdgcn_s_barrier();
      }
      uw(7, 1);
      last7();
      WAIT_V(5);
    } else {
      const bool wrk = v >= 2, csm = wrk && !aux;
      const int mat = il >> 1, r8 = (lane >> 2) & 7;
      LAS char* const cp = sm + RB + UNIT + hh * 4096 + mat * 2048 + r8 * 256 + (((4 * (il & 1) + 2 * k + (p4 >> 1)) ^ ((r8 & 3) + 4 * hh)) << 5) + (lane & 1) * 16;
      const u32 vf1 = (hh ? FL_A + 512 * n2 + 256 * k : FL_1 + 256 * xcd) + (lane & 31) * 8;
      auto stats = [&] {
        dma4s(rws, smb + L_CS + 4096 * k, lane * 16, lane * 16, lane * 16, lane * 16, WS_CS + (fj * 2 + k) * 4096);
        if (k) dma1s(rws, smb + L_ST, lane * 16, WS_ST + mi * 1024);
      };
      float s[8]; u32x4 ct[8]; u32x2 f1; u32 dsk;
      auto cadd = [&](int i) {
#pragma unroll
        for (int e = 0; e < 4; ++e) {
          if (i == 0) { s[2 * e] = bflo(ct[0][e]); s[2 * e + 1] = bfhi(ct[0][e]); }
          else asm("v_add_f32 %0, %0, %2\n\tv_add_f32 %1, %1, %3" : "+v"(s[2 * e]), "+v"(s[2 * e + 1]) : "v"(bflo(ct[i][e])), "v"(bfhi(ct[i][e])));
        }
      };
#define TCH 3
#define TDELAY 4
      dsk = p.dskip[ch0 + k];
      if (TDELAY) __builtin_amdgcn_s_sleep(TDELAY);
      if ((TCH & 1) && v == 2) asm volatile("s_mov_b32 m0, %0\n\ts_nop 0\n\tbuffer_load_dword %1, %2, %3 offen lds" :: "s"(smb + L_NU0), "v"(lane < 56 ? lane * 128 : 0), "s"(rwd), "s"(b * 7168));
      if ((TCH & 2) && v == 3 && !aux) {
        const rsrc_t rw = __builtin_amdgcn_make_buffer_rsrc((void*)(mat ? p.wu : p.wg), 0, D * F * 2, 0x00020000);
        asm volatile("s_mov_b32 m0, %0\n\ts_nop 0\n\tbuffer_load_dword %1, %2, %3 offen lds" :: "s"(smb + L_NU0), "v"((lane & 31) * (F * 2)), "s"(rw), "s"((128 * ma + 96) * (F * 2) + fj * 256 + (il & 1) * 128));
      }
      if (!aux) LOAD_EPOCH();
      MAKE_TAG();
#define WDELAY 8
      __builtin_amdgcn_s_sleep(WDELAY);
      wdq(0, -1);
#define CDELAY 0
      if (wrk) {
        __builtin_amdgcn_s_sleep(CDELAY);
#pragma unroll
        for (int i = 0; i < 4; ++i) st16_sc1(rws, (u32x4)(~0u), WS_ZT + (ch0 + k) * 4096 + lane * 16 + 1024 * i);
      }
#pragma unroll
      for (int c = 0; c < 7; ++c) {
        if (c) {
          if ((c == 2 && wrk) || (c == 3 && csm)) WAIT_V(1); else if (c == 6 && csm) WAIT_V(5); else WAIT_V(0);
          if (c == 1) PIN(dsk);
          take(c - 1, -1);
          WAIT_L(); SCHED();
        }
        if (!aux || c == 2) __builtin_amdgcn_s_barrier();
        FENCE(); SCHED();
#define KPRIO 1
#define STC 5
        __builtin_amdgcn_s_setprio(KPRIO);
        if (c == 6 && wrk) fbad = __builtin_amdgcn_ballot_w64(f1.x != tlo || f1.y != thi);
        if (c) wdq(c, -1);
        if (c == 6) last7();
        if (c == 1 && wrk && lane == 0) st8_sc1(rws, tag, FL_A + (ch0 + k) * 8, 0);
        if (c == STC && csm) stats();
        if (c == 5 && wrk) f1 = ld8_sc1(rws, vf1, 0);
        if (c == 6 && wrk) {
          if ((u32)fbad) wait_flags<8>(rws, FL_1 + 256 * xcd + (lane & 31) * 8, tlo, thi);
          FENCE();
          dma4s(rws, smb + L_U + US * k + 512, lane * 16, lane * 16, lane * 16, lane * 16, WS_U + (ch0 + k) * 4096);
        }
        __builtin_amdgcn_s_setprio(0);
        SCHED();
        if (c < 2 && csm) {
#pragma unroll
          for (int i = 0; i < 4; ++i) ct[4 * c + i] = *(LAS u32x4*)(cp + 3 * UNIT * c + 8192 * i);
#pragma unroll
          for (int i = 0; i < 4; ++i) cadd(4 * c + i);
          asm volatile("" : "+v"(s[0]), "+v"(s[1]), "+v"(s[2]), "+v"(s[3]), "+v"(s[4]), "+v"(s[5]), "+v"(s[6]), "+v"(s[7]));
        }
        if (c == 3 && wrk) {
        float kv[16];
        LAS char* pa = sm + (k ? L_P + 2 * PS : 0), *pb = sm + L_P + (2 * k + 1) * PS;
#pragma unroll
        for (int g = 0; g < 3; ++g) {
          const f32x4 a = *(LAS f32x4*)(pa + 1024 * g + 16 * lane), d = *(LAS f32x4*)(pb + 1024 * g + 16 * lane);
#pragma unroll
          for (int e = 0; e < 4; ++e) asm("v_add_f32 %0, %1, %2" : "=v"(kv[4 * g + e]) : "v"(a[e]), "v"(d[e]));
          asm("v_add_f32 %0, %1, %2" : "=v"(kv[12 + g]) : "v"(*(LAS float*)(pa + 3072 + 256 * g + 4 * lane)), "v"(*(LAS float*)(pb + 3072 + 256 * g + 4 * lane)));
        }
        const bool odd = lane & 1;
        if (lane == 0) kv[0] += bflo(dsk);
        kc[0][0] = (u32x4){pk(kv[0], kv[1]), pk(kv[2], kv[3]), pk(kv[4], kv[5]), pk(kv[6], kv[7])};
        kc[1][0] = (u32x4){pk(kv[8], kv[8]), pk(kv[8], kv[9]), pk(kv[9], kv[9]), pk(kv[10], kv[10])};
        kc[2][0] = (u32x4){pk(kv[11], kv[11]), pk(kv[11], kv[11]), pk(odd ? kv[12] : kv[11], kv[12]), pk(kv[12], kv[12])};
        kc[3][0] = (u32x4){pk(kv[13], kv[13]), pk(kv[13], kv[13]), pk(odd ? kv[14] : kv[13], kv[14]), pk(kv[14], kv[14])};
          asm volatile("" : "+v"(kc[0][0]), "+v"(kc[1][0]), "+v"(kc[2][0]), "+v"(kc[3][0]));
        }
        if (c == 4 && wrk) {
#pragma unroll
        for (int g = 0; g < 4; ++g) {
          const u32x4 cu = kc[g][0];
          int px = 0, py = 0;
          if (g) {
            px = __builtin_amdgcn_update_dpp(0, (int)kc[g ? g - 1 : 0][0].z, 0x13C, 0xf, 0xf, false);
            py = __builtin_amdgcn_update_dpp(0, (int)kc[g ? g - 1 : 0][0].w, 0x13C, 0xf, 0xf, false);
          }
          px = __builtin_amdgcn_update_dpp(px, (int)cu.z, 0x138, 0xf, 0xf, false);
          py = __builtin_amdgcn_update_dpp(py, (int)cu.w, 0x138, 0xf, 0xf, false);
          const u32 s0 = __builtin_amdgcn_alignbit(py, px, 16), s1 = __builtin_amdgcn_alignbit(cu.x, py, 16), s2 = __builtin_amdgcn_alignbit(cu.y, cu.x, 16),
                    s3 = __builtin_amdgcn_alignbit(cu.z, cu.y, 16), s4 = __builtin_amdgcn_alignbit(cu.w, cu.z, 16);
          kc[g][1] = (u32x4){s1, s2, s3, s4}; kc[g][2] = (u32x4){(u32)py, vmov(cu.x), vmov(cu.y), vmov(cu.z)}; kc[g][3] = (u32x4){s0, vmov(s1), vmov(s2), vmov(s3)};
          asm volatile("" : "+v"(kc[g][0]), "+v"(kc[g][1]), "+v"(kc[g][2]), "+v"(kc[g][3]));
        }
          STAMP_AT(2);
        }
        if (c == 2 && csm) {
          const float lo = add_x8(add_r4(fold16(fold32(s[0], s[4]), fold32(s[2], s[6])))), hi = add_x8(add_r4(fold16(fold32(s[1], s[5]), fold32(s[3], s[7]))));
          s[0] = lane & 8 ? hi : lo;
          const float th = rbf(s[0]);
          if (!(lane & 4)) st8_sc1(rws, (u32x2){pk(th, s[0] - th), tg32}, 8 * (8 * p4 + 4 * hh + 2 * blk + ((lane >> 3) & 1)), WS_CS + (((fj * 2 + mat) * 4 + ma) * 128 + 64 * (il & 1) + 32 * k) * 8);
        }
        if (c == STC + 1 && wrk && !aux) {
#pragma unroll 1
        for (u32 s = 0; s < SPIN_CAP; ++s) {
          u32 d = 0;
#pragma unroll
          for (int i = 0; i < 5; ++i) {
            const u32x4 t = *(LAS u32x4*)(sm + (i < 4 || !k ? L_CS + 4096 * k + 1024 * (i & 3) : L_ST) + 16 * lane);
            d |= (t.y ^ tg32) | (t.w ^ tg32);
          }
          if (!__any(d != 0)) break;
          stats();
          WAIT_V(0);
        }
        }
      }
    }
    STAMP_AT(4);
    MAKE_TAG();
    STAMP_AT(6);
    BARRIER();
    wdq(8, 2); wdq(9, 3); wdq(10, 4);
    WAIT_V(17); take(6, -1);
    LAS char* kd = sm + L_K + 4 * KCP * k + 16 * lane;
    LAS char* kz = sm + (lane < 32 ? L_U + US * k + 16 * lane : L_K + (4 * k + ((lane >> 1) & 3)) * KCP + 16 * (lane & 1));
    asm volatile("" : "+v"(kd), "+v"(kz));
    u32x4 zero = (u32x4)(0u);
    u32 two = 2;
    LAS char* hpw = sm + S_HP + 4 * v;
    asm volatile("" : "+v"(zero), "+v"(two), "+v"(hpw));
    STAMP_AT(5);
    SCHED();
    BARRIER();
    STAMP_AT(7);
    if (v >= 2) {
      WAIT_V(0);
      *(LAS u32x4*)kz = zero;
#pragma unroll
      for (int e = 0; e < 16; ++e) *(LAS u32x4*)(kd + 32 + KCP * (e & 3) + 1024 * (e >> 2)) = kc[e >> 2][e & 3];
      post(hpw, two);
      WAIT_L();
      if (fbad >> 32) wait_flags<8>(rws, FL_A + 512 * n2 + 256 * k + (lane & 31) * 8, tlo, thi);
    } else __builtin_amdgcn_s_sleep(8);
    STAMP_AT(8);
#define LATEQ 1
#define WPRIO 2
    if (!LATEQ) { wdq(11, 5); wdq(12, 6); wdq(13, 7); }
    __builtin_amdgcn_s_setprio(WPRIO);
#define HDELAY 0
#define AUXSLEEP 32
    if (!aux) { await<4>(sy + S_GP, peek(sy + S_GP), 2); __builtin_amdgcn_s_sleep(HDELAY); }
    else __builtin_amdgcn_s_sleep(AUXSLEEP);
    if (LATEQ == 1) { __builtin_amdgcn_s_setprio(0); wdq(11, 5); wdq(12, 6); wdq(13, 7); }
    STAMP_AT(9);
    const u32 vf2 = FL_2 + (ti2 * 28 + 7 * v + (lane < 7 ? lane : 0)) * FS;
#define APOLL 1
    if (APOLL) {
#pragma unroll 1
      for (u32 s = 0; s < SPIN_CAP; ++s) {
        u32x2 f = tag;
        if (lane < 7) { f = (u32x2)(0u); asm volatile("buffer_atomic_or_x2 %0, %1, %2, 0 offen sc0 sc1\n\ts_waitcnt vmcnt(0)" : "+v"(f) : "v"(vf2), "s"(rws) : "memory"); }
        if ((EXP & 4) || __all(f.x == tlo && f.y == thi)) break;
        __builtin_amdgcn_s_sleep(2);
      }
      FENCE();
    } else wait_flags<8>(rws, vf2, tlo, thi);
    __builtin_amdgcn_s_setprio(0);
    STAMP_AT(10);
    f32x4 acc[4][4];
#define PD 12
#define ZAT 8
    u32x4 hf[56];
    u32 hs;
    auto hload = [&](int n) { if (!(n & 3)) hs = (ti2 * 112 + ks_of(n >> 2)) * 2048; hf[n] = ld16_sc1(rws, lane * 16 + 1024 * (n & 3), hs); };
#pragma unroll
    for (int n = 0; n < PD; ++n) hload(n);
    bf16x4 wl[2][8];
    u32x2 fb; u32 pw_[4], wb_[4];
    SCHED();
#pragma unroll
    for (int i = 0; i < 14; ++i) {
#pragma unroll
      for (int n = 0; n < 16; ++n) {
        const int m = n >> 2, ct = m & 1 ? 3 - (n & 3) : n & 3, nx = 4 * i + m + PD;
        const bf16x8 a = i < 7 ? wr_[i < 7 ? i : 0][ct] : cat(wl[i & 1][2 * ct], wl[i & 1][2 * ct + 1]);
        const bf16x8 bb = __builtin_bit_cast(bf16x8, hf[4 * i + m]);
        if (i == 0) MFMA16_0(acc[ct][m], a, bb); else MFMA16_I(acc[ct][m], a, bb);
        SCHED();
#define LG 2
        if ((n & 3) == LG && nx < 56) hload(nx);
        if ((n & 3) != LG && n < 11 && i >= 6 && i < 13) {
          const int e = n - (n + 3 - LG) / 4;
          if (e < 8) wl[(i + 1) & 1][e] = tr4(sm + wb_[e >> 1] + (i < 9 ? i - 5 : i - 9) * (int)UNIT + 2048 * (e & 1));
        }
        if ((i == 5 || i == 8) && n >= 12) { wb_[n & 3] = (wo0 ^ ((n & 3) << 5)) + (i == 8 ? 4 * UNIT : 0); PIN(wb_[n & 3]); }
        if ((n & 3) == (LG + 3) % 4) {
          if (nx == 20) fb = ld8_sc1(rws, vf2, FL_2B);
          if (nx == 28) {
            if (__builtin_expect(!__all(fb.x == tlo && fb.y == thi), 0)) wait_flags<4>(rws, vf2 + FL_2B, tlo, thi);
            FENCE();
          }
          if (i == ZAT && m == 3) post(sm + S_HP + 4 * v, 3);
          if (i == 13) { pw_[m] = (RB + UNIT + 4096 * v + (lane & 15) * 128 + ((g4 ^ (lane & 7)) << 4)) ^ ((m & 1) << 6) ^ ((m >> 1) << 4); PIN(pw_[m]); }
        }
        SCHED();
      }
      if (i == 3) STAMP_AT(15);
      if (i == 6) STAMP_AT(16);
      if (i == 10) STAMP_AT(17);
    }
    STAMP_AT(11);
#define BAT 8
    asm volatile("s_nop 7" : "+v"(acc[0][3]), "+v"(acc[1][3]), "+v"(acc[2][3]), "+v"(acc[3][3]));
#pragma unroll
    for (int a = 0; a < 16; ++a) {
      const int ct = a & 3, m = a >> 2;
      if (a == BAT) { SCHED(); __builtin_amdgcn_s_barrier(); SCHED(); }
      *(LAS f32x4*)(sm + pw_[ct] + UNIT * (2 * (ct >> 1) + (m >> 1)) + 2048 * (m & 1)) = acc[ct][m];
    }
    STAMP_AT(12);
    BARRIER();
  } else {
    __builtin_amdgcn_s_setprio(1);
    const int c = v >> 1, ah = v & 1, ch = ch0 + c;
    u32 gi = (ch * NM + 64 * ah + lane) * 2, gd = ch * 2;
    u32 fx0 = (lane >> 3) * 1024 + (((lane & 7) ^ g4) << 4), fx1 = (fx0 ^ 64) + 7168, fx2 = fx0 + 14336, fx3 = fx1 + 14336;
    u32 fw0 = (g4 * (F * 2) + (((((lane & 15) >> 1) ^ g4) << 5) | ((lane & 1) << 4))) ^ (ah << 7), fw1 = fw0 + 4 * F * 2 - 1024;
    u32 fsx = aux ? 0x40000000u : (mi * 128 + 32 * v) * 1024 + 2 * ma * 128, fsw = aux ? 0x40000000u : (128 * ma + 8 * v) * (F * 2) + fj * 256;
    u32 fd0 = smb + RB + 4096 * v + (aux ? 4 * UNIT : 0), fd1 = fd0 + (aux ? 0 : UNIT);
    asm volatile("" : "+v"(gi), "+v"(gd), "+v"(fx0), "+v"(fx1), "+v"(fx2), "+v"(fx3), "+v"(fw0), "+v"(fw1));
    asm volatile("" : "+s"(fsx), "+s"(fsw), "+s"(fd0), "+s"(fd1));
    SCHED();
    u32 r0, r1, r2, r3, r4, r5, r6;
    asm volatile("global_load_ushort %6, %8, %15\n\tglobal_load_ushort %0, %7, %9\n\tglobal_load_ushort %1, %7, %10\n\tglobal_load_ushort %2, %7, %11\n\t"
                 "global_load_ushort %3, %7, %12\n\tglobal_load_ushort %4, %7, %13\n\tglobal_load_ushort %5, %7, %14"
                 : "=&v"(r0), "=&v"(r1), "=&v"(r2), "=&v"(r3), "=&v"(r4), "=&v"(r5), "=&v"(r6)
                 : "v"(gi), "v"(gd), "s"(p.are), "s"(p.aim), "s"(p.bre), "s"(p.bim), "s"(p.cre), "s"(p.cim), "s"(p.logdt));
    SCHED();
    DESCRIPTORS();
    {
#define LDELAY 1
      __builtin_amdgcn_s_sleep(LDELAY);
      dma4(rx, rx, fd0, fx0, fx1, fx2, fx3, fsx);
      dma4(__builtin_amdgcn_make_buffer_rsrc((void*)p.wg, 0, D * F * 2, 0x00020000),
           __builtin_amdgcn_make_buffer_rsrc((void*)((const char*)p.wu - 2048), 0, D * F * 2 + 2048, 0x00020000), fd1, fw0, fw1, fw0, fw1, fsw);
    }
    SCHED();
    STAMP_AT(2);
    const int tg = c, fq = ah;
    u32 xa[2], wa[4], iw[4], ir[2];
    {
      const u32 row = 64 * tg + (lane & 15), f = (row >> 1) & 7, scr = RB + 5 * UNIT + 4096 * v, cki = 2 * blk + (p4 >> 1);
#pragma unroll
      for (int s = 0; s < 4; ++s) {
        if (s < 2) xa[s] = RB + row * 128 + (((4 * s + g4) ^ f) << 4);
        wa[s] = RB + g4 * 4096 + q4 * 256 + (((4 * fq + s) ^ (q4 + 4 * blk)) << 5) + p4 * 8;
        iw[s] = scr + lane * 64 + ((s ^ ((lane >> 1) & 3)) << 4);
        asm volatile("" : "+v"(xa[s & 1]), "+v"(wa[s]), "+v"(iw[s]));
      }
      ir[0] = scr + (8 * hh + q4) * 64 + ((cki ^ (q4 >> 1)) << 4) + 8 * (p4 & 1);
      ir[1] = scr + (8 * hh + 4 + q4) * 64 + ((cki ^ (2 + (q4 >> 1))) << 4) + 8 * (p4 & 1);
      asm volatile("" : "+v"(ir[0]), "+v"(ir[1]));
    }
    LAS char* ka = sm + L_K + (4 * c + q4) * KCP + 2 * (28 + 128 * ah + 4 * p4 - 8 * g4);
    LAS char* ua = sm + L_U + US * c + 2 * (240 - 96 - 128 * ah + 16 * (lane & 15) + 8 * g4);
    LAS char *hpw = sm + S_HP + 8 + 4 * (lane & 1), *kp4 = sm + ((v ? L_P + PS * v : 0) + 16 * lane), *kp1 = sm + ((v ? L_P + PS * v : 0) + 3072 + 4 * lane);
    asm volatile("" : "+v"(ka), "+v"(ua), "+v"(hpw), "+v"(kp4), "+v"(kp1));
    u64 ev2;
    asm volatile("s_load_dwordx2 %0, %1, 0" : "=&s"(ev2) : "s"(p.ws + FL_E) : "memory");
    SCHED();
    STAMP_AT(15);
    asm volatile("s_waitcnt vmcnt(8)" : "+v"(r0), "+v"(r1), "+v"(r2), "+v"(r3), "+v"(r4), "+v"(r5), "+v"(r6));
    STAMP_AT(16);
    bf16x8 yf[8], xf[8];
    {
      const float dt = rbf(__builtin_amdgcn_exp2f(bflo(r6) * 1.44269504088896f));
      const float ar2 = dt * bflo(r0) * 1.44269504088896f, air = dt * bflo(r1) * 0.159154943091895f;
      auto put = [&](int img, int g, u32x4 re, u32x4 im) {
        if (EXP & 8) return;
        *(LAS u32x4*)(sm + iw[g] + 2 * img * UNIT) = re; *(LAS u32x4*)(sm + iw[g] + (1 - img) * UNIT) = im;
        SCHED();
      };
      auto get = [&](int img, int s) { const u32 d = (s < 4 ? 2 * img : 1 - img) * UNIT + 1024 * (s & 3); return trfrag(sm + ir[0] + d, sm + ir[1] + d); };
#define P2_(a) pk((a).x, (a).y)
#define P4_(a, b, c, d) (u32x4){P2_(a), P2_(b), P2_(c), P2_(d)}
      const cpx e8 = cexpl(ar2, air, 8.f);
      {
        const cpx s1 = {e8.r, -e8.i}, s2 = csq(s1), s4 = csq(s2), s8 = cexpl(ar2, -air, 64.f), s16 = csq(s8);
        const cp2 y0 = {{1.f, s1.r}, {0.f, s1.i}}, y1 = cmul2(y0, s2), y2 = cmul2(y0, s4), y3 = cmul2(y1, s4);
        SCHED();
        asm volatile("s_waitcnt lgkmcnt(0)" : "+s"(ev2) :: "memory");
        if (!aux) ev = ev2;
        put(0, 0, P4_(y0.r, y1.r, y2.r, y3.r), P4_(y0.i, y1.i, y2.i, y3.i));
        const cp2 y4 = cmul2(y0, s8), y5 = cmul2(y1, s8), y6 = cmul2(y2, s8), y7 = cmul2(y3, s8);
        put(0, 1, P4_(y4.r, y5.r, y6.r, y7.r), P4_(y4.i, y5.i, y6.i, y7.i));
        const cp2 y8 = cmul2(y0, s16), y9 = cmul2(y1, s16), ya = cmul2(y2, s16), yb = cmul2(y3, s16);
        put(0, 2, P4_(y8.r, y9.r, ya.r, yb.r), P4_(y8.i, y9.i, ya.i, yb.i));
        const cp2 yc = cmul2(y4, s16), yd = cmul2(y5, s16), ye = cmul2(y6, s16), yg = cmul2(y7, s16);
        put(0, 3, P4_(yc.r, yd.r, ye.r, yg.r), P4_(yc.i, yd.i, ye.i, yg.i));
      }
      {
        const cpx cf = cmul({bflo(r4), bflo(r5)}, {dt * bflo(r2), dt * bflo(r3)});
        const cpx e1 = cexpl(ar2, air, 1.f), e256 = cexpl(ar2, air, 256.f);
        const cpx e2 = csq(e1), e4 = csq(e2), e512 = csq(e256), e1024 = csq(e512);
        const cp2 cb = cmul2({{e1.r, e256.r}, {e1.i, e256.i}}, cf);
        const cp2 a0 = {{cf.r, cb.r.x}, {cf.i, cb.i.x}}, a1 = cmul2(a0, e2);
        const cp2 t0 = cmul2({{e2.r, e4.r}, {e2.i, e4.i}}, {cb.r.y, cb.i.y});
#define XA(m) (u32x4){P2_(a0.m), P2_(a1.m), pk(cb.m.y, cb.m.y), P2_(t0.m)}
#pragma unroll
        for (int s = 0; s < 4; ++s) yf[s] = get(0, s);
        put(1, 0, XA(r), XA(i));
        const cp2 a2 = cmul2(a0, e4), a3 = cmul2(a1, e4), t1 = cmul2(t0, e4);
#define XB(m) (u32x4){P2_(a2.m), P2_(a3.m), pk(t0.m.y, t0.m.y), P2_(t1.m)}
        put(1, 1, XB(r), XB(i));
#pragma unroll
        for (int s = 4; s < 8; ++s) yf[s] = get(0, s);
        const cp2 q0 = {{cf.r, cb.r.y}, {cf.i, cb.i.y}}, q1 = cmul2(q0, e512), q2 = cmul2(q0, e1024);
        const cp2 u4 = cmul2(q1, e4), u8 = cmul2(q1, e8);
#define XC(m) (u32x4){pk(q1.m.x, u4.m.x), pk(u8.m.x, q2.m.x), pk(q1.m.y, u4.m.y), pk(u8.m.y, q2.m.y)}
        put(1, 2, XC(r), XC(i));
        const cp2 q3 = cmul2(q1, e1024), v8 = cmul2(q2, e8), w8 = cmul2(q3, e8);
#define XD(m) (u32x4){pk(v8.m.x, q3.m.x), pk(w8.m.x, 0.f), pk(v8.m.y, q3.m.y), pk(w8.m.y, 0.f)}
        put(1, 3, XD(r), XD(i));
#pragma unroll
        for (int s = 0; s < 8; ++s) xf[s] = get(1, s);
      }

      STAMP_AT(17);
    }
    f32x16 kacc = (f32x16)(0.f);
#pragma unroll
    for (int s = 0; s < 8; ++s) kacc = MFMA32(xf[s], yf[s], kacc);
    auto kstore = [&](int g) {
      if (g < 3) *(LAS f32x4*)(kp4 + 1024 * g) = (f32x4){kacc[4 * g], kacc[4 * g + 1], kacc[4 * g + 2], kacc[4 * g + 3]};
      else { *(LAS float*)kp1 = kacc[12]; *(LAS float*)(kp1 + 256) = kacc[13]; *(LAS float*)(kp1 + 512) = kacc[14]; }
    };
    kstore(0); kstore(1); kstore(2); kstore(3);
    WAIT_V(0);
    STAMP_AT(1);
    bf16x8 X0[4]; bf16x4 W0[16];
    BARRIER();
    MAKE_TAG();
    W0[0] = tr4(sm + UNIT + wa[0]); W0[1] = tr4(sm + UNIT + wa[0] + 1024);
#pragma unroll
    for (int t = 0; t < 4; ++t) X0[t] = ldfrag(sm + xa[0] + 2048 * t);
#pragma unroll
    for (int t = 2; t < 16; ++t) W0[t] = tr4(sm + UNIT + wa[t >> 2] + 1024 * (t & 3));
    SCHED();
    bf16x4 A[2][16]; bf16x8 B[2][8];
    auto cld1 = [&](int a, int n) {
      const int i = n / 3, r = n % 3;
      if (r < 2) A[a & 1][2 * i + r] = __builtin_amdgcn_ds_read_tr16_b64_v4bf16((LAS bf16x4*)(ka + 64 * a + 512 * i + 8 - 8 * r));
      else B[a & 1][i] = ldfrag(ua + 64 * (3 - a) + 512 * i);
    };
    auto cld = [&](int a, int i) { cld1(a, 3 * i); cld1(a, 3 * i + 1); cld1(a, 3 * i + 2); };
    if (aux) { BARRIER(); BARRIER();
      await(hpw, 0, 2);
#pragma unroll
      for (int i = 0; i < 8; ++i) cld(0, i);
    }
    else {
      f32x4 acc[4][8];
      bf16x8 X1[4]; bf16x4 W1[16];
      auto step = [&](const bf16x8 (&Xc)[4], const bf16x4 (&Wc)[16], bf16x8 (&Xn)[4], bf16x4 (&Wn)[16], u32 xb, u32 wb, int s, bool first, auto mid) {
        u32 xp, wp;
#pragma unroll
        for (int n = 0; n < 32; ++n) {
          const int t = n >> 2, r = n & 3, m = t & 1 ? 3 - r : r;
          const bf16x8 wf = cat(Wc[2 * t], Wc[2 * t + 1]);
          if (first) MFMA16_0(acc[m][t], wf, Xc[m]); else MFMA16_I(acc[m][t], wf, Xc[m]);
          SCHED();
          mid(n);
          if (n == 1) { xp = xb + xa[s]; PIN(xp); }
          if (n == 2 || n == 8 || n == 15 || n == 22) { wp = wb + wa[n / 7]; PIN(wp); }
          if (n >= 4 && n < 8) Wn[n - 4] = tr4(sm + wp + 1024 * (n - 4));
          if (n >= 9 && n < 13) Wn[n - 5] = tr4(sm + wp + 1024 * (n - 9));
          if (n == 13 || n == 14) Xn[n - 13] = ldfrag(sm + xp + 2048 * (n - 13));
          if (n >= 16 && n < 20) Wn[n - 8] = tr4(sm + wp + 1024 * (n - 16));
          if (n == 20 || n == 21) Xn[n - 18] = ldfrag(sm + xp + 2048 * (n - 18));
          if (n >= 23 && n < 27) Wn[n - 11] = tr4(sm + wp + 1024 * (n - 23));
          SCHED();
        }
      };
      u32 xb = 0;
      step(X0, W0, X1, W1, xb, 2 * UNIT, 1, true, [&](int) {});
#pragma unroll 1
      for (int cc = 0; cc < 6; ++cc) {
        xb = (xb + 3 * UNIT) & (RING - 1);
        step(X1, W1, X0, W0, xb, (xb + UNIT) & (RING - 1), 0, false, [&](int n) { if (n == 3) BARRIER(); });
        step(X0, W0, X1, W1, xb, (xb + 2 * UNIT) & (RING - 1), 1, false, [&](int) {});
      }
      step(X1, W1, X0, W0, 5 * UNIT, 6 * UNIT, 0, false, [&](int n) { if (n == 3) BARRIER(); });
      u32 st[4]; u32x2 cs[4]; u32x4 XM[4];
      float ka[4], kb[4], kr[4];
      step(X0, W0, X1, W1, 5 * UNIT, 7 * UNIT, 1, false, [&](int n) {
        if (n == 0) { LAS char* a = sm + L_ST + 8 * (64 * tg + (lane & 15)); st[0] = *(LAS u32*)a; st[1] = *(LAS u32*)(a + 128); st[2] = *(LAS u32*)(a + 256); st[3] = *(LAS u32*)(a + 384); }
        if (n >= 27 && n < 31) { LAS char* a = sm + L_CS + 1024 * g4 + 8 * (64 * fq + (lane & 15)) + 128 * (n - 27); cs[n - 27] = (u32x2){*(LAS u32*)a, *(LAS u32*)(a + 4096)}; }
        if (n >= 8) {
          const int t = (n - 8) / 6, r = (n - 8) % 6;
          if (r == 0) { kr[t] = bfhi(st[t]); ka[t] = __builtin_amdgcn_rcpf(kr[t]); PIN(kr[t]); PIN(ka[t]); }
          if (r == 1) { const u32 mm = ((st[t] & 0xffffu) ^ 0x8000u) * 0x10001u; XM[t].x = mm; PIN(XM[t]); }
          if (r == 2) { kb[t] = -2.f * 0.7978845608028654f * 1.44269504088896f * ka[t]; ka[t] *= ka[t]; PIN(kb[t]); PIN(ka[t]); }
          if (r == 3) { ka[t] *= 0.044715f; kr[t] *= kr[t]; PIN(ka[t]); PIN(kr[t]); }
          if (r == 4) { ka[t] *= kb[t]; XM[t].w = XM[t].x; PIN(ka[t]); PIN(XM[t]); }
          if (r == 5) { XM[t].y = 0; XM[t].z = 0; PIN(XM[t]); }
        }
      });
      STAMP_NW(19);
      auto last = [&](int a, int e) { const int t = (a & 4) + e; const bf16x8 wf = cat(W1[2 * t], W1[2 * t + 1]); MFMA16_I(acc[a & 3][t], wf, X1[a & 3]); SCHED(); };
      auto corr = [&](int a, int e) {
        const int t = (a & 4) + e;
        const u32x4 xm = XM[a & 3];
        const u32x2 xh = e & 1 ? (u32x2){xm.z, xm.w} : (u32x2){xm.x, xm.y};
        asm volatile("v_mfma_f32_16x16x16_bf16 %0, %1, %2, %0" : "+v"(acc[a & 3][t]) : "v"(cs[t >> 1]), "v"(xh));
        SCHED();
      };
#pragma unroll
      for (int e = 0; e < 4; ++e) last(0, e);
#pragma unroll
      for (int e = 0; e < 4; ++e) corr(0, e);
      asm volatile("s_nop 7" : "+v"(acc[0][0]), "+v"(acc[0][1]), "+v"(acc[0][2]), "+v"(acc[0][3]));
      u32 hseen;
      const u32 vh = ((2 * mi + tg) * 112 + 8 * fj + 4 * fq) * 2048 + lane * 16;
      SCHED();
#pragma unroll
      for (int a = 0; a < 8; ++a) {
        const int m = a & 3;
        f32x2 z[4], gu[4];
#pragma unroll
        for (int e = 0; e < 4; ++e) {
          const f32x4 gq = acc[m][(a & 4) + (e & 2)], uq = acc[m][(a & 4) + (e & 2) + 1];
          const f32x2 g = {gq[2 * (e & 1)], gq[2 * (e & 1) + 1]}, u = {uq[2 * (e & 1)], uq[2 * (e & 1) + 1]};
          z[e] = (g * g * ka[m] + kb[m]) * g; gu[e] = g * u;
        }
        SCHED();
        if (a == 7) { cld(0, 0); cld(0, 1); cld(0, 2); cld(0, 3); SCHED(); }
#pragma unroll
        for (int e = 0; e < 8; ++e) {
          if (a < 7 && e < 4) last(a + 1, e);
          z[e >> 1][e & 1] = __builtin_amdgcn_exp2f(z[e >> 1][e & 1]);
          SCHED();
        }
#pragma unroll
        for (int e = 0; e < 4; ++e) z[e] = z[e] * kr[m] + kr[m];
        SCHED();
        if (a == 7) { cld(0, 4); cld(0, 5); cld(0, 6); cld(0, 7); SCHED(); }
#pragma unroll
        for (int e = 0; e < 8; ++e) {
          if (a < 7 && e < 4) corr(a + 1, e);
          z[e >> 1][e & 1] = __builtin_amdgcn_rcpf(z[e >> 1][e & 1]);
          SCHED();
        }
        u32x4 hv;
#pragma unroll
        for (int e = 0; e < 4; ++e) { const f32x2 hq = gu[e] * z[e]; hv[e] = pk(hq.x, hq.y); }
        if (EXP & 32) asm volatile("" :: "v"(hv)); else st16_sc1(rws, hv, vh + 1024 * a);
#define FLAGA 6
#define B8AT 1
        if (a == B8AT) BARRIER();
        if (a == 1) STAMP_NW(20);
        if (a == 3) STAMP_NW(21);
        if (a == FLAGA) {
          STAMP_NW(22);
          asm volatile("s_waitcnt vmcnt(%0)" :: "i"(FLAGA - 3) : "memory");
          if (lane < 16) st8_sc1(rws, tag, lane * 8, FL_2 + ((2 * mi + tg) * 28 + 2 * fj + fq) * FS);
          STAMP_NW(18);
        }
#define GATEA 6
        if (a == GATEA) post(sm + S_GP + 4 * v, 2);
        if (a == 5) hseen = peek(hpw);
        if (a == 6) await(hpw, hseen, 2);
        SCHED();
      }
      STAMP_NW(23); STAMP_NWEND();
      STAMP_AT(3);
    }
    f32x4 y[8];
    const u32 vo = (64 * ti2 + 8 * v + (lane >> 3)) * (D * 2) + n2 * 128 + (lane & 7) * 16;
    u32x4 xres[2];
    LAS char* const pw = sm + S_GP + 4 * (v ^ 1);
    u32 pseen, vzs, zsrc; LAS char *exw, *exr; u32x2 zo[4];
    STAMP_AT(4);
#pragma unroll
    for (int a = 0; a < 4; ++a) {
      auto cm = [&](int n, int i, int j) {
        const bf16x8 af = __builtin_shufflevector(A[a & 1][2 * i], A[a & 1][2 * i + 1], 0, 1, 2, 3, 4, 5, 6, 7);
        if (a == 0 && j - i <= 1 && j >= i) asm volatile("v_mfma_f32_16x16x32_bf16 %0, %1, %2, 0" : "=&v"(y[i + j]) : "v"(af), "v"(B[a & 1][j]));
        else if ((EXP & 16) && a) asm volatile("" : "+v"(y[i + j]) : "v"(af), "v"(B[a & 1][j]));
        else asm volatile("v_mfma_f32_16x16x32_bf16 %0, %1, %2, %0" : "+v"(y[i + j]) : "v"(af), "v"(B[a & 1][j]));
        SCHED();
        if (a < 3 && n >= 4 && n < 28) cld1(a + 1, n - 4);
        if (a == 3) {
          if (n == 4) { exw = sm + (L_K + (4 * c + ah) * KCP + 16 * lane); PIN(exw); }
          if (n == 8) { exr = sm + (L_K + (4 * c + (ah ^ 1)) * KCP + 16 * lane); PIN(exr); }
          if (n == 12) { zsrc = 64 * p4 + (lane & 60); PIN(zsrc); }
          if (n == 16) { vzs = WS_ZT + ch * 4096 + 2048 * ah + 8 * lane; PIN(vzs); }
          if (n == 20) pseen = peek(pw);
        }
        SCHED();
      };
      cm(0, 0, 0); cm(1, 0, 1); cm(2, 1, 1); cm(3, 1, 0); cm(4, 0, 2); cm(5, 1, 2); cm(6, 2, 2); cm(7, 2, 0); cm(8, 2, 1);
      cm(9, 0, 3); cm(10, 1, 3); cm(11, 2, 3); cm(12, 3, 3); cm(13, 3, 0); cm(14, 3, 1); cm(15, 3, 2); cm(16, 0, 4); cm(17, 1, 4);
      cm(18, 2, 4); cm(19, 3, 4); cm(20, 4, 0); cm(21, 4, 1); cm(22, 4, 2); cm(23, 4, 3); cm(24, 0, 5); cm(25, 1, 5); cm(26, 2, 5);
      cm(27, 5, 0); cm(28, 5, 1); cm(29, 5, 2); cm(30, 0, 6); cm(31, 1, 6); cm(32, 6, 0); cm(33, 6, 1); cm(34, 0, 7); cm(35, 7, 0);
      if (a == 2) post(sm + S_GP + 4 * v, 3);
#define FLAG_AT 1
      if (a == FLAG_AT) {
        STAMP_AT(5);
        WAIT_V(0);
        if (!aux && lane < 16) st8_sc1(rws, tag, lane * 8, FL_2 + FL_2B + ((2 * mi + c) * 28 + 2 * fj + ah) * FS);
        xres[0] = __builtin_amdgcn_raw_buffer_load_b128(rx, vo, 0, 0);
        xres[1] = __builtin_amdgcn_raw_buffer_load_b128(rx, vo, 32 * D * 2, 0);
        STAMP_AT(6);
      }
    }
    STAMP_AT(7);
    asm volatile("s_nop 7" : "+v"(y[0]), "+v"(y[1]), "+v"(y[2]), "+v"(y[3]), "+v"(y[4]), "+v"(y[5]), "+v"(y[6]), "+v"(y[7]));
    await<0>(pw, pseen, 3);
    auto finish = [&](int send, int keep) {
#pragma unroll
      for (int i = 0; i < 4; ++i) *(LAS f32x4*)(exw + 1024 * i) = y[send + i];
      post(sm + S_GP + 4 * v, 4);
      await<0>(pw, peek(pw), 4);
      f32x4 t[4];
#pragma unroll
      for (int i = 0; i < 4; ++i) t[i] = *(LAS f32x4*)(exr + 1024 * i);
      SCHED();
#pragma unroll
      for (int i = 0; i < 4; ++i) {
        zo[i].x = __builtin_amdgcn_ds_bpermute(zsrc, min(pk(vadd(t[i][0], y[keep + i][0]), vadd(t[i][1], y[keep + i][1])), ~1u));
        zo[i].y = __builtin_amdgcn_ds_bpermute(zsrc, min(pk(vadd(t[i][2], y[keep + i][2]), vadd(t[i][3], y[keep + i][3])), ~1u));
      }
    };
    if (ah) finish(0, 4); else finish(4, 0);
    STAMP_AT(8);
#pragma unroll
    for (int i = 0; i < 4; ++i) {
      if (EXP & 1) asm volatile("" :: "v"(zo[i])); else st8_sc1(rws, zo[i], vzs + 512 * i, 0);
      SCHED();
    }
    STAMP_AT(9);
#define ZSLEEP 8
    u32 vz = WS_ZT + (64 * n2 + 16 * v + (lane >> 3)) * 4096 + 128 * ti2 + (lane & 7) * 16;
    const int tok = 8 * v + (lane >> 3), dc = lane & 7;
    const u32 stg = L_K + (4 * c + 3) * KCP + 2048 * ah;
    LAS char *sw = sm + (stg + 16 * lane), *tr = sm + (stg + q4 * 128 + 32 * g4 + 8 * p4), *rb = sm + (stg - KCP + 32 * lane);
    LAS char* rz = sm + (L_K + (4 * (dc >> 2) + 2) * KCP + 2048 * ((dc >> 1) & 1) + 32 * tok + 16 * (dc & 1));
    u32 t0 = RB + UNIT + UNIT * 2 * (dc >> 2) + tok * 128 + (((2 * (dc & 3)) ^ (tok & 7) ^ (dc >> 2)) << 4), t1 = t0 ^ 16u;
    u32 op0 = vo, op1 = vo + 32 * D * 2;
    const rsrc_t ro = __builtin_amdgcn_make_buffer_rsrc((void*)p.out, 0, L * D * 2, 0x00020000);
    f32x4 s0[2], s1[2];
#pragma unroll
    for (int i = 0; i < 2; ++i) {
      s0[i] = (f32x4){bflo(xres[i][0]), bfhi(xres[i][0]), bflo(xres[i][1]), bfhi(xres[i][1])};
      s1[i] = (f32x4){bflo(xres[i][2]), bfhi(xres[i][2]), bflo(xres[i][3]), bfhi(xres[i][3])};
    }
    asm volatile("" : "+v"(vz), "+v"(sw), "+v"(tr), "+v"(rb), "+v"(rz), "+v"(t0), "+v"(t1), "+v"(op0), "+v"(op1), "+v"(s0[0]), "+v"(s0[1]), "+v"(s1[0]), "+v"(s1[1]));
    if (b == 255 && v == 0) {
#pragma unroll 1
      for (int i = 0; i < 4; ++i) wait_flags<8>(rws, FL_A + (64 * i + lane) * 16, tlo, thi);
      if (lane == 0) st8_sc1(rws, tag, FL_E, 0);
    }
#define ZDELAY 12
    __builtin_amdgcn_s_sleep(ZDELAY);
    await(sy + S_HP, peek(sy + S_HP), 3);
    STAMP_AT(10);
#pragma unroll 1
    for (u32 s = 0; s < SPIN_CAP; ++s) {
      FENCE();
      const u32x4 z0 = ld16_sc1(rws, vz, 0), z1 = ld16_sc1(rws, vz, 8 * 4096);
      *(LAS u32x4*)sw = z0;
      *(LAS u32x4*)(sw + 1024) = z1;
      if (!__any(max(max(max(z0.x, z0.y), max(z0.z, z0.w)), max(max(z1.x, z1.y), max(z1.z, z1.w))) == ~0u)) break;
      __builtin_amdgcn_s_sleep(ZSLEEP);
    }
    STAMP_AT(11);
    const bf16x8 ra = trfrag(tr, tr + 512), rc = trfrag(tr + 1024, tr + 1536);
    *(LAS bf16x8*)rb = ra;
    *(LAS bf16x8*)(rb + 16) = rc;
    STAMP_AT(12);
    BARRIER();
#pragma unroll
    for (int i = 0; i < 2; ++i) {
      const u32x4 z = *(LAS u32x4*)(rz + 1024 * i);
      s0[i] += (f32x4){bflo(z[0]), bfhi(z[0]), bflo(z[1]), bfhi(z[1])};
      s1[i] += (f32x4){bflo(z[2]), bfhi(z[2]), bflo(z[3]), bfhi(z[3])};
    }
    asm volatile("" : "+v"(s0[0]), "+v"(s0[1]), "+v"(s1[0]), "+v"(s1[1]));
    SCHED(); BARRIER(); SCHED();
    STAMP_AT(13);
    f32x4 pa[2][4], pb[2][4];
#pragma unroll
    for (int g = 0; g < 8; ++g) { pa[g >> 2][g & 3] = *(LAS f32x4*)(sm + t0 + 4096 * g); pb[g >> 2][g & 3] = *(LAS f32x4*)(sm + t1 + 4096 * g); }
    SCHED();
#pragma unroll
    for (int i = 0; i < 2; ++i) {
#pragma unroll
      for (int g = 0; g < 4; ++g) { s0[i] += pa[i][g]; s1[i] += pb[i][g]; }
      st16_sc1(ro, (u32x4){pk(s0[i][0], s0[i][1]), pk(s0[i][2], s0[i][3]), pk(s1[i][0], s1[i][1]), pk(s1[i][2], s1[i][3])}, i ? op1 : op0);
      SCHED();
    }
  }
  STAMP_AT(14); STAMP_OUT();
}

#define WSOFF 0
extern "C" void kernel_launch(void* const* d_in, const int* in_sizes, int n_in, void* d_out, int out_size, void* d_ws,
                              size_t ws_size, hipStream_t stream) {
  Params p;
  p.x = (const u16*)d_in[0]; p.are = (const u16*)d_in[1]; p.aim = (const u16*)d_in[2];
  p.bre = (const u16*)d_in[3]; p.bim = (const u16*)d_in[4]; p.cre = (const u16*)d_in[5];
  p.cim = (const u16*)d_in[6]; p.logdt = (const u16*)d_in[7]; p.dskip = (const u16*)d_in[8];
  p.wg = (const u16*)d_in[10]; p.wu = (const u16*)d_in[11]; p.wd = (const u16*)d_in[12];
  p.out = (u16*)d_out; p.ws = (char*)d_ws + ((size_t)WSOFF << 20);
  s4ffn_kernel<<<256, 512, 0, stream>>>(p);
}
```

```cpp
#include <hip/hip_runtime.h>
#include <stdint.h>

typedef unsigned short u16;
typedef unsigned u32;
typedef unsigned long long u64;
typedef __bf16 bf16x2 __attribute__((ext_vector_type(2)));
typedef __bf16 bf16x4 __attribute__((ext_vector_type(4)));
typedef __bf16 bf16x8 __attribute__((ext_vector_type(8)));
typedef float f32x2 __attribute__((ext_vector_type(2)));
typedef float f32x4 __attribute__((ext_vector_type(4)));
typedef float f32x16 __attribute__((ext_vector_type(16)));
typedef u32 u32x2 __attribute__((ext_vector_type(2)));
typedef u32 u32x4 __attribute__((ext_vector_type(4)));
#define LAS __attribute__((address_space(3)))
#define GAS __attribute__((address_space(1)))
#define DI __device__ __forceinline__
typedef __amdgpu_buffer_rsrc_t rsrc_t;

constexpr int L = 2048, D = 512, NM = 128, F = 1792;
constexpr u32 WS_H = 0, WS_U = 8u << 20, WS_ZT = 10u << 20, WS_ST = 14u << 20, WS_CS = WS_ST + 65536, WS_FL = 15u << 20, WS_END = 16u << 20;
constexpr u32 FL_E = WS_FL, FL_1 = WS_FL + 4096, FL_A = WS_FL + 8192, FL_2 = WS_FL + 131072, FL_2B = 131072, FS = 128;
constexpr u32 KCP = 4160, UNIT = 16384, RB = 32768, RING = 8 * UNIT;
constexpr u32 L_K = 0, L_U = 8 * KCP, US = 4608;
constexpr u32 S_GP = 4128, S_HP = 4144;
constexpr u32 L_P = 320, PS = 3840;
constexpr u32 L_NU0 = 15872, L_NUL = L_U + 2 * US;
constexpr u32 L_ST = 0;
constexpr u32 L_CS = KCP;
constexpr u32 CODE_BYTES = 30720;
constexpr u32 SPIN_CAP = 1u << 16;

struct Params {
  const u16 *x, *are, *aim, *bre, *bim, *cre, *cim, *logdt, *dskip, *wg, *wu, *wd;
  u16* out;
  char* ws;
};

#define STAMP_DECL
#define STAMP_AT(k)
#define STAMP_RT(v)
#define STAMP_NW(k)
#define STAMP_NWEND()
#define STAMP_OUT()

DI float bflo(u32 v) { return __builtin_bit_cast(float, v << 16); }
DI float bfhi(u32 v) { return __builtin_bit_cast(float, v & 0xffff0000u); }
DI u32 pk(float a, float b) { f32x2 f = {a, b}; return __builtin_bit_cast(u32, __builtin_convertvector(f, bf16x2)); }
DI float rbf(float a) { return bflo(pk(a, a)); }
DI void dma4(rsrc_t ra, rsrc_t rb, u32 lds, u32 v0, u32 v1, u32 v2, u32 v3, u32 soff) {
  asm volatile("s_mov_b32 m0, %0\n\ts_nop 0\n\tbuffer_load_dwordx4 %1, %5, %7 offen lds\n\tbuffer_load_dwordx4 %2, %5, %7 offen offset:1024 lds\n\t"
               "buffer_load_dwordx4 %3, %6, %7 offen offset:2048 lds\n\tbuffer_load_dwordx4 %4, %6, %7 offen offset:3072 lds"
               :: "s"(lds), "v"(v0), "v"(v1), "v"(v2), "v"(v3), "s"(ra), "s"(rb), "s"(soff));
}
DI void dma4s(rsrc_t r, u32 lds, u32 v0, u32 v1, u32 v2, u32 v3, u32 soff) {
  asm volatile("s_mov_b32 m0, %0\n\ts_nop 0\n\tbuffer_load_dwordx4 %1, %5, %6 offen sc1 lds\n\tbuffer_load_dwordx4 %2, %5, %6 offen offset:1024 sc1 lds\n\t"
               "buffer_load_dwordx4 %3, %5, %6 offen offset:2048 sc1 lds\n\tbuffer_load_dwordx4 %4, %5, %6 offen offset:3072 sc1 lds"
               :: "s"(lds), "v"(v0), "v"(v1), "v"(v2), "v"(v3), "s"(r), "s"(soff));
}
DI void dma1s(rsrc_t r, u32 lds, u32 v0, u32 soff) {
  asm volatile("s_mov_b32 m0, %0\n\ts_nop 0\n\tbuffer_load_dwordx4 %1, %2, %3 offen sc1 lds" :: "s"(lds), "v"(v0), "s"(r), "s"(soff));
}
#define WAIT_V(n) asm volatile("s_waitcnt vmcnt(" #n ")" ::: "memory")
#define WAIT_L() asm volatile("s_waitcnt lgkmcnt(0)" ::: "memory")
#define BARRIER() asm volatile("s_waitcnt lgkmcnt(0)\n\ts_barrier" ::: "memory")
#define FENCE() asm volatile("" ::: "memory")
#define SCHED() __builtin_amdgcn_sched_barrier(0)
DI bf16x8 ldfrag(LAS char* a) { return *(LAS bf16x8*)a; }
DI bf16x4 tr4(LAS char* a) { return __builtin_amdgcn_ds_read_tr16_b64_v4bf16((LAS bf16x4*)a); }
DI bf16x8 cat(bf16x4 lo, bf16x4 hi) { return __builtin_shufflevector(lo, hi, 0, 1, 2, 3, 4, 5, 6, 7); }
DI bf16x8 trfrag(LAS char* a0, LAS char* a1) {
  bf16x4 lo = __builtin_amdgcn_ds_read_tr16_b64_v4bf16((LAS bf16x4*)a0);
  bf16x4 hi = __builtin_amdgcn_ds_read_tr16_b64_v4bf16((LAS bf16x4*)a1);
  return __builtin_shufflevector(lo, hi, 0, 1, 2, 3, 4, 5, 6, 7);
}
#define MFMA32(a, b, c) __builtin_amdgcn_mfma_f32_32x32x16_bf16(a, b, c, 0, 0, 0)
#define MFMA16_0(d, a, b) asm volatile("v_mfma_f32_16x16x32_bf16 %0, %1, %2, 0" : "=&v"(d) : "v"(a), "v"(b))
#define MFMA16_I(d, a, b) asm volatile("v_mfma_f32_16x16x32_bf16 %0, %1, %2, %0" : "+v"(d) : "v"(a), "v"(b))
#define MFMA16(a, b, c) __builtin_amdgcn_mfma_f32_16x16x32_bf16(a, b, c, 0, 0, 0)
DI u32x2 ld8_sc1(rsrc_t r, u32 voff, u32 soff) { return __builtin_amdgcn_raw_buffer_load_b64(r, voff, soff, 16); }
DI u32x4 ld16_sc1(rsrc_t r, u32 voff, u32 soff) { return __builtin_amdgcn_raw_buffer_load_b128(r, voff, soff, 16); }
DI void st8_sc1(rsrc_t r, u32x2 v, u32 voff, u32 soff) { __builtin_amdgcn_raw_buffer_store_b64(v, r, voff, soff, 16); }
DI void st16_sc1(rsrc_t r, u32x4 v, u32 voff) { __builtin_amdgcn_raw_buffer_store_b128(v, r, voff, 0, 16); }
template <int SLEEP> DI void wait_flags(rsrc_t r, u32 voff, u32 tlo, u32 thi) {
#pragma unroll 1
  for (u32 s = 0; s < SPIN_CAP; ++s) {
    FENCE();
    u32x2 f = ld8_sc1(r, voff, 0);
    if (__all(f.x == tlo && f.y == thi)) break;
    __builtin_amdgcn_s_sleep(SLEEP);
  }
  WAIT_V(0);
}
DI void post(LAS char* word, u32 v) {
  asm volatile("s_mov_b64 exec, 1\n\tds_write_b32 %0, %1\n\ts_mov_b64 exec, -1" :: "v"(word), "v"(v) : "memory");
}
DI u32 peek(LAS char* word) { return *(volatile LAS u32*)word; }
template <int SLEEP = 1> DI void await(LAS char* word, u32 seen, u32 need) {
  if (__builtin_expect(__any(seen < need), 0)) {
#pragma unroll 1
    for (u32 s = 0; s < SPIN_CAP * 16; ++s) { __builtin_amdgcn_s_sleep(SLEEP); if (!__any(peek(word) < need)) break; }
  }
  FENCE();
}
#define DPPADD(name, ctl) DI float name(float x) { float r; asm("s_nop 1\n\tv_add_f32_dpp %0, %1, %1 " ctl " row_mask:0xf bank_mask:0xf" : "=v"(r) : "v"(x)); return r; }
DPPADD(add_x1, "quad_perm:[1,0,3,2]") DPPADD(add_x2, "quad_perm:[2,3,0,1]") DPPADD(add_m8, "row_half_mirror") DPPADD(add_x8, "row_ror:8") DPPADD(add_r4, "row_ror:4")
DI float fold32(float a, float b) {
  asm("s_nop 1\n\tv_permlane32_swap_b32 %0, %1\n\tv_add_f32 %0, %0, %1" : "+v"(a), "+v"(b)); return a;
}
DI float fold16(float a, float b) {
  asm("s_nop 1\n\tv_permlane16_swap_b32 %0, %1\n\tv_add_f32 %0, %0, %1" : "+v"(a), "+v"(b)); return a;
}
DI u32 vmov(u32 a) { u32 r; asm("v_mov_b32 %0, %1" : "=v"(r) : "v"(a)); return r; }
DI float vadd(float a, float b) { float r; asm("v_add_f32 %0, %1, %2" : "=v"(r) : "v"(a), "v"(b)); return r; }
#define PIN(x) asm volatile("" : "+v"(x))
struct cpx { float r, i; };
struct cp2 { f32x2 r, i; };
DI cpx cmul(cpx a, cpx b) { return {a.r * b.r - a.i * b.i, a.r * b.i + a.i * b.r}; }
DI cpx csq(cpx a) { return {a.r * a.r - a.i * a.i, (a.r + a.r) * a.i}; }
DI cp2 cmul2(cp2 a, cpx b) { return {a.r * b.r - a.i * b.i, a.i * b.r + a.r * b.i}; }
DI cpx cexpl(float ar2, float air, float l) {
  float m = __builtin_amdgcn_exp2f(ar2 * l), f = __builtin_amdgcn_fractf(air * l);
  return {m * __builtin_amdgcn_cosf(f), m * __builtin_amdgcn_sinf(f)};
}

#define EXP 0
__global__ __launch_bounds__(512, 2) void s4ffn_kernel(Params p) {
  __shared__ __attribute__((aligned(16))) char smem[163840];
  LAS char* sm = (LAS char*)smem;
  const u32 smb = (u32)(size_t)sm;
  const int tid = threadIdx.x, lane = tid & 63;
  const int w = __builtin_amdgcn_readfirstlane(tid >> 6), v = w & 3;
  const int b = blockIdx.x, xcd = b & 7, o = b >> 3;
  if ((w >> 1) == 1) {
    u64 pc; u32 d;
    asm volatile("s_getpc_b64 %0" : "=s"(pc));
    const u32 off = (2 * o + (w & 1)) * 512;
    asm volatile(".set pf_off, 0\n\t.rept 8\n\ts_load_dword %0, %1, pf_off\n\t.set pf_off, pf_off + 64\n\t.endr\n\ts_waitcnt lgkmcnt(0)"
                 : "=&s"(d) : "s"((pc & ~255ull) + (off < CODE_BYTES ? off : 0)));
  }
  const bool aux = o >= 28;
  const int ma = xcd >> 1, fh = xcd & 1;
  const int il = o / 7, jl = o - il * 7;
  const int mi = 4 * ma + il, fj = 7 * fh + jl;
  const int ti2 = 4 * xcd + (o >> 3), n2 = o & 7;
  const int ch0 = 64 * xcd + 2 * o;
  const int hh = lane >> 5, blk = (lane >> 4) & 1, q4 = (lane >> 2) & 3, p4 = lane & 3, g4 = lane >> 4;
  STAMP_DECL; STAMP_RT(rt0_); STAMP_AT(0);
#define DESCRIPTORS() const rsrc_t rx = __builtin_amdgcn_make_buffer_rsrc((void*)p.x, 0, L * D * 2, 0x00020000), \
  rws = __builtin_amdgcn_make_buffer_rsrc((void*)p.ws, 0, WS_END, 0x00020000)
  u64 ev;
#define LOAD_EPOCH() asm volatile("s_load_dwordx2 %0, %1, 0\n\ts_waitcnt lgkmcnt(0)" : "=&s"(ev) : "s"(p.ws + FL_E) : "memory")
#define MAKE_TAG() const u32 tlo = (u32)(ev + 0x9E3779B97F4A7C15ull), thi = (u32)((ev + 0x9E3779B97F4A7C15ull) >> 32), tg32 = tlo ^ thi; const u32x2 tag = {tlo, thi}
  LAS char* const sy = sm + 4 * p4;
  if (w == 3 && lane < 2) *(LAS u32x4*)(sm + S_GP + 16 * lane) = (u32x4)(0u);

  if (__builtin_expect(aux, 0)) {
    DESCRIPTORS();
    const int ab = 8 * ma + 4 * fh + o - 28, rb = 8 * ab + w;
    u32x4 xr[8];
#pragma unroll
    for (int i = 0; i < 8; ++i) xr[i] = __builtin_amdgcn_raw_buffer_load_b128(rx, lane * 16 + 1024 * (i & 3), (8 * rb + (i & 4)) * 1024, 0);
    LOAD_EPOCH();
    MAKE_TAG();
          u32 one = 0x3f803f80u;
          asm("" : "+v"(one));
          float su[8], sq[8];
#pragma unroll
          for (int r = 0; r < 8; ++r) {
            su[r] = sq[r] = 0.f;
#pragma unroll
            for (int e = 0; e < 4; ++e) {
              const u32 d = xr[r][e];
              su[r] = __builtin_amdgcn_fdot2_f32_bf16(__builtin_bit_cast(bf16x2, d), __builtin_bit_cast(bf16x2, one), su[r], false);
              sq[r] = __builtin_amdgcn_fdot2_f32_bf16(__builtin_bit_cast(bf16x2, d), __builtin_bit_cast(bf16x2, d), sq[r], false);
            }
          }
          asm volatile("s_nop 2" : "+v"(su[0]), "+v"(su[1]), "+v"(su[2]), "+v"(su[3]), "+v"(su[4]), "+v"(su[5]), "+v"(su[6]), "+v"(su[7]),
                                   "+v"(sq[0]), "+v"(sq[1]), "+v"(sq[2]), "+v"(sq[3]), "+v"(sq[4]), "+v"(sq[5]), "+v"(sq[6]), "+v"(sq[7]));
          auto total = [&](const float (&a)[8]) {
            const float lo = add_x8(fold16(fold32(a[0], a[2]), fold32(a[1], a[3]))), hi = add_x8(fold16(fold32(a[4], a[6]), fold32(a[5], a[7])));
            return add_m8(add_x2(add_x1(lane & 8 ? hi : lo)));
          };
          const float mean = total(su) * (1.f / D), nsq = -mean * mean;
          const float var = rbf(__builtin_fmaxf(__builtin_fmaf(total(sq), 1.f / D, nsq), 0.f));
          const u32 gran = pk(mean, __builtin_amdgcn_sqrtf(rbf(var + 1e-5f)));
          const float r = __builtin_amdgcn_rcpf(bfhi(gran)), mr = -bflo(gran) * r;
          u32 n[8][4];
#pragma unroll
          for (int i = 0; i < 4; ++i) {
            const int l0 = 16 * (2 * i & 3) + 8 * (i >> 1);
            auto at = [&](float t, int l) { return __builtin_bit_cast(float, __builtin_amdgcn_readlane(__builtin_bit_cast(int, t), l)); };
            const f32x2 r2 = {at(r, l0), at(r, l0 + 16)}, m2 = {at(mr, l0), at(mr, l0 + 16)};
#pragma unroll
            for (int e = 0; e < 4; ++e) {
              const u32 a = xr[2 * i][e], c = xr[2 * i + 1][e];
              const f32x2 lo = (f32x2){bflo(a), bflo(c)} * r2 + m2, hi = (f32x2){bfhi(a), bfhi(c)} * r2 + m2;
              n[2 * e][i] = pk(lo.x, lo.y); n[2 * e + 1][i] = pk(hi.x, hi.y);
            }
          }
    if ((lane & 7) == 0) st8_sc1(rws, (u32x2){gran, tg32}, 8 * g4 + 4 * (lane & 8), WS_ST + 64 * rb);
#pragma unroll
    for (int e = 0; e < 8; ++e) *(LAS u32x4*)(sm + RB + (8 * lane + e) * 128 + ((w ^ (lane & 7)) << 4)) = (u32x4){n[e][0], n[e][1], n[e][2], n[e][3]};
    STAMP_AT(18);
    BARRIER();
#pragma unroll
    for (int e = 0; e < 8; ++e) {
      const u32 row = 64 * w + 8 * e + (lane >> 3);
      st16_sc1(rws, *(LAS u32x4*)(sm + RB + row * 128 + (((lane & 7) ^ e) << 4)), WS_U + row * 4096 + ab * 128 + (lane & 7) * 16);
    }
    WAIT_V(0);
    if (lane == 0) st8_sc1(rws, tag, FL_1 + (w * 32 + ab) * 8, 0);
    STAMP_AT(19);
  }

  if (w < 4) {
    const int k = v & 1;
    u32 vx0 = (lane >> 3) * 1024 + (((lane & 7) ^ g4) << 4), vx1 = (vx0 ^ 64) + 7168, vx2 = vx0 + 14336, vx3 = vx1 + 14336;
    u32 vg0 = g4 * (F * 2) + (((((lane & 15) >> 1) ^ g4) << 5) | ((lane & 1) << 4)), vg1 = vg0 + 4 * F * 2 - 1024;
    u32 vu0 = vg0 ^ 128, vu1 = vu0 + 4 * F * 2 - 1024;
    u32 sx0 = (mi * 128 + 64 * v) * 1024, sw0 = 16 * v * (F * 2) + fj * 256;
    asm volatile("" : "+v"(vx0), "+v"(vx1), "+v"(vx2), "+v"(vx3), "+v"(vg0), "+v"(vg1), "+v"(vu0), "+v"(vu1));
    asm volatile("" : "+s"(sx0), "+s"(sw0));
    SCHED();
    DESCRIPTORS();
    const rsrc_t rwd = __builtin_amdgcn_make_buffer_rsrc((void*)p.wd, 0, D * F * 2, 0x00020000);
#define ROT2 0
    const int rot2 = ROT2;
    const u32 dq = smb + RB + 4096 * v;
    const u32 vd0 = (lane >> 3) * (D * 2) + (((lane & 7) ^ (g4 << 1)) << 4), vd1 = vd0 + 7168, vd2 = vd0 + 14336, vd3 = vd0 + 21504;
    auto ks_of = [&](int q) { int r = rot2; if (!__builtin_constant_p(r)) asm volatile("" : "+s"(r)); int j = q % 7 + r; j = j >= 7 ? j - 7 : j; return 28 * v + 4 * j + 2 * (q / 7); };
    const u32 sd0 = 28 * v * (16 * D * 2) + n2 * 128;
    auto wdq = [&](int q, int slot) {
      u32 so = sd0;
      asm volatile("" : "+s"(so));
      dma4(rwd, rwd, dq + slot * UNIT, vd0, vd1, vd2, vd3, so + (ks_of(q) - 28 * v) * (16 * D * 2));
    };
    const u32 wo0 = RB + 4096 * v + hh * 1024 + (4 * blk + q4) * 128 + ((2 * blk + (q4 >> 1)) << 5) + p4 * 8;
    auto whalf = [&](int slot, int ct, int t) { return tr4(sm + (wo0 ^ (ct << 5)) + slot * (int)UNIT + 2048 * t); };
    bf16x8 wr_[7][4];
    auto take = [&](int q, int slot) {
#pragma unroll
      for (int e = 0; e < 4; ++e) wr_[q][e] = cat(whalf(slot, e, 0), whalf(slot, e, 1));
    };
    auto last7 = [&] {
    wdq(7, 1);
    {
      const u32 r = lane < 24 ? lane : 0, j = 4 + (r >> 3) + rot2, ks = 4 * (j >= 7 ? j - 7 : j);
      asm volatile("s_mov_b32 m0, %0\n\ts_nop 0\n\tbuffer_load_dword %1, %2, %3 offen lds" :: "s"(smb + L_NUL), "v"((16 * ks + (r & 7)) * (D * 2)), "s"(rwd), "s"((28 * v + 2) * (16 * D * 2) + (ti2 & 3) * 8192 + n2 * 128));
    }
    };
    u32x4 kc[4][4];
    if (v < 2 && !aux) {
        const rsrc_t rwg = __builtin_amdgcn_make_buffer_rsrc((void*)p.wg, 0, D * F * 2, 0x00020000);
        const rsrc_t rwu2 = __builtin_amdgcn_make_buffer_rsrc((void*)((const char*)p.wu - 2048), 0, D * F * 2 + 2048, 0x00020000);
        u32 dst = 8192 * v;
        auto ux = [&](u32 c) {
          const u32 s = sx0 + ((2 * ma + c) & 7) * 128;
          dma4(rx, rx, smb + RB + dst, vx0, vx1, vx2, vx3, s); dma4(rx, rx, smb + RB + dst + 4096, vx0, vx1, vx2, vx3, s + 32768);
          dst = (dst + UNIT) & (RING - 1);
        };
        auto uw = [&](u32 c, u32 h) {
          const u32 s = sw0 + (2 * ((2 * ma + c) & 7) + h) * (32 * F * 2);
          dma4(rwg, rwu2, smb + RB + dst, vg0, vg1, vg0, vg1, s); dma4(rwg, rwu2, smb + RB + dst + 4096, vu0, vu1, vu0, vu1, s + 8 * F * 2);
          dst = (dst + UNIT) & (RING - 1);
        };
#define LDELAY 4
      __builtin_amdgcn_s_sleep(LDELAY);
      ux(0); uw(0, 0);
      LOAD_EPOCH();
      uw(0, 1); ux(1); uw(1, 0);
#pragma unroll
      for (int c = 0; c < 7; ++c) {
        if (c) { uw(c, 1); ux(c + 1); WAIT_V(16); take(c - 1, -1); uw(c + 1, 0); WAIT_L(); SCHED(); }
        wdq(c, -1);
        WAIT_V(20);
        if (c == 0) STAMP_AT(1);
        __builtin_amdgcn_s_barrier();
      }
      uw(7, 1);
      last7();
      WAIT_V(5);
    } else {
      const bool wrk = v >= 2, csm = wrk && !aux;
      const int mat = il >> 1, r8 = (lane >> 2) & 7;
      LAS char* const cp = sm + RB + UNIT + hh * 4096 + mat * 2048 + r8 * 256 + (((4 * (il & 1) + 2 * k + (p4 >> 1)) ^ ((r8 & 3) + 4 * hh)) << 5) + (lane & 1) * 16;
      const u32 vf1 = (hh ? FL_A + 512 * n2 + 256 * k : FL_1 + 256 * xcd) + (lane & 31) * 8;
      auto stats = [&] {
        dma4s(rws, smb + L_CS + 4096 * k, lane * 16, lane * 16, lane * 16, lane * 16, WS_CS + (fj * 2 + k) * 4096);
        if (k) dma1s(rws, smb + L_ST, lane * 16, WS_ST + mi * 1024);
      };
      float s[8]; u32x4 ct[8]; u32x2 f1; u32 dsk; bool f1bad;
      auto cadd = [&](int i) {
#pragma unroll
        for (int e = 0; e < 4; ++e) {
          if (i == 0) { s[2 * e] = bflo(ct[0][e]); s[2 * e + 1] = bfhi(ct[0][e]); }
          else asm("v_add_f32 %0, %0, %2\n\tv_add_f32 %1, %1, %3" : "+v"(s[2 * e]), "+v"(s[2 * e + 1]) : "v"(bflo(ct[i][e])), "v"(bfhi(ct[i][e])));
        }
      };
#define TCH 3
#define TDELAY 4
      dsk = p.dskip[ch0 + k];
      if (TDELAY) __builtin_amdgcn_s_sleep(TDELAY);
      if ((TCH & 1) && v == 2) asm volatile("s_mov_b32 m0, %0\n\ts_nop 0\n\tbuffer_load_dword %1, %2, %3 offen lds" :: "s"(smb + L_NU0), "v"(lane < 56 ? lane * 128 : 0), "s"(rwd), "s"(b * 7168));
      if ((TCH & 2) && v == 3 && !aux) {
        const rsrc_t rw = __builtin_amdgcn_make_buffer_rsrc((void*)(mat ? p.wu : p.wg), 0, D * F * 2, 0x00020000);
        asm volatile("s_mov_b32 m0, %0\n\ts_nop 0\n\tbuffer_load_dword %1, %2, %3 offen lds" :: "s"(smb + L_NU0), "v"((lane & 31) * (F * 2)), "s"(rw), "s"((128 * ma + 96) * (F * 2) + fj * 256 + (il & 1) * 128));
      }
      if (!aux) LOAD_EPOCH();
      MAKE_TAG();
#define WDELAY 8
      __builtin_amdgcn_s_sleep(WDELAY);
      wdq(0, -1);
#define CDELAY 4
      if (wrk) {
        __builtin_amdgcn_s_sleep(CDELAY);
#pragma unroll
        for (int i = 0; i < 4; ++i) st16_sc1(rws, (u32x4)(~0u), WS_ZT + (ch0 + k) * 4096 + lane * 16 + 1024 * i);
      }
#pragma unroll
      for (int c = 0; c < 7; ++c) {
        if (c) {
          if ((c == 2 && wrk) || (c == 3 && csm)) WAIT_V(1); else if (c == 6 && csm) WAIT_V(5); else WAIT_V(0);
          if (c == 1) PIN(dsk);
          take(c - 1, -1);
          WAIT_L(); SCHED();
        }
        if (!aux || c == 2) __builtin_amdgcn_s_barrier();
        FENCE(); SCHED();
#define KPRIO 1
#define STC 5
        __builtin_amdgcn_s_setprio(KPRIO);
        if (c == 6 && wrk) f1bad = !__all(f1.x == tlo && f1.y == thi);
        if (c) wdq(c, -1);
        if (c == 6) last7();
        if (c == 1 && wrk && lane == 0) st8_sc1(rws, tag, FL_A + (ch0 + k) * 8, 0);
        if (c == STC && csm) stats();
        if (c == 5 && wrk) f1 = ld8_sc1(rws, vf1, 0);
        if (c == 6 && wrk) {
          if (f1bad) wait_flags<8>(rws, vf1, tlo, thi);
          FENCE();
          dma4s(rws, smb + L_U + US * k + 512, lane * 16, lane * 16, lane * 16, lane * 16, WS_U + (ch0 + k) * 4096);
        }
        __builtin_amdgcn_s_setprio(0);
        SCHED();
        if (c < 2 && csm) {
#pragma unroll
          for (int i = 0; i < 4; ++i) ct[4 * c + i] = *(LAS u32x4*)(cp + 3 * UNIT * c + 8192 * i);
#pragma unroll
          for (int i = 0; i < 4; ++i) cadd(4 * c + i);
          asm volatile("" : "+v"(s[0]), "+v"(s[1]), "+v"(s[2]), "+v"(s[3]), "+v"(s[4]), "+v"(s[5]), "+v"(s[6]), "+v"(s[7]));
        }
        if (c == 3 && wrk) {
        float kv[16];
        LAS char* pa = sm + (k ? L_P + 2 * PS : 0), *pb = sm + L_P + (2 * k + 1) * PS;
#pragma unroll
        for (int g = 0; g < 3; ++g) {
          const f32x4 a = *(LAS f32x4*)(pa + 1024 * g + 16 * lane), d = *(LAS f32x4*)(pb + 1024 * g + 16 * lane);
#pragma unroll
          for (int e = 0; e < 4; ++e) asm("v_add_f32 %0, %1, %2" : "=v"(kv[4 * g + e]) : "v"(a[e]), "v"(d[e]));
          asm("v_add_f32 %0, %1, %2" : "=v"(kv[12 + g]) : "v"(*(LAS float*)(pa + 3072 + 256 * g + 4 * lane)), "v"(*(LAS float*)(pb + 3072 + 256 * g + 4 * lane)));
        }
        const bool odd = lane & 1;
        if (lane == 0) kv[0] += bflo(dsk);
        kc[0][0] = (u32x4){pk(kv[0], kv[1]), pk(kv[2], kv[3]), pk(kv[4], kv[5]), pk(kv[6], kv[7])};
        kc[1][0] = (u32x4){pk(kv[8], kv[8]), pk(kv[8], kv[9]), pk(kv[9], kv[9]), pk(kv[10], kv[10])};
        kc[2][0] = (u32x4){pk(kv[11], kv[11]), pk(kv[11], kv[11]), pk(odd ? kv[12] : kv[11], kv[12]), pk(kv[12], kv[12])};
        kc[3][0] = (u32x4){pk(kv[13], kv[13]), pk(kv[13], kv[13]), pk(odd ? kv[14] : kv[13], kv[14]), pk(kv[14], kv[14])};
          asm volatile("" : "+v"(kc[0][0]), "+v"(kc[1][0]), "+v"(kc[2][0]), "+v"(kc[3][0]));
        }
        if (c == 4 && wrk) {
#pragma unroll
        for (int g = 0; g < 4; ++g) {
          const u32x4 cu = kc[g][0];
          int px = 0, py = 0;
          if (g) {
            px = __builtin_amdgcn_update_dpp(0, (int)kc[g ? g - 1 : 0][0].z, 0x13C, 0xf, 0xf, false);
            py = __builtin_amdgcn_update_dpp(0, (int)kc[g ? g - 1 : 0][0].w, 0x13C, 0xf, 0xf, false);
          }
          px = __builtin_amdgcn_update_dpp(px, (int)cu.z, 0x138, 0xf, 0xf, false);
          py = __builtin_amdgcn_update_dpp(py, (int)cu.w, 0x138, 0xf, 0xf, false);
          const u32 s0 = __builtin_amdgcn_alignbit(py, px, 16), s1 = __builtin_amdgcn_alignbit(cu.x, py, 16), s2 = __builtin_amdgcn_alignbit(cu.y, cu.x, 16),
                    s3 = __builtin_amdgcn_alignbit(cu.z, cu.y, 16), s4 = __builtin_amdgcn_alignbit(cu.w, cu.z, 16);
          kc[g][1] = (u32x4){s1, s2, s3, s4}; kc[g][2] = (u32x4){(u32)py, vmov(cu.x), vmov(cu.y), vmov(cu.z)}; kc[g][3] = (u32x4){s0, vmov(s1), vmov(s2), vmov(s3)};
          asm volatile("" : "+v"(kc[g][0]), "+v"(kc[g][1]), "+v"(kc[g][2]), "+v"(kc[g][3]));
        }
          STAMP_AT(2);
        }
        if (c == 2 && csm) {
          const float lo = add_x8(add_r4(fold16(fold32(s[0], s[4]), fold32(s[2], s[6])))), hi = add_x8(add_r4(fold16(fold32(s[1], s[5]), fold32(s[3], s[7]))));
          s[0] = lane & 8 ? hi : lo;
          const float th = rbf(s[0]);
          if (!(lane & 4)) st8_sc1(rws, (u32x2){pk(th, s[0] - th), tg32}, 8 * (8 * p4 + 4 * hh + 2 * blk + ((lane >> 3) & 1)), WS_CS + (((fj * 2 + mat) * 4 + ma) * 128 + 64 * (il & 1) + 32 * k) * 8);
        }
        if (c == STC + 1 && wrk && !aux) {
#pragma unroll 1
        for (u32 s = 0; s < SPIN_CAP; ++s) {
          u32 d = 0;
#pragma unroll
          for (int i = 0; i < 5; ++i) {
            const u32x4 t = *(LAS u32x4*)(sm + (i < 4 || !k ? L_CS + 4096 * k + 1024 * (i & 3) : L_ST) + 16 * lane);
            d |= (t.y ^ tg32) | (t.w ^ tg32);
          }
          if (!__any(d != 0)) break;
          stats();
          WAIT_V(0);
        }
        }
      }
    }
    STAMP_AT(4);
    MAKE_TAG();
    STAMP_AT(6);
    BARRIER();
    wdq(8, 2); wdq(9, 3); wdq(10, 4);
    WAIT_V(17); take(6, -1);
    LAS char* kd = sm + L_K + 4 * KCP * k + 16 * lane;
    LAS char* kz = sm + (lane < 32 ? L_U + US * k + 16 * lane : L_K + (4 * k + ((lane >> 1) & 3)) * KCP + 16 * (lane & 1));
    asm volatile("" : "+v"(kd), "+v"(kz));
    u32x4 zero = (u32x4)(0u);
    u32 two = 2;
    LAS char* hpw = sm + S_HP + 4 * v;
    asm volatile("" : "+v"(zero), "+v"(two), "+v"(hpw));
    STAMP_AT(5);
    SCHED();
    BARRIER();
    STAMP_AT(7);
    if (v >= 2) {
      WAIT_V(0);
      *(LAS u32x4*)kz = zero;
#pragma unroll
      for (int e = 0; e < 16; ++e) *(LAS u32x4*)(kd + 32 + KCP * (e & 3) + 1024 * (e >> 2)) = kc[e >> 2][e & 3];
      post(hpw, two);
      WAIT_L();
    } else __builtin_amdgcn_s_sleep(8);
    STAMP_AT(8);
#define LATEQ 1
#define WPRIO 2
    if (!LATEQ) { wdq(11, 5); wdq(12, 6); wdq(13, 7); }
    __builtin_amdgcn_s_setprio(WPRIO);
#define HDELAY 0
#define AUXSLEEP 32
    if (!aux) { await<4>(sy + S_GP, peek(sy + S_GP), 2); __builtin_amdgcn_s_sleep(HDELAY); }
    else __builtin_amdgcn_s_sleep(AUXSLEEP);
    if (LATEQ == 1) { __builtin_amdgcn_s_setprio(0); wdq(11, 5); wdq(12, 6); wdq(13, 7); }
    STAMP_AT(9);
    const u32 vf2 = FL_2 + (ti2 * 28 + 7 * v + (lane < 7 ? lane : 0)) * FS;
#define APOLL 1
    if (APOLL) {
#pragma unroll 1
      for (u32 s = 0; s < SPIN_CAP; ++s) {
        u32x2 f = tag;
        if (lane < 7) { f = (u32x2)(0u); asm volatile("buffer_atomic_or_x2 %0, %1, %2, 0 offen sc0 sc1\n\ts_waitcnt vmcnt(0)" : "+v"(f) : "v"(vf2), "s"(rws) : "memory"); }
        if ((EXP & 4) || __all(f.x == tlo && f.y == thi)) break;
        __builtin_amdgcn_s_sleep(2);
      }
      FENCE();
    } else wait_flags<8>(rws, vf2, tlo, thi);
    __builtin_amdgcn_s_setprio(0);
    STAMP_AT(10);
    f32x4 acc[4][4];
#define PD 12
#define ZAT 9
    u32x4 hf[56];
    u32 hs;
    auto hload = [&](int n) { if (!(n & 3)) hs = (ti2 * 112 + ks_of(n >> 2)) * 2048; hf[n] = ld16_sc1(rws, lane * 16 + 1024 * (n & 3), hs); };
#pragma unroll
    for (int n = 0; n < PD; ++n) hload(n);
    bf16x4 wl[2][8];
    u32x2 fb; u32 pw_[4], wb_[4];
    SCHED();
#pragma unroll
    for (int i = 0; i < 14; ++i) {
#pragma unroll
      for (int n = 0; n < 16; ++n) {
        const int m = n >> 2, ct = m & 1 ? 3 - (n & 3) : n & 3, nx = 4 * i + m + PD;
        const bf16x8 a = i < 7 ? wr_[i < 7 ? i : 0][ct] : cat(wl[i & 1][2 * ct], wl[i & 1][2 * ct + 1]);
        const bf16x8 bb = __builtin_bit_cast(bf16x8, hf[4 * i + m]);
        if (i == 0) MFMA16_0(acc[ct][m], a, bb); else MFMA16_I(acc[ct][m], a, bb);
        SCHED();
#define LG 2
        if ((n & 3) == LG && nx < 56) hload(nx);
        if ((n & 3) != LG && n < 11 && i >= 6 && i < 13) {
          const int e = n - (n + 3 - LG) / 4;
          if (e < 8) wl[(i + 1) & 1][e] = tr4(sm + wb_[e >> 1] + (i < 9 ? i - 5 : i - 9) * (int)UNIT + 2048 * (e & 1));
        }
        if ((i == 5 || i == 8) && n >= 12) { wb_[n & 3] = (wo0 ^ ((n & 3) << 5)) + (i == 8 ? 4 * UNIT : 0); PIN(wb_[n & 3]); }
        if ((n & 3) == (LG + 3) % 4) {
          if (nx == 20) fb = ld8_sc1(rws, vf2, FL_2B);
          if (nx == 28) {
            if (__builtin_expect(!__all(fb.x == tlo && fb.y == thi), 0)) wait_flags<4>(rws, vf2 + FL_2B, tlo, thi);
            FENCE();
          }
          if (i == ZAT && m == 3) post(sm + S_HP + 4 * v, 3);
          if (i == 13) { pw_[m] = (RB + UNIT + 4096 * v + (lane & 15) * 128 + ((g4 ^ (lane & 7)) << 4)) ^ ((m & 1) << 6) ^ ((m >> 1) << 4); PIN(pw_[m]); }
        }
        SCHED();
      }
      if (i == 3) STAMP_AT(15);
      if (i == 6) STAMP_AT(16);
      if (i == 10) STAMP_AT(17);
    }
    STAMP_AT(11);
#define BAT 8
    asm volatile("s_nop 7" : "+v"(acc[0][3]), "+v"(acc[1][3]), "+v"(acc[2][3]), "+v"(acc[3][3]));
#pragma unroll
    for (int a = 0; a < 16; ++a) {
      const int ct = a & 3, m = a >> 2;
      if (a == BAT) { SCHED(); __builtin_amdgcn_s_barrier(); SCHED(); }
      *(LAS f32x4*)(sm + pw_[ct] + UNIT * (2 * (ct >> 1) + (m >> 1)) + 2048 * (m & 1)) = acc[ct][m];
    }
    STAMP_AT(12);
    BARRIER();
  } else {
    __builtin_amdgcn_s_setprio(1);
    const int c = v >> 1, ah = v & 1, ch = ch0 + c;
    u32 gi = (ch * NM + 64 * ah + lane) * 2, gd = ch * 2;
    asm volatile("" : "+v"(gi), "+v"(gd));
    SCHED();
    auto at = [](const u16* q, u32 off) { return (u32)*(const u16*)((const char*)q + off); };
    const u32 r6 = at(p.logdt, gd), r0 = at(p.are, gi), r1 = at(p.aim, gi), r2 = at(p.bre, gi), r3 = at(p.bim, gi), r4 = at(p.cre, gi), r5 = at(p.cim, gi);
    SCHED();
    STAMP_AT(2);
    DESCRIPTORS();
    const int tg = c, fq = ah;
    u32 xa[2], wa[4], iw[4], ir[2];
    {
      const u32 row = 64 * tg + (lane & 15), f = (row >> 1) & 7, scr = RB + 5 * UNIT + 4096 * v, cki = 2 * blk + (p4 >> 1);
#pragma unroll
      for (int s = 0; s < 4; ++s) {
        if (s < 2) xa[s] = RB + row * 128 + (((4 * s + g4) ^ f) << 4);
        wa[s] = RB + g4 * 4096 + q4 * 256 + (((4 * fq + s) ^ (q4 + 4 * blk)) << 5) + p4 * 8;
        iw[s] = scr + lane * 64 + ((s ^ ((lane >> 1) & 3)) << 4);
        asm volatile("" : "+v"(xa[s & 1]), "+v"(wa[s]), "+v"(iw[s]));
      }
      ir[0] = scr + (8 * hh + q4) * 64 + ((cki ^ (q4 >> 1)) << 4) + 8 * (p4 & 1);
      ir[1] = scr + (8 * hh + 4 + q4) * 64 + ((cki ^ (2 + (q4 >> 1))) << 4) + 8 * (p4 & 1);
      asm volatile("" : "+v"(ir[0]), "+v"(ir[1]));
    }
    LAS char* ka = sm + L_K + (4 * c + q4) * KCP + 2 * (28 + 128 * ah + 4 * p4 - 8 * g4);
    LAS char* ua = sm + L_U + US * c + 2 * (240 - 96 - 128 * ah + 16 * (lane & 15) + 8 * g4);
    LAS char *hpw = sm + S_HP + 8 + 4 * (lane & 1), *kp4 = sm + ((v ? L_P + PS * v : 0) + 16 * lane), *kp1 = sm + ((v ? L_P + PS * v : 0) + 3072 + 4 * lane);
    asm volatile("" : "+v"(ka), "+v"(ua), "+v"(hpw), "+v"(kp4), "+v"(kp1));
    u64 ev2;
    asm volatile("s_load_dwordx2 %0, %1, 0" : "=&s"(ev2) : "s"(p.ws + FL_E) : "memory");
    SCHED();
    STAMP_AT(15);
    STAMP_AT(16);
    bf16x8 yf[8], xf[8];
    {
      const float dt = rbf(__builtin_amdgcn_exp2f(bflo(r6) * 1.44269504088896f));
      const float ar2 = dt * bflo(r0) * 1.44269504088896f, air = dt * bflo(r1) * 0.159154943091895f;
      auto put = [&](int img, int g, u32x4 re, u32x4 im) {
        if (EXP & 8) return;
        *(LAS u32x4*)(sm + iw[g] + 2 * img * UNIT) = re; *(LAS u32x4*)(sm + iw[g] + (1 - img) * UNIT) = im;
        SCHED();
      };
      auto get = [&](int img, int s) { const u32 d = (s < 4 ? 2 * img : 1 - img) * UNIT + 1024 * (s & 3); return trfrag(sm + ir[0] + d, sm + ir[1] + d); };
#define P2_(a) pk((a).x, (a).y)
#define P4_(a, b, c, d) (u32x4){P2_(a), P2_(b), P2_(c), P2_(d)}
      const cpx e8 = cexpl(ar2, air, 8.f);
      {
        const cpx s1 = {e8.r, -e8.i}, s2 = csq(s1), s4 = csq(s2), s8 = cexpl(ar2, -air, 64.f), s16 = csq(s8);
        const cp2 y0 = {{1.f, s1.r}, {0.f, s1.i}}, y1 = cmul2(y0, s2), y2 = cmul2(y0, s4), y3 = cmul2(y1, s4);
        SCHED();
        asm volatile("s_waitcnt lgkmcnt(0)" : "+s"(ev2) :: "memory");
        if (!aux) ev = ev2;
        put(0, 0, P4_(y0.r, y1.r, y2.r, y3.r), P4_(y0.i, y1.i, y2.i, y3.i));
        const cp2 y4 = cmul2(y0, s8), y5 = cmul2(y1, s8), y6 = cmul2(y2, s8), y7 = cmul2(y3, s8);
        put(0, 1, P4_(y4.r, y5.r, y6.r, y7.r), P4_(y4.i, y5.i, y6.i, y7.i));
        const cp2 y8 = cmul2(y0, s16), y9 = cmul2(y1, s16), ya = cmul2(y2, s16), yb = cmul2(y3, s16);
        put(0, 2, P4_(y8.r, y9.r, ya.r, yb.r), P4_(y8.i, y9.i, ya.i, yb.i));
        const cp2 yc = cmul2(y4, s16), yd = cmul2(y5, s16), ye = cmul2(y6, s16), yg = cmul2(y7, s16);
        put(0, 3, P4_(yc.r, yd.r, ye.r, yg.r), P4_(yc.i, yd.i, ye.i, yg.i));
      }
      {
        const cpx cf = cmul({bflo(r4), bflo(r5)}, {dt * bflo(r2), dt * bflo(r3)});
        const cpx e1 = cexpl(ar2, air, 1.f), e256 = cexpl(ar2, air, 256.f);
        const cpx e2 = csq(e1), e4 = csq(e2), e512 = csq(e256), e1024 = csq(e512);
        const cp2 cb = cmul2({{e1.r, e256.r}, {e1.i, e256.i}}, cf);
        const cp2 a0 = {{cf.r, cb.r.x}, {cf.i, cb.i.x}}, a1 = cmul2(a0, e2);
        const cp2 t0 = cmul2({{e2.r, e4.r}, {e2.i, e4.i}}, {cb.r.y, cb.i.y});
#define XA(m) (u32x4){P2_(a0.m), P2_(a1.m), pk(cb.m.y, cb.m.y), P2_(t0.m)}
#pragma unroll
        for (int s = 0; s < 4; ++s) yf[s] = get(0, s);
        put(1, 0, XA(r), XA(i));
        const cp2 a2 = cmul2(a0, e4), a3 = cmul2(a1, e4), t1 = cmul2(t0, e4);
#define XB(m) (u32x4){P2_(a2.m), P2_(a3.m), pk(t0.m.y, t0.m.y), P2_(t1.m)}
        put(1, 1, XB(r), XB(i));
#pragma unroll
        for (int s = 4; s < 8; ++s) yf[s] = get(0, s);
        const cp2 q0 = {{cf.r, cb.r.y}, {cf.i, cb.i.y}}, q1 = cmul2(q0, e512), q2 = cmul2(q0, e1024);
        const cp2 u4 = cmul2(q1, e4), u8 = cmul2(q1, e8);
#define XC(m) (u32x4){pk(q1.m.x, u4.m.x), pk(u8.m.x, q2.m.x), pk(q1.m.y, u4.m.y), pk(u8.m.y, q2.m.y)}
        put(1, 2, XC(r), XC(i));
        const cp2 q3 = cmul2(q1, e1024), v8 = cmul2(q2, e8), w8 = cmul2(q3, e8);
#define XD(m) (u32x4){pk(v8.m.x, q3.m.x), pk(w8.m.x, 0.f), pk(v8.m.y, q3.m.y), pk(w8.m.y, 0.f)}
        put(1, 3, XD(r), XD(i));
#pragma unroll
        for (int s = 0; s < 8; ++s) xf[s] = get(1, s);
      }

      STAMP_AT(17);
    }
    f32x16 kacc = (f32x16)(0.f);
#pragma unroll
    for (int s = 0; s < 8; ++s) kacc = MFMA32(xf[s], yf[s], kacc);
    auto kstore = [&](int g) {
      if (g < 3) *(LAS f32x4*)(kp4 + 1024 * g) = (f32x4){kacc[4 * g], kacc[4 * g + 1], kacc[4 * g + 2], kacc[4 * g + 3]};
      else { *(LAS float*)kp1 = kacc[12]; *(LAS float*)(kp1 + 256) = kacc[13]; *(LAS float*)(kp1 + 512) = kacc[14]; }
    };
    kstore(0); kstore(1); kstore(2); kstore(3);
    STAMP_AT(1);
    bf16x8 X0[4]; bf16x4 W0[16];
    BARRIER();
    MAKE_TAG();
    W0[0] = tr4(sm + UNIT + wa[0]); W0[1] = tr4(sm + UNIT + wa[0] + 1024);
#pragma unroll
    for (int t = 0; t < 4; ++t) X0[t] = ldfrag(sm + xa[0] + 2048 * t);
#pragma unroll
    for (int t = 2; t < 16; ++t) W0[t] = tr4(sm + UNIT + wa[t >> 2] + 1024 * (t & 3));
    SCHED();
    bf16x4 A[2][16]; bf16x8 B[2][8];
    auto cld1 = [&](int a, int n) {
      const int i = n / 3, r = n % 3;
      if (r < 2) A[a & 1][2 * i + r] = __builtin_amdgcn_ds_read_tr16_b64_v4bf16((LAS bf16x4*)(ka + 64 * a + 512 * i + 8 - 8 * r));
      else B[a & 1][i] = ldfrag(ua + 64 * (3 - a) + 512 * i);
    };
    auto cld = [&](int a, int i) { cld1(a, 3 * i); cld1(a, 3 * i + 1); cld1(a, 3 * i + 2); };
    if (aux) { BARRIER(); BARRIER();
      await(hpw, 0, 2);
#pragma unroll
      for (int i = 0; i < 8; ++i) cld(0, i);
    }
    else {
      f32x4 acc[4][8];
      bf16x8 X1[4]; bf16x4 W1[16];
      auto step = [&](const bf16x8 (&Xc)[4], const bf16x4 (&Wc)[16], bf16x8 (&Xn)[4], bf16x4 (&Wn)[16], u32 xb, u32 wb, int s, bool first, auto mid) {
        u32 xp, wp;
#pragma unroll
        for (int n = 0; n < 32; ++n) {
          const int t = n >> 2, r = n & 3, m = t & 1 ? 3 - r : r;
          const bf16x8 wf = cat(Wc[2 * t], Wc[2 * t + 1]);
          if (first) MFMA16_0(acc[m][t], wf, Xc[m]); else MFMA16_I(acc[m][t], wf, Xc[m]);
          SCHED();
          mid(n);
          if (n == 1) { xp = xb + xa[s]; PIN(xp); }
          if (n == 2 || n == 8 || n == 15 || n == 22) { wp = wb + wa[n / 7]; PIN(wp); }
          if (n >= 4 && n < 8) Wn[n - 4] = tr4(sm + wp + 1024 * (n - 4));
          if (n >= 9 && n < 13) Wn[n - 5] = tr4(sm + wp + 1024 * (n - 9));
          if (n == 13 || n == 14) Xn[n - 13] = ldfrag(sm + xp + 2048 * (n - 13));
          if (n >= 16 && n < 20) Wn[n - 8] = tr4(sm + wp + 1024 * (n - 16));
          if (n == 20 || n == 21) Xn[n - 18] = ldfrag(sm + xp + 2048 * (n - 18));
          if (n >= 23 && n < 27) Wn[n - 11] = tr4(sm + wp + 1024 * (n - 23));
          SCHED();
        }
      };
      u32 xb = 0;
      step(X0, W0, X1, W1, xb, 2 * UNIT, 1, true, [&](int) {});
#pragma unroll 1
      for (int cc = 0; cc < 6; ++cc) {
        xb = (xb + 3 * UNIT) & (RING - 1);
        step(X1, W1, X0, W0, xb, (xb + UNIT) & (RING - 1), 0, false, [&](int n) { if (n == 3) BARRIER(); });
        step(X0, W0, X1, W1, xb, (xb + 2 * UNIT) & (RING - 1), 1, false, [&](int) {});
      }
      step(X1, W1, X0, W0, 5 * UNIT, 6 * UNIT, 0, false, [&](int n) { if (n == 3) BARRIER(); });
      u32 st[4]; u32x2 cs[4]; u32x4 XM[4];
      float ka[4], kb[4], kr[4];
      step(X0, W0, X1, W1, 5 * UNIT, 7 * UNIT, 1, false, [&](int n) {
        if (n == 0) { LAS char* a = sm + L_ST + 8 * (64 * tg + (lane & 15)); st[0] = *(LAS u32*)a; st[1] = *(LAS u32*)(a + 128); st[2] = *(LAS u32*)(a + 256); st[3] = *(LAS u32*)(a + 384); }
        if (n >= 27 && n < 31) { LAS char* a = sm + L_CS + 1024 * g4 + 8 * (64 * fq + (lane & 15)) + 128 * (n - 27); cs[n - 27] = (u32x2){*(LAS u32*)a, *(LAS u32*)(a + 4096)}; }
        if (n >= 8) {
          const int t = (n - 8) / 6, r = (n - 8) % 6;
          if (r == 0) { kr[t] = bfhi(st[t]); ka[t] = __builtin_amdgcn_rcpf(kr[t]); PIN(kr[t]); PIN(ka[t]); }
          if (r == 1) { const u32 mm = ((st[t] & 0xffffu) ^ 0x8000u) * 0x10001u; XM[t].x = mm; PIN(XM[t]); }
          if (r == 2) { kb[t] = -2.f * 0.7978845608028654f * 1.44269504088896f * ka[t]; ka[t] *= ka[t]; PIN(kb[t]); PIN(ka[t]); }
          if (r == 3) { ka[t] *= 0.044715f; kr[t] *= kr[t]; PIN(ka[t]); PIN(kr[t]); }
          if (r == 4) { ka[t] *= kb[t]; XM[t].w = XM[t].x; PIN(ka[t]); PIN(XM[t]); }
          if (r == 5) { XM[t].y = 0; XM[t].z = 0; PIN(XM[t]); }
        }
      });
      STAMP_NW(19);
      auto last = [&](int a, int e) { const int t = (a & 4) + e; const bf16x8 wf = cat(W1[2 * t], W1[2 * t + 1]); MFMA16_I(acc[a & 3][t], wf, X1[a & 3]); SCHED(); };
      auto corr = [&](int a, int e) {
        const int t = (a & 4) + e;
        const u32x4 xm = XM[a & 3];
        const u32x2 xh = e & 1 ? (u32x2){xm.z, xm.w} : (u32x2){xm.x, xm.y};
        asm volatile("v_mfma_f32_16x16x16_bf16 %0, %1, %2, %0" : "+v"(acc[a & 3][t]) : "v"(cs[t >> 1]), "v"(xh));
        SCHED();
      };
#pragma unroll
      for (int e = 0; e < 4; ++e) last(0, e);
#pragma unroll
      for (int e = 0; e < 4; ++e) corr(0, e);
      asm volatile("s_nop 7" : "+v"(acc[0][0]), "+v"(acc[0][1]), "+v"(acc[0][2]), "+v"(acc[0][3]));
      u32 hseen;
      const u32 vh = ((2 * mi + tg) * 112 + 8 * fj + 4 * fq) * 2048 + lane * 16;
      SCHED();
#pragma unroll
      for (int a = 0; a < 8; ++a) {
        const int m = a & 3;
        f32x2 z[4], gu[4];
#pragma unroll
        for (int e = 0; e < 4; ++e) {
          const f32x4 gq = acc[m][(a & 4) + (e & 2)], uq = acc[m][(a & 4) + (e & 2) + 1];
          const f32x2 g = {gq[2 * (e & 1)], gq[2 * (e & 1) + 1]}, u = {uq[2 * (e & 1)], uq[2 * (e & 1) + 1]};
          z[e] = (g * g * ka[m] + kb[m]) * g; gu[e] = g * u;
        }
        SCHED();
        if (a == 7) { cld(0, 0); cld(0, 1); cld(0, 2); cld(0, 3); SCHED(); }
#pragma unroll
        for (int e = 0; e < 8; ++e) {
          if (a < 7 && e < 4) last(a + 1, e);
          z[e >> 1][e & 1] = __builtin_amdgcn_exp2f(z[e >> 1][e & 1]);
          SCHED();
        }
#pragma unroll
        for (int e = 0; e < 4; ++e) z[e] = z[e] * kr[m] + kr[m];
        SCHED();
        if (a == 7) { cld(0, 4); cld(0, 5); cld(0, 6); cld(0, 7); SCHED(); }
#pragma unroll
        for (int e = 0; e < 8; ++e) {
          if (a < 7 && e < 4) corr(a + 1, e);
          z[e >> 1][e & 1] = __builtin_amdgcn_rcpf(z[e >> 1][e & 1]);
          SCHED();
        }
        u32x4 hv;
#pragma unroll
        for (int e = 0; e < 4; ++e) { const f32x2 hq = gu[e] * z[e]; hv[e] = pk(hq.x, hq.y); }
        if (EXP & 32) asm volatile("" :: "v"(hv)); else st16_sc1(rws, hv, vh + 1024 * a);
#define FLAGA 6
#define B8AT 1
        if (a == B8AT) BARRIER();
        if (a == 1) STAMP_NW(20);
        if (a == 3) STAMP_NW(21);
        if (a == FLAGA) {
          STAMP_NW(22);
          asm volatile("s_waitcnt vmcnt(%0)" :: "i"(FLAGA - 3) : "memory");
          if (lane < 16) st8_sc1(rws, tag, lane * 8, FL_2 + ((2 * mi + tg) * 28 + 2 * fj + fq) * FS);
          STAMP_NW(18);
        }
#define GATEA 6
        if (a == GATEA) post(sm + S_GP + 4 * v, 2);
        if (a == 5) hseen = peek(hpw);
        if (a == 6) await(hpw, hseen, 2);
        SCHED();
      }
      STAMP_NW(23); STAMP_NWEND();
      STAMP_AT(3);
    }
    f32x4 y[8];
    const u32 vo = (64 * ti2 + 8 * v + (lane >> 3)) * (D * 2) + n2 * 128 + (lane & 7) * 16;
    u32x4 xres[2];
    LAS char* const pw = sm + S_GP + 4 * (v ^ 1);
    u32 pseen, vzs, zsrc; LAS char *exw, *exr; u32x2 zo[4];
    STAMP_AT(4);
#pragma unroll
    for (int a = 0; a < 4; ++a) {
      auto cm = [&](int n, int i, int j) {
        const bf16x8 af = __builtin_shufflevector(A[a & 1][2 * i], A[a & 1][2 * i + 1], 0, 1, 2, 3, 4, 5, 6, 7);
        if (a == 0 && j - i <= 1 && j >= i) asm volatile("v_mfma_f32_16x16x32_bf16 %0, %1, %2, 0" : "=&v"(y[i + j]) : "v"(af), "v"(B[a & 1][j]));
        else if ((EXP & 16) && a) asm volatile("" : "+v"(y[i + j]) : "v"(af), "v"(B[a & 1][j]));
        else asm volatile("v_mfma_f32_16x16x32_bf16 %0, %1, %2, %0" : "+v"(y[i + j]) : "v"(af), "v"(B[a & 1][j]));
        SCHED();
        if (a < 3 && n >= 4 && n < 28) cld1(a + 1, n - 4);
        if (a == 3) {
          if (n == 4) { exw = sm + (L_K + (4 * c + ah) * KCP + 16 * lane); PIN(exw); }
          if (n == 8) { exr = sm + (L_K + (4 * c + (ah ^ 1)) * KCP + 16 * lane); PIN(exr); }
          if (n == 12) { zsrc = 64 * p4 + (lane & 60); PIN(zsrc); }
          if (n == 16) { vzs = WS_ZT + ch * 4096 + 2048 * ah + 8 * lane; PIN(vzs); }
          if (n == 20) pseen = peek(pw);
        }
        SCHED();
      };
      cm(0, 0, 0); cm(1, 0, 1); cm(2, 1, 1); cm(3, 1, 0); cm(4, 0, 2); cm(5, 1, 2); cm(6, 2, 2); cm(7, 2, 0); cm(8, 2, 1);
      cm(9, 0, 3); cm(10, 1, 3); cm(11, 2, 3); cm(12, 3, 3); cm(13, 3, 0); cm(14, 3, 1); cm(15, 3, 2); cm(16, 0, 4); cm(17, 1, 4);
      cm(18, 2, 4); cm(19, 3, 4); cm(20, 4, 0); cm(21, 4, 1); cm(22, 4, 2); cm(23, 4, 3); cm(24, 0, 5); cm(25, 1, 5); cm(26, 2, 5);
      cm(27, 5, 0); cm(28, 5, 1); cm(29, 5, 2); cm(30, 0, 6); cm(31, 1, 6); cm(32, 6, 0); cm(33, 6, 1); cm(34, 0, 7); cm(35, 7, 0);
      if (a == 2) post(sm + S_GP + 4 * v, 3);
#define FLAG_AT 1
      if (a == FLAG_AT) {
        STAMP_AT(5);
        WAIT_V(0);
        if (!aux && lane < 16) st8_sc1(rws, tag, lane * 8, FL_2 + FL_2B + ((2 * mi + c) * 28 + 2 * fj + ah) * FS);
        xres[0] = __builtin_amdgcn_raw_buffer_load_b128(rx, vo, 0, 0);
        xres[1] = __builtin_amdgcn_raw_buffer_load_b128(rx, vo, 32 * D * 2, 0);
        STAMP_AT(6);
      }
    }
    STAMP_AT(7);
    asm volatile("s_nop 7" : "+v"(y[0]), "+v"(y[1]), "+v"(y[2]), "+v"(y[3]), "+v"(y[4]), "+v"(y[5]), "+v"(y[6]), "+v"(y[7]));
    await<0>(pw, pseen, 3);
    auto finish = [&](int send, int keep) {
#pragma unroll
      for (int i = 0; i < 4; ++i) *(LAS f32x4*)(exw + 1024 * i) = y[send + i];
      post(sm + S_GP + 4 * v, 4);
      await<0>(pw, peek(pw), 4);
      f32x4 t[4];
#pragma unroll
      for (int i = 0; i < 4; ++i) t[i] = *(LAS f32x4*)(exr + 1024 * i);
      SCHED();
#pragma unroll
      for (int i = 0; i < 4; ++i) {
        zo[i].x = __builtin_amdgcn_ds_bpermute(zsrc, min(pk(vadd(t[i][0], y[keep + i][0]), vadd(t[i][1], y[keep + i][1])), ~1u));
        zo[i].y = __builtin_amdgcn_ds_bpermute(zsrc, min(pk(vadd(t[i][2], y[keep + i][2]), vadd(t[i][3], y[keep + i][3])), ~1u));
      }
    };
    if (ah) finish(0, 4); else finish(4, 0);
    STAMP_AT(8);
#pragma unroll
    for (int i = 0; i < 4; ++i) {
      if (EXP & 1) asm volatile("" :: "v"(zo[i])); else st8_sc1(rws, zo[i], vzs + 512 * i, 0);
      SCHED();
    }
    STAMP_AT(9);
#define ZSLEEP 8
    u32 vz = WS_ZT + (64 * n2 + 16 * v + (lane >> 3)) * 4096 + 128 * ti2 + (lane & 7) * 16;
    const int tok = 8 * v + (lane >> 3), dc = lane & 7;
    const u32 stg = L_K + (4 * c + 3) * KCP + 2048 * ah;
    LAS char *sw = sm + (stg + 16 * lane), *tr = sm + (stg + q4 * 128 + 32 * g4 + 8 * p4), *rb = sm + (stg - KCP + 32 * lane);
    LAS char* rz = sm + (L_K + (4 * (dc >> 2) + 2) * KCP + 2048 * ((dc >> 1) & 1) + 32 * tok + 16 * (dc & 1));
    u32 t0 = RB + UNIT + UNIT * 2 * (dc >> 2) + tok * 128 + (((2 * (dc & 3)) ^ (tok & 7) ^ (dc >> 2)) << 4), t1 = t0 ^ 16u;
    u32 op0 = vo, op1 = vo + 32 * D * 2;
    const rsrc_t ro = __builtin_amdgcn_make_buffer_rsrc((void*)p.out, 0, L * D * 2, 0x00020000);
    f32x4 s0[2], s1[2];
#pragma unroll
    for (int i = 0; i < 2; ++i) {
      s0[i] = (f32x4){bflo(xres[i][0]), bfhi(xres[i][0]), bflo(xres[i][1]), bfhi(xres[i][1])};
      s1[i] = (f32x4){bflo(xres[i][2]), bfhi(xres[i][2]), bflo(xres[i][3]), bfhi(xres[i][3])};
    }
    asm volatile("" : "+v"(vz), "+v"(sw), "+v"(tr), "+v"(rb), "+v"(rz), "+v"(t0), "+v"(t1), "+v"(op0), "+v"(op1), "+v"(s0[0]), "+v"(s0[1]), "+v"(s1[0]), "+v"(s1[1]));
    if (b == 255 && v == 0) {
#pragma unroll 1
      for (int i = 0; i < 4; ++i) wait_flags<8>(rws, FL_A + (64 * i + lane) * 16, tlo, thi);
      if (lane == 0) st8_sc1(rws, tag, FL_E, 0);
    }
#define ZDELAY 12
    __builtin_amdgcn_s_sleep(ZDELAY);
    await(sy + S_HP, peek(sy + S_HP), 3);
    STAMP_AT(10);
#pragma unroll 1
    for (u32 s = 0; s < SPIN_CAP; ++s) {
      FENCE();
      const u32x4 z0 = ld16_sc1(rws, vz, 0), z1 = ld16_sc1(rws, vz, 8 * 4096);
      *(LAS u32x4*)sw = z0;
      *(LAS u32x4*)(sw + 1024) = z1;
      if (!__any(max(max(max(z0.x, z0.y), max(z0.z, z0.w)), max(max(z1.x, z1.y), max(z1.z, z1.w))) == ~0u)) break;
      __builtin_amdgcn_s_sleep(ZSLEEP);
    }
    STAMP_AT(11);
    const bf16x8 ra = trfrag(tr, tr + 512), rc = trfrag(tr + 1024, tr + 1536);
    *(LAS bf16x8*)rb = ra;
    *(LAS bf16x8*)(rb + 16) = rc;
    STAMP_AT(12);
    BARRIER();
#pragma unroll
    for (int i = 0; i < 2; ++i) {
      const u32x4 z = *(LAS u32x4*)(rz + 1024 * i);
      s0[i] += (f32x4){bflo(z[0]), bfhi(z[0]), bflo(z[1]), bfhi(z[1])};
      s1[i] += (f32x4){bflo(z[2]), bfhi(z[2]), bflo(z[3]), bfhi(z[3])};
    }
    asm volatile("" : "+v"(s0[0]), "+v"(s0[1]), "+v"(s1[0]), "+v"(s1[1]));
    SCHED(); BARRIER(); SCHED();
    STAMP_AT(13);
    f32x4 pa[2][4], pb[2][4];
#pragma unroll
    for (int g = 0; g < 8; ++g) { pa[g >> 2][g & 3] = *(LAS f32x4*)(sm + t0 + 4096 * g); pb[g >> 2][g & 3] = *(LAS f32x4*)(sm + t1 + 4096 * g); }
    SCHED();
#pragma unroll
    for (int i = 0; i < 2; ++i) {
#pragma unroll
      for (int g = 0; g < 4; ++g) { s0[i] += pa[i][g]; s1[i] += pb[i][g]; }
      st16_sc1(ro, (u32x4){pk(s0[i][0], s0[i][1]), pk(s0[i][2], s0[i][3]), pk(s1[i][0], s1[i][1]), pk(s1[i][2], s1[i][3])}, i ? op1 : op0);
      SCHED();
    }
  }
  STAMP_AT(14); STAMP_OUT();
}

#define WSOFF 0
extern "C" void kernel_launch(void* const* d_in, const int* in_sizes, int n_in, void* d_out, int out_size, void* d_ws,
                              size_t ws_size, hipStream_t stream) {
  Params p;
  p.x = (const u16*)d_in[0]; p.are = (const u16*)d_in[1]; p.aim = (const u16*)d_in[2];
  p.bre = (const u16*)d_in[3]; p.bim = (const u16*)d_in[4]; p.cre = (const u16*)d_in[5];
  p.cim = (const u16*)d_in[6]; p.logdt = (const u16*)d_in[7]; p.dskip = (const u16*)d_in[8];
  p.wg = (const u16*)d_in[10]; p.wu = (const u16*)d_in[11]; p.wd = (const u16*)d_in[12];
  p.out = (u16*)d_out; p.ws = (char*)d_ws + ((size_t)WSOFF << 20);
  s4ffn_kernel<<<256, 512, 0, stream>>>(p);
}
```
